```python
import torch

def _softmax(x, axis=-1):
    return torch.softmax(x, dim=axis)

def _gelu(x):
    return torch.nn.functional.gelu(x, approximate='tanh')

def _silu(x):
    return torch.nn.functional.silu(x)

def _sigmoid(x):
    return torch.sigmoid(x)

def _layernorm(x, axis=-1, eps=1e-05):
    m = x.mean(dim=axis, keepdim=True)
    v = x.var(dim=axis, keepdim=True, unbiased=False)
    return (x - m) / torch.sqrt(v + eps)

def _scatter_add(tgt, idx, src, axis=0):
    return tgt.clone().index_add_(axis, idx, src)

def _segment_sum(src, seg, n):
    out = torch.zeros((n,) + src.shape[1:], dtype=src.dtype)
    return out.index_add_(0, seg, src)

def _pad(x, pw):
    flat = tuple((v for pair in reversed(pw) for v in pair))
    return torch.nn.functional.pad(x, flat)
kernel_name = 'adaLN_cond__sliding_block__geglu_ffn__prompt'

def setup_inputs():

    _w = lambda a: (lambda t: t.to(torch.bfloat16) if t.is_floating_point() else t)(torch.as_tensor(a))

    def _A_setup():
        B, S, D = (1, 2048, 512)
        return {'x': _w((lambda *s: torch.randn(*s, dtype=torch.bfloat16) * 0.05)(B, S, D)), 'wqkv': _w((lambda *s: torch.randn(*s, dtype=torch.bfloat16) * 0.05)(D, 3 * D)), 'wo': _w((lambda *s: torch.randn(*s, dtype=torch.bfloat16) * 0.05)(D, D))}
    _A_in = _A_setup()

    def _B_setup():
        B, S, D, F = (1, 2048, 512, 1792)
        return {'x': _w((lambda *s: torch.randn(*s, dtype=torch.bfloat16) * 0.05)(B, S, D)), 'w_gate': _w((lambda *s: torch.randn(*s, dtype=torch.bfloat16) * 0.05)(D, F)), 'w_up': _w((lambda *s: torch.randn(*s, dtype=torch.bfloat16) * 0.05)(D, F)), 'w_down': _w((lambda *s: torch.randn(*s, dtype=torch.bfloat16) * 0.05)(F, D))}
    _B_in = _B_setup()
    return {'A_x': _A_in['x'], 'A_wqkv': _A_in['wqkv'], 'A_wo': _A_in['wo'], 'B_x': _B_in['x'], 'B_w_gate': _B_in['w_gate'], 'B_w_up': _B_in['w_up'], 'B_w_down': _B_in['w_down'], 'c': _w((lambda *s: torch.randn(*s, dtype=torch.bfloat16) * 0.1)(1, 512) * 8.0), 'w_mod': _w((lambda *s: torch.randn(*s, dtype=torch.bfloat16) * 0.1)(512, 6 * 512) * 512 ** (-0.5) * 8.0)}

def reference(A_x, A_wqkv, A_wo, B_x, B_w_gate, B_w_up, B_w_down, c, w_mod):

    def _A_ref(x, wqkv, wo):
        B, S, D = x.shape
        H = 32
        Dh = D // H
        W = 256
        qkv = (lambda x, *d: x.permute(*d))((x @ wqkv).reshape(B, S, 3, H, Dh), 2, 0, 3, 1, 4)
        q, k, v = (qkv[0], qkv[1], qkv[2])
        scores = torch.einsum('bhqd,bhkd->bhqk', q, k) * Dh ** (-0.5)
        idx = torch.arange(S)
        mask = (idx[:, None] - idx[None, :] >= 0) & (idx[:, None] - idx[None, :] < W)
        scores = torch.where(mask[None, None], scores, -1000000000.0)
        p = _softmax(scores, axis=-1)
        o = torch.einsum('bhqk,bhkd->bhqd', p, v)
        return (lambda x, *d: x.permute(*d))(o, 0, 2, 1, 3).reshape(B, S, D) @ wo

    def _B_ref(x, w_gate, w_up, w_down):
        return _gelu(x @ w_gate) * (x @ w_up) @ w_down
    x = A_x
    mod = (_silu(c) @ w_mod).reshape(c.shape[0], 6, 1, -1)
    sa, ca, ga, sb, cb, gb = (mod[:, 0].clone(), mod[:, 1].clone(), mod[:, 2].clone(), mod[:, 3].clone(), mod[:, 4].clone(), mod[:, 5].clone())
    h = _layernorm(x, axis=-1) * (1 + ca) + sa
    x = x + ga * _A_ref(h, A_wqkv, A_wo)
    h = _layernorm(x, axis=-1) * (1 + cb) + sb
    x = x + gb * _B_ref(h, B_w_gate, B_w_up, B_w_down)
    return x
```

```cpp
#include <hip/hip_runtime.h>
#include <hip/hip_bf16.h>
#include <stdint.h>
typedef unsigned short u16;
__device__ __forceinline__ float bf2f(u16 v){ return __uint_as_float(((unsigned)v)<<16); }
__device__ __forceinline__ u16 f2bf(float f){ unsigned u=__float_as_uint(f); u += 0x7fffu + ((u>>16)&1u); return (u16)(u>>16); }
constexpr int S=2048,D=512,F=1792,H=32,DH=16,W=256;

__global__ void n_mod(const u16* c,const u16* wm,float* mod){
  int n=blockIdx.x*blockDim.x+threadIdx.x; if(n>=6*D) return;
  float a=0; for(int k=0;k<D;++k){ float v=bf2f(c[k]); float s=v/(1.f+__expf(-v)); s=bf2f(f2bf(s)); a+=s*bf2f(wm[k*6*D+n]); }
  mod[n]=bf2f(f2bf(a));
}
__global__ void n_ln(const u16* x,const float* sh,const float* sc,u16* h){
  int row=blockIdx.x; int l=threadIdx.x; float v[8]; float s=0;
  for(int j=0;j<8;++j){ v[j]=bf2f(x[row*D+l*8+j]); s+=v[j]; }
  for(int o=32;o;o>>=1) s+=__shfl_xor(s,o);
  float m=s/D; float q=0; for(int j=0;j<8;++j){ float d=v[j]-m; q+=d*d; }
  for(int o=32;o;o>>=1) q+=__shfl_xor(q,o);
  float r=rsqrtf(q/D+1e-5f);
  for(int j=0;j<8;++j){ int k=l*8+j; h[row*D+k]=f2bf((v[j]-m)*r*(1.f+sc[k])+sh[k]); }
}
__global__ void n_gemm(const u16* A,const u16* B,float* C,int M,int N,int K){
  int n=blockIdx.x*blockDim.x+threadIdx.x; int m=blockIdx.y; if(n>=N) return;
  float a=0; for(int k=0;k<K;++k) a+=bf2f(A[m*K+k])*bf2f(B[k*N+n]);
  C[(long)m*N+n]=a;
}
__global__ void n_attn(const float* qkv,u16* o){
  int t=blockIdx.x*blockDim.x+threadIdx.x; int h=blockIdx.y;
  float q[16]; for(int d=0;d<16;++d) q[d]=bf2f(f2bf(qkv[t*1536+h*16+d]));
  int j0=t-255<0?0:t-255; float m=-1e30f;
  for(int j=j0;j<=t;++j){ float s=0; for(int d=0;d<16;++d) s+=q[d]*bf2f(f2bf(qkv[j*1536+512+h*16+d])); s*=0.25f; m=fmaxf(m,s);}
  float l=0,acc[16]={0};
  for(int j=j0;j<=t;++j){ float s=0; for(int d=0;d<16;++d) s+=q[d]*bf2f(f2bf(qkv[j*1536+512+h*16+d])); s*=0.25f; float p=__expf(s-m); l+=p; for(int d=0;d<16;++d) acc[d]+=p*bf2f(f2bf(qkv[j*1536+1024+h*16+d])); }
  for(int d=0;d<16;++d) o[t*512+h*16+d]=f2bf(acc[d]/l);
}
__global__ void n_res(const u16* x,const float* y,const float* g,u16* out){
  int i=blockIdx.x*blockDim.x+threadIdx.x; int k=i%D; out[i]=f2bf(bf2f(x[i])+g[k]*y[i]);
}
__global__ void n_act(const float* g,const float* u,u16* a){
  int i=blockIdx.x*blockDim.x+threadIdx.x; float x=g[i]; float z=0.7978845608f*(x+0.044715f*x*x*x); float ge=0.5f*x*(1.f+tanhf(z)); a[i]=f2bf(ge*u[i]);
}
extern "C" void kernel_launch(void* const* d_in, const int* in_sizes, int n_in, void* d_out, int out_size, void* d_ws, size_t ws_size, hipStream_t stream){
  const u16* x=(const u16*)d_in[0]; const u16* wqkv=(const u16*)d_in[1]; const u16* wo=(const u16*)d_in[2];
  const u16* wg=(const u16*)d_in[4]; const u16* wu=(const u16*)d_in[5]; const u16* wd=(const u16*)d_in[6];
  const u16* c=(const u16*)d_in[7]; const u16* wm=(const u16*)d_in[8];
  char* ws=(char*)d_ws; float* mod=(float*)ws; u16* h=(u16*)(ws+(1<<20)); float* t0=(float*)(ws+(4<<20)); float* t1=(float*)(ws+(32<<20)); u16* o=(u16*)(ws+(64<<20)); u16* x1=(u16*)(ws+(68<<20)); u16* act=(u16*)(ws+(72<<20));
  n_mod<<<12,256,0,stream>>>(c,wm,mod);
  n_ln<<<S,64,0,stream>>>(x,mod,mod+D,h);
  n_gemm<<<dim3(6,S),256,0,stream>>>(h,wqkv,t0,S,1536,D);
  n_attn<<<dim3(S/64,H),64,0,stream>>>(t0,o);
  n_gemm<<<dim3(2,S),256,0,stream>>>(o,wo,t1,S,D,D);
  n_res<<<S*D/256,256,0,stream>>>(x,t1,mod+2*D,x1);
  n_ln<<<S,64,0,stream>>>(x1,mod+3*D,mod+4*D,h);
  n_gemm<<<dim3(7,S),256,0,stream>>>(h,wg,t0,S,F,D);
  n_gemm<<<dim3(7,S),256,0,stream>>>(h,wu,t1,S,F,D);
  n_act<<<S*F/256,256,0,stream>>>(t0,t1,act);
  n_gemm<<<dim3(2,S),256,0,stream>>>(act,wd,t0,S,D,F);
  n_res<<<S*D/256,256,0,stream>>>(x1,t0,mod+5*D,(u16*)d_out);
}
```

```cpp
#include <hip/hip_runtime.h>
#include <stdint.h>

typedef unsigned u32;
typedef unsigned long long u64;
typedef __attribute__((ext_vector_type(2))) u32 u32x2;
typedef __attribute__((ext_vector_type(4))) u32 u32x4;
typedef __attribute__((ext_vector_type(2))) float f32x2;
typedef __attribute__((ext_vector_type(4))) float f32x4;
typedef __attribute__((ext_vector_type(16))) float f32x16;
typedef __attribute__((ext_vector_type(8))) short bf16x8;
typedef __attribute__((ext_vector_type(2))) __bf16 bf16x2;
typedef __amdgpu_buffer_rsrc_t rsrc_t;
#define LAS __attribute__((address_space(3)))
#define GAS __attribute__((address_space(1)))
#define DI __device__ __forceinline__
#define RLX_AGENT __ATOMIC_RELAXED, __HIP_MEMORY_SCOPE_AGENT

constexpr u32 WS_SEED = 0;
constexpr u32 WS_ARR = 4096;
constexpr u32 WS_MODG = 65536;
constexpr u32 WS_MB = 262144;
constexpr u32 MB_QKV = 0, MB_O = 32, MB_X1 = 64, MB_E = 80, MB_W = 96, MB_ACT = 128;
constexpr u32 WS_ST = 786432;
constexpr u32 WS_Q = 1u << 20, WS_K = 3u << 20, WS_V = 5u << 20;
constexpr u32 WS_O = 7u << 20;
constexpr u32 WS_X1 = 9u << 20;
constexpr u32 WS_ACT = 11u << 20;
constexpr u32 WS_WP = 18u << 20;
constexpr u32 WS_BYTES = 22u << 20;
constexpr u32 WS_DBG = 24u << 20;
constexpr u32 SPIN_CAP = 1u << 15;

DI float bflo(u32 w) { return __uint_as_float(w << 16); }
DI float bfhi(u32 w) { return __uint_as_float(w & 0xffff0000u); }
DI u32 pk2(float a, float b) { f32x2 v = {a, b}; return __builtin_bit_cast(u32, __builtin_convertvector(v, bf16x2)); }
DI float rbf(float a) { return bflo(pk2(a, a)); }
template <int CTRL> DI float dppf(float x) { return __builtin_bit_cast(float, __builtin_amdgcn_update_dpp(0, __builtin_bit_cast(int, x), CTRL, 0xf, 0xf, true)); }
DI void swap32f(float& a, float& b) { auto r = __builtin_amdgcn_permlane32_swap(__float_as_uint(a), __float_as_uint(b), false, false); a = __uint_as_float(r[0]); b = __uint_as_float(r[1]); }
DI void swap16f(float& a, float& b) { auto r = __builtin_amdgcn_permlane16_swap(__float_as_uint(a), __float_as_uint(b), false, false); a = __uint_as_float(r[0]); b = __uint_as_float(r[1]); }
DI void swap32u(u32& a, u32& b) { auto r = __builtin_amdgcn_permlane32_swap(a, b, false, false); a = r[0]; b = r[1]; }
DI void swap16u(u32& a, u32& b) { auto r = __builtin_amdgcn_permlane16_swap(a, b, false, false); a = r[0]; b = r[1]; }
DI float row16_sum(float x) { x += dppf<0xB1>(x); x += dppf<0x4E>(x); x += dppf<0x141>(x); x += dppf<0x140>(x); return x; }
DI float rdlane(float x, int l) { return __uint_as_float(__builtin_amdgcn_readlane(__float_as_uint(x), l)); }

#define MFMA16(a, b, c) __builtin_amdgcn_mfma_f32_16x16x32_bf16((a), (b), (c), 0, 0, 0)
#define MFMA32(a, b, c) __builtin_amdgcn_mfma_f32_32x32x16_bf16((a), (b), (c), 0, 0, 0)
#define BF8(x) __builtin_bit_cast(bf16x8, (x))
#define WAITV(n) asm volatile("s_waitcnt vmcnt(%0)" ::"i"(n) : "memory")
#define WAITBAR(n) asm volatile("s_waitcnt vmcnt(%0) lgkmcnt(0)\n\ts_barrier" ::"i"(n) : "memory")
#define LBAR() asm volatile("s_waitcnt lgkmcnt(0)\n\ts_barrier" ::: "memory")
#define SBAR() __builtin_amdgcn_sched_barrier(0)
#define NOP_ do {} while (0)

DI void dma(u32 lds, u32 voff, rsrc_t rs, u32 soff) {
  asm volatile("s_mov_b32 m0, %0\n\ts_nop 0\n\tbuffer_load_dwordx4 %1, %2, %3 offen lds" ::"s"(lds), "v"(voff), "s"(rs), "s"(soff) : "memory");
}
DI void dma_sc1(u32 lds, u32 voff, rsrc_t rs, u32 soff) {
  asm volatile("s_mov_b32 m0, %0\n\ts_nop 0\n\tbuffer_load_dwordx4 %1, %2, %3 offen sc1 lds" ::"s"(lds), "v"(voff), "s"(rs), "s"(soff) : "memory");
}
DI void dma4(u32 lds, u32 voff, rsrc_t rs, u32 soff) {
  asm volatile("s_mov_b32 m0, %0\n\ts_nop 0\n\tbuffer_load_dword %1, %2, %3 offen lds" ::"s"(lds), "v"(voff), "s"(rs), "s"(soff) : "memory");
}
DI void ld16(u32x4& r, u32 voff, rsrc_t rs, u32 soff) { asm volatile("buffer_load_dwordx4 %0, %1, %2, %3 offen" : "=v"(r) : "v"(voff), "s"(rs), "s"(soff) : "memory"); }
DI void ld16_sc1(u32x4& r, u32 voff, rsrc_t rs, u32 soff) { asm volatile("buffer_load_dwordx4 %0, %1, %2, %3 offen sc1" : "=v"(r) : "v"(voff), "s"(rs), "s"(soff) : "memory"); }
DI void ld8_sc1(u32x2& r, u32 voff, rsrc_t rs, u32 soff) { asm volatile("buffer_load_dwordx2 %0, %1, %2, %3 offen sc1" : "=v"(r) : "v"(voff), "s"(rs), "s"(soff) : "memory"); }
DI void ld4(u32& r, u32 voff, rsrc_t rs, u32 soff) { asm volatile("buffer_load_dword %0, %1, %2, %3 offen" : "=v"(r) : "v"(voff), "s"(rs), "s"(soff) : "memory"); }
DI void ld2(u32& r, u32 voff, rsrc_t rs, u32 soff) { asm volatile("buffer_load_ushort %0, %1, %2, %3 offen" : "=v"(r) : "v"(voff), "s"(rs), "s"(soff) : "memory"); }
#define WAITV1(n, a) asm volatile("s_waitcnt vmcnt(%1)" : "+v"(a) : "i"(n) : "memory")
#define PIN(a) asm volatile("" : "+v"(a))
DI void st16_sc1(u32x4 v, rsrc_t rs, u32 voff, u32 soff) { __builtin_amdgcn_raw_buffer_store_b128(v, rs, voff + soff, 0, 16); }

DI bf16x8 lds16(u32 a) { return *(const LAS bf16x8*)(uintptr_t)a; }
DI u32x4 lds16u(u32 a) { return *(const LAS u32x4*)(uintptr_t)a; }
DI f32x4 lds16f(u32 a) { return *(const LAS f32x4*)(uintptr_t)a; }
DI f32x2 lds8f(u32 a) { return *(const LAS f32x2*)(uintptr_t)a; }
DI void sts16(u32 a, u32x4 v) { *(LAS u32x4*)(uintptr_t)a = v; }
DI void sts16f(u32 a, f32x4 v) { *(LAS f32x4*)(uintptr_t)a = v; }
DI void sts8f(u32 a, f32x2 v) { *(LAS f32x2*)(uintptr_t)a = v; }
DI void sts4f(u32 a, float v) { *(LAS float*)(uintptr_t)a = v; }
typedef short v4i16_t __attribute__((ext_vector_type(4)));
DI bf16x8 ldtr(u32 a) {
  v4i16_t lo = __builtin_amdgcn_ds_read_tr16_b64_v4i16((LAS v4i16_t*)(uintptr_t)a);
  v4i16_t hi = __builtin_amdgcn_ds_read_tr16_b64_v4i16((LAS v4i16_t*)(uintptr_t)(a + 512));
  return __builtin_shufflevector(lo, hi, 0, 1, 2, 3, 4, 5, 6, 7);
}

DI void poll(const GAS u32* p, bool active, u32 tag) {
#pragma unroll 1
  for (u32 spins = 0; spins < SPIN_CAP; ++spins) {
    u32 v = tag;
    if (active) v = __hip_atomic_load(p, RLX_AGENT);
    if (__all(v == tag)) break;
    __builtin_amdgcn_s_sleep(1);
  }
  asm volatile("" ::: "memory");
}
DI void post(char* ws, u32 word, bool active, u32 tag) { if (active) __hip_atomic_store((GAS u32*)ws + word, tag, RLX_AGENT); }
DI u32 mbox(int xcd, int bi) { return WS_MB / 4 + (u32)(bi * 8 + xcd) * 256; }
DI bool tags_ok(u32x4 a, u32x4 b, u32 tag) { return a[1] == tag && a[3] == tag && b[1] == tag && b[3] == tag; }
DI u32x4 gvals(u32x4 a, u32x4 b) { return (u32x4){a[0], a[2], b[0], b[2]}; }

#define STAMP(i)

DI float silu_bf(u32 cv) { const float c = bflo(cv); return rbf(c * __builtin_amdgcn_rcpf(1.f + __builtin_amdgcn_exp2f(c * -1.4426950408889634f))); }
DI void mod_part(u32 scr, u32 c0, u32 c1, u32x4 w0, u32x4 w1, int w, int lane) {
  const float s0 = silu_bf(c0), s1 = silu_bf(c1);
  float v[8];
#pragma unroll
  for (int j = 0; j < 4; ++j) {
    v[2 * j] = __builtin_fmaf(s0, bflo(w0[j]), s1 * bflo(w1[j]));
    v[2 * j + 1] = __builtin_fmaf(s0, bfhi(w0[j]), s1 * bfhi(w1[j]));
  }
#pragma unroll
  for (int j = 0; j < 4; ++j) { swap32f(v[j], v[j + 4]); v[j] += v[j + 4]; }
#pragma unroll
  for (int j = 0; j < 2; ++j) { swap16f(v[j], v[j + 2]); v[j] += v[j + 2]; }
  const bool o = lane & 8;
  float a = o ? v[1] : v[0], c = o ? v[0] : v[1];
  a += dppf<0x128>(c);
  a += dppf<0x4E>(a);
  a += dppf<0x104>(a);
  if ((lane & 6) == 0) sts4f(scr + ((lane & 1) * 8 + (lane >> 3)) * 32 + w * 4, a);
}
DI float sum8(f32x4 a, f32x4 d) { return ((a[0] + a[1]) + (a[2] + a[3])) + ((d[0] + d[1]) + (d[2] + d[3])); }
DI void mod_publish(u32 scr, char* ws, u32 col, u32 tag, int lane) {
  const u32 a = scr + (lane & 7) * 64;
  const float t0 = sum8(lds16f(a), lds16f(a + 16)), t1 = sum8(lds16f(a + 32), lds16f(a + 48));
  __hip_atomic_store((GAS u64*)(ws + WS_MODG) + (lane >> 3) * 1536 + (col >> 1) + (lane & 7), ((u64)tag << 32) | pk2(t0, t1), RLX_AGENT);
}

constexpr u64 upper_mask(int e) {
  u64 m = 0;
  for (int l = 0; l < 64; ++l) if ((e & 3) + 8 * (e >> 2) + 4 * (l >> 5) > (l & 31)) m |= 1ull << l;
  return m;
}
template <int E> DI bool upper() { constexpr u64 m = upper_mask(E); return __builtin_amdgcn_inverse_ballot_w64(m); }
#define MX3(a, b, c) __builtin_fmaxf(__builtin_fmaxf((a), (b)), (c))
#define AT_MAX(s) do { _Pragma("unroll") for (int e_ = 0; e_ < 16; e_ += 4) { m0 = MX3(m0, s[e_], s[e_ + 1]); m1 = MX3(m1, s[e_ + 2], s[e_ + 3]); } PIN(m0); PIN(m1); } while (0)
#define EX2(x) __builtin_amdgcn_exp2f(x)
template <int I> DI void sel_fill(f32x16& sel, const f32x16& sf, const f32x16& sd) {
  if constexpr (I < 16) { sel[I] = upper<I>() ? sf[I] : sd[I]; sel_fill<I + 1>(sel, sf, sd); }
}
template <bool FULL>
DI void attn_wave(int w, int lane, bf16x8 qf, const u32 (&mk)[8], u32 kb, u32 vb0, u32 vb1, u32x4& outv, u32 tv, rsrc_t rws, u32 tsrc) {
  const f32x16 zero = {};
  bf16x8 kf[9];
  float m0, m1;
  f32x16 sel, sa, sb;
  if constexpr (FULL) {
    kf[8] = lds16(kb + 8192); kf[0] = lds16(kb);
#pragma unroll
    for (int i = 1; i < 8; ++i) kf[i] = lds16(kb + i * 1024);
    SBAR();
    sa = MFMA32(kf[8], qf, zero); sb = MFMA32(kf[0], qf, zero);
    f32x16 sc = MFMA32(kf[1], qf, zero); SBAR();
    sel_fill<0>(sel, sb, sa); PIN(sel); SBAR();
    sa = MFMA32(kf[2], qf, zero); SBAR();
    m0 = __builtin_fmaxf(sel[0], sel[1]); m1 = __builtin_fmaxf(sel[2], sel[3]);
#pragma unroll
    for (int e = 4; e < 16; e += 4) { m0 = MX3(m0, sel[e], sel[e + 1]); m1 = MX3(m1, sel[e + 2], sel[e + 3]); }
    PIN(m0); PIN(m1); SBAR();
    sb = MFMA32(kf[3], qf, zero); SBAR(); AT_MAX(sc); SBAR();
    sc = MFMA32(kf[4], qf, zero); SBAR(); AT_MAX(sa); SBAR();
    sa = MFMA32(kf[5], qf, zero); SBAR(); AT_MAX(sb); SBAR();
    sb = MFMA32(kf[6], qf, zero); SBAR(); AT_MAX(sc); SBAR();
    sc = MFMA32(kf[7], qf, zero); SBAR(); AT_MAX(sa); SBAR();
    AT_MAX(sb); SBAR(); AT_MAX(sc);
  } else {
#pragma unroll
    for (int i = 1; i < 9; ++i) if (w + i >= 8) kf[i] = lds16(kb + i * 1024);
    sa = MFMA32(kf[8], qf, zero);
#pragma unroll
    for (int e = 0; e < 16; ++e) sb[e] = -INFINITY;
    sel_fill<0>(sel, sb, sa);
    m0 = __builtin_fmaxf(sel[0], sel[1]); m1 = __builtin_fmaxf(sel[2], sel[3]);
#pragma unroll
    for (int e = 4; e < 16; e += 4) { m0 = MX3(m0, sel[e], sel[e + 1]); m1 = MX3(m1, sel[e + 2], sel[e + 3]); }
#pragma unroll
    for (int i = 1; i < 8; ++i) if (w + i >= 8) { sa = MFMA32(kf[i], qf, zero); AT_MAX(sa); }
  }
  dma4(57344 + w * 256, tv, rws, tsrc);
  float m = __builtin_fmaxf(m0, m1), mo = m;
  swap32f(m, mo);
  m = __builtin_fmaxf(m, mo);
  f32x16 negm;
#pragma unroll
  for (int e = 0; e < 16; ++e) negm[e] = -m;
  f32x16 o = zero;
  u32x4 pd[2], pf[2], p0, p1;
  bf16x8 va, vc, vd, ve;
  if constexpr (FULL) {
    PIN(negm);
    va = lds16(vb0 + 8 * 1040); vc = lds16(vb1 + 8 * 1040); vd = lds16(vb0); ve = lds16(vb1);
    sa = MFMA32(kf[1], qf, negm); SBAR();
  }
#pragma unroll
  for (int e = 0; e < 8; ++e) {
    const u32 p = pk2(EX2(sel[2 * e] - m), EX2(sel[2 * e + 1] - m));
    pf[e >> 2][e & 3] = p & mk[e];
    pd[e >> 2][e & 3] = p & ~mk[e];
  }
  if constexpr (FULL) {
    PIN(pf[0]); PIN(pf[1]); PIN(pd[0]); PIN(pd[1]); SBAR();
#define AT_Q(P, S, o_) do { P[(o_) & 3] = pk2(EX2(S[2 * (o_)]), EX2(S[2 * (o_) + 1])); P[((o_) & 3) + 1] = pk2(EX2(S[2 * (o_) + 2]), EX2(S[2 * (o_) + 3])); PIN(P); SBAR(); } while (0)
#define AT_STEP(S, N, i, M0, M1, M2, M3) do { \
      if ((i) < 7) { N = MFMA32(kf[(i) < 7 ? (i) + 1 : 0], qf, negm); SBAR(); } \
      u32x4 q0_, q1_; \
      AT_Q(q0_, S, 0); M0; SBAR(); AT_Q(q0_, S, 2); M1; SBAR(); AT_Q(q1_, S, 4); M2; SBAR(); AT_Q(q1_, S, 6); M3; \
      va = lds16(vb0 + (i) * 1040); vc = lds16(vb1 + (i) * 1040); p0 = q0_; p1 = q1_; SBAR(); } while (0)
#define PV(V, P) o = MFMA32(V, BF8(P), o)
    AT_STEP(sa, sb, 1, PV(va, pd[0]), PV(vc, pd[1]), PV(vd, pf[0]), PV(ve, pf[1]));
    AT_STEP(sb, sa, 2, NOP_, PV(va, p0), PV(vc, p1), NOP_);
    AT_STEP(sa, sb, 3, NOP_, PV(va, p0), PV(vc, p1), NOP_);
    AT_STEP(sb, sa, 4, NOP_, PV(va, p0), PV(vc, p1), NOP_);
    AT_STEP(sa, sb, 5, NOP_, PV(va, p0), PV(vc, p1), NOP_);
    AT_STEP(sb, sa, 6, NOP_, PV(va, p0), PV(vc, p1), NOP_);
    AT_STEP(sa, sb, 7, NOP_, PV(va, p0), PV(vc, p1), NOP_);
    PV(va, p0); PV(vc, p1);
  } else {
    o = MFMA32(lds16(vb0 + 8 * 1040), BF8(pd[0]), o);
    o = MFMA32(lds16(vb1 + 8 * 1040), BF8(pd[1]), o);
#pragma unroll
    for (int i = 1; i < 8; ++i) if (w + i >= 8) {
      sa = MFMA32(kf[i], qf, negm);
#pragma unroll
      for (int e = 0; e < 4; ++e) {
        p0[e] = pk2(EX2(sa[2 * e]), EX2(sa[2 * e + 1]));
        p1[e] = pk2(EX2(sa[8 + 2 * e]), EX2(sa[8 + 2 * e + 1]));
      }
      o = MFMA32(lds16(vb0 + i * 1040), BF8(p0), o);
      o = MFMA32(lds16(vb1 + i * 1040), BF8(p1), o);
    }
  }
  const float rl = __builtin_amdgcn_rcpf(o[8]);
  u32 a0 = pk2(o[0] * rl, o[1] * rl), a1 = pk2(o[2] * rl, o[3] * rl), b0 = pk2(o[4] * rl, o[5] * rl), b1 = pk2(o[6] * rl, o[7] * rl);
  swap32u(a0, b0); swap32u(a1, b1);
  outv = (u32x4){a0, a1, b0, b1};
}

DI u32x4 resid_epi(float (&y)[8], u32x4 res, u32x4 gate) {
#pragma unroll
  for (int i = 0; i < 4; ++i) swap32f(y[i], y[4 + i]);
  u32x4 o;
#pragma unroll
  for (int i = 0; i < 4; ++i) {
    const u32 yb = pk2(y[2 * i], y[2 * i + 1]);
    const u32 tb = pk2(bflo(yb) * bflo(gate[i]), bfhi(yb) * bfhi(gate[i]));
    o[i] = pk2(bflo(res[i]) + bflo(tb), bfhi(res[i]) + bfhi(tb));
  }
  return o;
}

#define WD_DMA(base) dma((u32)((base) + w) * 1024, pv, rwd, (u32)(((base) / 40) * 448 + ((base) % 40) * 8 + w * 8) * 1024)

template <int NF>
DI void ffn_up(int w, int lane, u32 tag, char* ws, rsrc_t rws, rsrc_t rwd, u32 pv, int m128, int nt, u32 tdv, int b) {
  constexpr int NW = NF;
  constexpr int F0 = NF == 4 ? 0 : 4;
  const int p = w & 3, tile = 4 * m128 + p;
  const u32 lv = lane * 16;
  const GAS u32* mb = (const GAS u32*)(ws + WS_MB) + b * 256;
  const u32 wsrc = WS_WP + (u32)nt * (33 * 7168) + w * 1024;
  const u32 xsrc = WS_X1 + (u32)(tile * 32 + 2 * (w >> 2)) * 1024;
  const u32 xdst = 114688 + (p * 4 + 2 * (w >> 2)) * 1024;
#define UP_W1(g, i) dma_sc1(((g) & 3) * 28672 + w * 1024 + (i) * 8192, lv, rws, wsrc + (g) * 28672 + (i) * 8192)
#define UP_X1(g, i) dma_sc1(xdst + ((g) % 3) * 16384 + (i) * 1024, lv, rws, xsrc + (g) * 4096 + (i) * 1024)
#pragma unroll
  for (int g = 1; g < 4; ++g)
#pragma unroll
    for (int i = 0; i < NW; ++i) UP_W1(g, i);
  STAMP(12);
  poll(mb + MB_X1 + lane, lane < 16 ? (lane >> 3) == (p >> 1) : lane < 30, tag);
  STAMP(13);
#pragma unroll
  for (int g = 0; g < 3; ++g) { UP_X1(g, 0); UP_X1(g, 1); }
  f32x16 acc[NF];
#pragma unroll
  for (int f = 0; f < NF; ++f) acc[f] = (f32x16){};
  u32 wl = lv + F0 * 1024, wh = lv + F0 * 1024 + 57344, xl = lv + 114688 + p * 4096;
  PIN(wl); PIN(wh); PIN(xl);
  bf16x8 xa, wa[NF], xb, wb[NF];
  u32x2 st[4];
  const u32 stv = (u32)(lane >> 5) * 1024 + (lane & 31) * 8;
#define UP_ST(i) ld8_sc1(st[i], stv, rws, WS_ST + (u32)tile * 2048 + (i) * 256)
#define UP_RX(X, ks) X = lds16(xl + (((ks) >> 2) % 3) * 16384 + ((ks) & 3) * 1024)
#define UP_RW(W, f, ks) W[f] = lds16(((((ks) >> 2) & 2) ? wh : wl) + (((ks) >> 2) & 1) * 28672 + (((ks) & 3) * 7 + (f)) * 1024)
#define UP_STEP(XC, WC, XN, WN, ks, RD, MM, D0, D1) do { \
    if (RD) { UP_RX(XN, ks); UP_RW(WN, 0, ks); } SBAR(); if (MM) acc[0] = MFMA32(WC[0], XC, acc[0]); SBAR(); \
    if (RD) { UP_RW(WN, 1, ks); UP_RW(WN, 2, ks); } SBAR(); if (MM) acc[1] = MFMA32(WC[1], XC, acc[1]); SBAR(); \
    if (RD && NF == 4) UP_RW(WN, NF - 1, ks); D0; SBAR(); if (MM) acc[2] = MFMA32(WC[2], XC, acc[2]); SBAR(); \
    D1; if (MM && NF == 4) acc[NF - 1] = MFMA32(WC[NF - 1], XC, acc[NF - 1]); SBAR(); } while (0)
#define UP_WL(g) do { if (NF == 4) UP_W1(g, 3); } while (0)
#define UP_GROUP(g, MM0, A0, A1, B0, B1, C0, C1, E0, E1) do { \
    UP_STEP(xb, wb, xa, wa, 4 * (g), true, MM0, A0, A1); UP_STEP(xa, wa, xb, wb, 4 * (g) + 1, true, true, B0, B1); \
    UP_STEP(xb, wb, xa, wa, 4 * (g) + 2, true, true, C0, C1); UP_STEP(xa, wa, xb, wb, 4 * (g) + 3, true, true, E0, E1); } while (0)
#define UP_GREFILL(g) UP_GROUP(g, true, UP_X1((g) + 2, 0), UP_X1((g) + 2, 1), UP_W1((g) + 3, 0), NOP_, UP_W1((g) + 3, 1), UP_W1((g) + 3, 2), UP_WL((g) + 3), NOP_)
  u32 td;
  WAITBAR(4); UP_GROUP(0, false, NOP_, NOP_, UP_ST(0), UP_ST(1), UP_ST(2), UP_ST(3), ld4(td, tdv, rwd, 0), NOP_);
  WAITBAR(7); UP_GREFILL(1);
  WAITBAR(NW + 7); UP_GREFILL(2);
  WAITBAR(2 * NW + 2); UP_GREFILL(3);
  WAITBAR(2 * NW + 2); UP_GREFILL(4);
  WAITBAR(2 * NW + 2);
  UP_GROUP(5, true, UP_X1(7, 0), UP_X1(7, 1),
           dma_sc1(16384 + (w < 7 ? w : 6) * 1024, lv, rws, WS_WP + (u32)nt * (33 * 7168) + 32 * 7168 + (w < 7 ? w : 6) * 1024), NOP_,
           WD_DMA(0), NOP_, WD_DMA(8), NOP_);
  WAITBAR(NW + 5);
  asm volatile("" : "+v"(st[0]), "+v"(st[1]), "+v"(st[2]), "+v"(st[3]), "+v"(td));
  float s1 = (__uint_as_float(st[0][0]) + __uint_as_float(st[1][0])) + (__uint_as_float(st[2][0]) + __uint_as_float(st[3][0]));
  float s2 = (__uint_as_float(st[0][1]) + __uint_as_float(st[1][1])) + (__uint_as_float(st[2][1]) + __uint_as_float(st[3][1]));
  { float t1 = s1, t2 = s2; swap32f(s1, t1); swap32f(s2, t2); s1 += t1; s2 += t2; }
  const float mu = s1 * (1.f / 512.f);
  const float var = __builtin_fmaxf(s2 * (1.f / 512.f) - mu * mu, 0.f) + 1e-5f;
  float rstd = __builtin_amdgcn_rsqf(var);
  const float sig = var * rstd;
  const float mh = rbf(-mu), ml = -mu - mh, sh = rbf(sig), sl = sig - sh;
  u32x4 ev = {pk2(mh, mh), pk2(ml, sh), pk2(sh, sl), 0u};
  if (lane >= 32) ev = (u32x4){0u, 0u, 0u, 0u};
  PIN(ev); PIN(rstd); SBAR();
  UP_GROUP(6, true, WD_DMA(24), WD_DMA(32), WD_DMA(40), NOP_, WD_DMA(48), NOP_, WD_DMA(144), NOP_);
  WAITBAR(7);
  UP_GROUP(7, true, WD_DMA(56), WD_DMA(64), WD_DMA(72), NOP_, WD_DMA(112), NOP_, WD_DMA(120), NOP_);
#pragma unroll
  for (int f = 0; f < NF; ++f) wa[f] = lds16(wl + 16384 + f * 1024);
  UP_STEP(xb, wb, xa, wa, 0, false, true, NOP_, NOP_);
  LBAR();
  STAMP(14);
#pragma unroll
  for (int f = 0; f < NF; ++f) acc[f] = MFMA32(wa[f], BF8(ev), acc[f]);
  WD_DMA(80); WD_DMA(88);
  SBAR();
  constexpr float C1 = -2.f * 0.7978845608028654f * 1.4426950408889634f, C2 = C1 * 0.044715f;
#pragma unroll
  for (int f = 0; f < NF; ++f) {
    float a[8];
#pragma unroll
    for (int e = 0; e < 8; ++e) {
      const float g = acc[f][e] * rstd, u = acc[f][8 + e] * rstd;
      const float z = g * __builtin_fmaf(g * g, C2, C1);
      a[e] = g * u * __builtin_amdgcn_rcpf(1.f + __builtin_amdgcn_exp2f(z));
    }
    u32 a0 = pk2(a[0], a[1]), a1 = pk2(a[2], a[3]), b0 = pk2(a[4], a[5]), b1 = pk2(a[6], a[7]);
    swap32u(a0, b0); swap32u(a1, b1);
    st16_sc1((u32x4){a0, a1, b0, b1}, rws, lv, WS_ACT + (u32)(tile * 112 + nt * 7 + F0 + f) * 1024);
    if (f == 0) { WD_DMA(96); WD_DMA(104); }
    if (f == 1) { WD_DMA(128); WD_DMA(136); }
    if (f == 2) { WD_DMA(16); WD_DMA(152); }
    SBAR();
  }
}

__global__ void __launch_bounds__(512, 2)
fused_block(const void* __restrict__ px, const void* __restrict__ pwqkv, const void* __restrict__ pwo, const void* __restrict__ pwg,
            const void* __restrict__ pwu, const void* __restrict__ pwd, const void* __restrict__ pc, const void* __restrict__ pwm,
            void* pout, char* ws) {
  __shared__ __attribute__((aligned(16))) char smem[163840];
  asm volatile("" ::"v"((u32)(uintptr_t)(LAS char*)smem));
  const int tid = threadIdx.x, lane = tid & 63, w = __builtin_amdgcn_readfirstlane(tid >> 6);
  const int b = blockIdx.x, xcd = b & 7, bi = b >> 3;
  u32 seed;
  asm volatile("s_load_dword %0, %1, 0x0" : "=s"(seed) : "s"(ws) : "memory");
  const rsrc_t rx = __builtin_amdgcn_make_buffer_rsrc((void*)px, 0, 2048 * 512 * 2, 0x00020000);
  const rsrc_t rqkv = __builtin_amdgcn_make_buffer_rsrc((void*)pwqkv, 0, 512 * 1536 * 2, 0x00020000);
  const rsrc_t rwo = __builtin_amdgcn_make_buffer_rsrc((void*)pwo, 0, 512 * 512 * 2, 0x00020000);
  const rsrc_t rwd = __builtin_amdgcn_make_buffer_rsrc((void*)pwd, 0, 1792 * 512 * 2, 0x00020000);
  const rsrc_t rout = __builtin_amdgcn_make_buffer_rsrc(pout, 0, 2048 * 512 * 2, 0x00020000);
  const rsrc_t rws = __builtin_amdgcn_make_buffer_rsrc(ws, 0, WS_BYTES, 0x00020000);
  const u32 lv = lane * 16;
  const GAS u32* mb = (const GAS u32*)(ws + WS_MB) + b * 256;
  const u32 modg = WS_MODG + (u32)xcd * 12288;
  STAMP(0);

  const int m64a = 8 * (xcd & 3) + (bi & 7), grp = 4 * (xcd >> 2) + (bi >> 3);
  const int mrole = bi & 3;
  const bool late = mrole == 1 || mrole == 2;
  const u32 mcol = mrole == 0 ? (u32)(xcd * 8 + (bi >> 2)) * 16 : 1024 + (u32)(xcd * 16 + (bi >> 2) * 2 + mrole - 1) * 16;
  constexpr u32 SCR = 155648;
#define MOD_LD(on, c0, c1, wm0, wm1) do { const rsrc_t rc_ = __builtin_amdgcn_make_buffer_rsrc((void*)pc, 0, (on) ? 512 * 2 : 0, 0x00020000); \
    const rsrc_t rm_ = __builtin_amdgcn_make_buffer_rsrc((void*)pwm, 0, (on) ? 512 * 3072 * 2 : 0, 0x00020000); \
    ld16(wm0, (u32)(tid >> 1) * 6144 + (tid & 1) * 16, rm_, mcol * 2); ld16(wm1, (u32)(tid >> 1) * 6144 + (tid & 1) * 16, rm_, mcol * 2 + 256 * 6144); \
    ld2(c0, (tid >> 1) * 2, rc_, 0); ld2(c1, (tid >> 1) * 2, rc_, 512); } while (0)
  u32 tag;
  if (mrole == 0) {
    u32 c0, c1; u32x4 wm0, wm1;
    MOD_LD(true, c0, c1, wm0, wm1);
    asm volatile("s_waitcnt vmcnt(0) lgkmcnt(0)" : "+v"(c0), "+v"(c1), "+v"(wm0), "+v"(wm1), "+s"(seed)::"memory");
    mod_part(SCR, c0, c1, wm0, wm1, w, lane);
    LBAR();
    if (w == 0) mod_publish(SCR, ws, mcol, seed + 1u, lane);
  }
  STAMP(1);
  u32x4 xr[8];
#pragma unroll
  for (int j = 0; j < 8; ++j) ld16(xr[j], lv, rx, (u32)(m64a * 64 + w * 8 + j) * 1024);
  const int wid = xcd * 28 + bi;
  const int wmat = wid >= 112, wcg = wid - wmat * 112;
  u32 wr[8];
#define WR_LD() do { const rsrc_t rg_ = __builtin_amdgcn_make_buffer_rsrc((void*)(wmat ? pwu : pwg), 0, bi < 28 ? 512 * 1792 * 2 : 0, 0x00020000); \
    _Pragma("unroll") for (int j = 0; j < 8; ++j) ld4(wr[j], (u32)(tid >> 3) * (8 * 3584) + (tid & 7) * 4, rg_, (u32)j * 3584 + (u32)wcg * 32); } while (0)
  const u32 qv = (u32)(w * 8 + (lane >> 3)) * 3072 + (((lane & 7) ^ ((((lane >> 4) & 1) | ((w & 1) << 1)) << 1)) * 16) + grp * 128;
#define QKV_DMA(s, seg) dma(65536 + ((s) & 3) * 24576 + (seg) * 8192 + w * 1024, qv, rqkv, (s) * 64 * 3072 + (seg) * 1024)
  if (w != 0) {
#pragma unroll
    for (int s = 0; s < 4; ++s) { QKV_DMA(s, 0); QKV_DMA(s, 1); if (s < 3) QKV_DMA(s, 2); }
    if (w == 1) {
      const u32 q0 = (u32)(lane >> 3) * 3072 + (((lane & 7) ^ (((lane >> 4) & 1) << 1)) * 16) + grp * 128;
#pragma unroll
      for (int s = 0; s < 4; ++s)
#pragma unroll
        for (int sg = 0; sg < 3; ++sg) if (s < 3 || sg < 2) dma(65536 + s * 24576 + sg * 8192, q0, rqkv, s * 64 * 3072 + sg * 1024);
    }
  }
  u32 l0, l1; u32x4 lm0, lm1;
  MOD_LD(late, l0, l1, lm0, lm1); WR_LD();
  if (w == 0) WAITV(12); else if (w == 1) WAITV(34); else WAITV(23);
  asm volatile("s_waitcnt lgkmcnt(0)" : "+s"(seed)::"memory");
  tag = seed + 1u;
  float xn[8][8];
  {
    asm volatile("" : "+v"(xr[0]), "+v"(xr[1]), "+v"(xr[2]), "+v"(xr[3]), "+v"(xr[4]), "+v"(xr[5]), "+v"(xr[6]), "+v"(xr[7])::"memory");
    float S[8], Q[8];
#pragma unroll
    for (int j = 0; j < 8; ++j) {
#pragma unroll
      for (int e = 0; e < 4; ++e) { xn[j][2 * e] = bflo(xr[j][e]); xn[j][2 * e + 1] = bfhi(xr[j][e]); }
      float q = xn[j][0] * xn[j][0];
#pragma unroll
      for (int e = 1; e < 8; ++e) q = __builtin_fmaf(xn[j][e], xn[j][e], q);
      S[j] = ((xn[j][0] + xn[j][1]) + (xn[j][2] + xn[j][3])) + ((xn[j][4] + xn[j][5]) + (xn[j][6] + xn[j][7])); Q[j] = q;
    }
#pragma unroll
    for (int j = 0; j < 4; ++j) { swap32f(S[j], S[j + 4]); S[j] += S[j + 4]; swap32f(Q[j], Q[j + 4]); Q[j] += Q[j + 4]; }
#pragma unroll
    for (int j = 0; j < 2; ++j) { swap16f(S[j], S[j + 2]); S[j] += S[j + 2]; swap16f(Q[j], Q[j + 2]); Q[j] += Q[j + 2]; }
    float ra[2], rb[2];
#pragma unroll
    for (int j = 0; j < 2; ++j) {
      const float mu = row16_sum(S[j]) * (1.f / 512.f);
      const float var = __builtin_fmaxf(row16_sum(Q[j]) * (1.f / 512.f) - mu * mu, 0.f) + 1e-5f;
      ra[j] = __builtin_amdgcn_rsqf(var); rb[j] = -mu * ra[j];
    }
#pragma unroll
    for (int j = 0; j < 8; ++j) {
      const float a = rdlane(ra[j & 1], (j >> 1) * 16), c = rdlane(rb[j & 1], (j >> 1) * 16);
#pragma unroll
      for (int e = 0; e < 8; ++e) { xn[j][e] = __builtin_fmaf(xn[j][e], a, c); PIN(xn[j][e]); }
    }
  }
  if (w == 0) {
    u32x4 a0, a1, a2, a3;
#pragma unroll 1
    for (u32 spins = 0; spins < SPIN_CAP; ++spins) {
      ld16_sc1(a0, lane * 32, rws, modg); ld16_sc1(a1, lane * 32 + 16, rws, modg);
      ld16_sc1(a2, lane * 32, rws, modg + 2048); ld16_sc1(a3, lane * 32 + 16, rws, modg + 2048);
      asm volatile("s_waitcnt vmcnt(0)" : "+v"(a0), "+v"(a1), "+v"(a2), "+v"(a3)::"memory");
      if (__all(tags_ok(a0, a1, tag) && tags_ok(a2, a3, tag))) break;
      __builtin_amdgcn_s_sleep(1);
    }
    sts16(SCR + 6144 + lv, gvals(a0, a1)); sts16(SCR + 7168 + lv, gvals(a2, a3));
  } else {
    if (w <= 3) dma4(SCR + w * 1024, (u32)(256 + 32 * (bi & 7) + (lane & 31)) * 3072 + grp * 128, rqkv, (w - 1) * 1024);
    else if (w <= 5) dma4(SCR + w * 1024, (u32)(16 * bi + 8 * (w - 4) + (lane >> 3)) * 1024 + (lane & 7) * 128, rwo, 0);
  }
  LBAR();
  STAMP(2);
  asm volatile("s_waitcnt vmcnt(8)" : "+v"(l0), "+v"(l1), "+v"(lm0), "+v"(lm1)::"memory");
  {
    const u32x4 sa = lds16u(SCR + 6144 + lv), ca = lds16u(SCR + 7168 + lv);
    float fs[8], fa[8];
#pragma unroll
    for (int e = 0; e < 4; ++e) {
      const u32 t = pk2(1.f + bflo(ca[e]), 1.f + bfhi(ca[e]));
      fs[2 * e] = bflo(t); fs[2 * e + 1] = bfhi(t); fa[2 * e] = bflo(sa[e]); fa[2 * e + 1] = bfhi(sa[e]);
    }
#pragma unroll
    for (int j = 0; j < 8; ++j) {
      u32x4 h;
#pragma unroll
      for (int e = 0; e < 4; ++e) h[e] = pk2(__builtin_fmaf(xn[j][2 * e], fs[2 * e], fa[2 * e]), __builtin_fmaf(xn[j][2 * e + 1], fs[2 * e + 1], fa[2 * e + 1]));
      const int r = w * 8 + j;
      sts16((u32)r * 1024 + ((lane ^ (r & 15)) * 16), h);
    }
  }
  if (late) mod_part(SCR, l0, l1, lm0, lm1, w, lane);
  const int n64 = bi >> 2, m64 = 4 * xcd + (bi & 3);
  const u32 pv = (u32)(lane >> 3) * 1024 + (((lane & 7) ^ (((lane >> 4) & 1) << 2)) * 16) + n64 * 128;
  {
    const int wm = w >> 2, hn = w & 3, fr = lane & 15, g = lane >> 4;
    u32 ab[4];
#pragma unroll
    for (int t = 0; t < 4; ++t) { ab[t] = (u32)(wm * 32 + fr) * 1024 + ((g ^ (fr & 3)) * 16) + ((t ^ (fr >> 2)) * 64); PIN(ab[t]); }
    u32 bl = 65536 + (u32)(8 * g + (fr >> 2)) * 128 + ((hn ^ (((fr >> 3) & 1) | ((g & 1) << 1))) * 32) + (lane & 3) * 8, bh = bl + 49152;
    PIN(bl); PIN(bh);
    f32x4 acc[2][3] = {};
    bf16x8 fa[2][2][2], fb[2][2][3];
#define Q_RA(S, s, kk, m) fa[S][kk][m] = lds16(ab[(2 * (s) + (kk)) & 3] + ((2 * (s) + (kk)) >> 2) * 256 + (m) * 16384)
#define Q_RB(S, s, kk, sg) fb[S][kk][sg] = ldtr((((s) & 2) ? bh : bl) + ((s) & 1) * 24576 + (sg) * 8192 + (kk) * 4096)
#define Q_M(S, kk, m, sg) acc[m][sg] = (sg) < 2 ? MFMA16(fb[S][kk][sg], fa[S][kk][m], acc[m][sg]) : MFMA16(fa[S][kk][m], fb[S][kk][sg], acc[m][sg])
#define Q_STAGE(C, N, s, RD, D0, D1, D2) do { \
      if (RD) { Q_RA(N, s, 0, 0); Q_RA(N, s, 0, 1); } Q_M(C, 0, 0, 0); SBAR(); \
      if (RD) Q_RB(N, s, 0, 0); Q_M(C, 0, 0, 1); SBAR(); \
      if (RD) Q_RB(N, s, 0, 1); Q_M(C, 0, 0, 2); SBAR(); \
      if (RD) Q_RB(N, s, 0, 2); Q_M(C, 0, 1, 0); SBAR(); \
      if (RD) { Q_RA(N, s, 1, 0); Q_RA(N, s, 1, 1); } Q_M(C, 0, 1, 1); SBAR(); \
      if (RD) Q_RB(N, s, 1, 0); Q_M(C, 0, 1, 2); SBAR(); \
      if (RD) Q_RB(N, s, 1, 1); Q_M(C, 1, 0, 0); SBAR(); \
      if (RD) Q_RB(N, s, 1, 2); Q_M(C, 1, 0, 1); SBAR(); \
      D0; Q_M(C, 1, 0, 2); SBAR(); D1; Q_M(C, 1, 1, 0); SBAR(); D2; Q_M(C, 1, 1, 1); SBAR(); Q_M(C, 1, 1, 2); SBAR(); } while (0)
#define WO_DMA(i) dma(65536 + ((i) * 8 + w) * 1024, pv, rwo, (u32)((i) * 8 + w) * 8192)
    LBAR();
    if (late && w == 0) { mod_publish(SCR, ws, mcol, tag, lane); asm volatile("s_waitcnt lgkmcnt(0)" ::: "memory"); }
    QKV_DMA(3, 2);
#pragma unroll
    for (int kk = 0; kk < 2; ++kk) { Q_RA(0, 0, kk, 0); Q_RA(0, 0, kk, 1); Q_RB(0, 0, kk, 0); Q_RB(0, 0, kk, 1); Q_RB(0, 0, kk, 2); }
    STAMP(20);
    LBAR(); Q_STAGE(0, 1, 1, true, QKV_DMA(4, 0), QKV_DMA(4, 1), QKV_DMA(4, 2));
    LBAR(); Q_STAGE(1, 0, 2, true, QKV_DMA(5, 0), QKV_DMA(5, 1), QKV_DMA(5, 2));
    WAITBAR(6); Q_STAGE(0, 1, 3, true, QKV_DMA(6, 0), QKV_DMA(6, 1), QKV_DMA(6, 2));
    WAITBAR(6); Q_STAGE(1, 0, 4, true, QKV_DMA(7, 0), QKV_DMA(7, 1), QKV_DMA(7, 2));
    WAITBAR(6); Q_STAGE(0, 1, 5, true, WO_DMA(0), WO_DMA(1), WO_DMA(2));
    WAITBAR(6); Q_STAGE(1, 0, 6, true, WO_DMA(3), WO_DMA(4), WO_DMA(5));
    WAITBAR(6); Q_STAGE(0, 1, 7, true, WO_DMA(6), WO_DMA(7), NOP_);
    Q_STAGE(1, 0, 0, false, NOP_, NOP_, NOP_);
    STAMP(3);
    constexpr float QS = 0.25f * 1.4426950408889634f;
    const int head = 4 * grp + hn, tile = 2 * m64a + wm;
    const u32 dst = (u32)(head * 64 + tile) * 1024;
    {
      u32 p0 = pk2(acc[0][0][0] * QS, acc[0][0][1] * QS), p1 = pk2(acc[0][0][2] * QS, acc[0][0][3] * QS);
      u32 r0 = pk2(acc[1][0][0] * QS, acc[1][0][1] * QS), r1 = pk2(acc[1][0][2] * QS, acc[1][0][3] * QS);
      swap16u(p0, r0); swap16u(p1, r1);
      st16_sc1((u32x4){p0, p1, r0, r1}, rws, lv, WS_Q + dst);
    }
    {
      u32 p0 = pk2(acc[0][1][0], acc[0][1][1]), p1 = pk2(acc[0][1][2], acc[0][1][3]);
      u32 r0 = pk2(acc[1][1][0], acc[1][1][1]), r1 = pk2(acc[1][1][2], acc[1][1][3]);
      swap16u(p0, r0); swap16u(p1, r1);
      st16_sc1((u32x4){p0, p1, r0, r1}, rws, lv, WS_K + dst);
    }
    {
      u32 p0 = pk2(acc[0][2][0], acc[0][2][1]), p1 = pk2(acc[0][2][2], acc[0][2][3]);
      u32 r0 = pk2(acc[1][2][0], acc[1][2][1]), r1 = pk2(acc[1][2][2], acc[1][2][3]);
      swap32u(p0, r0); swap32u(p1, r1);
      st16_sc1((u32x4){p0, p1, r0, r1}, rws, lv, WS_V + dst);
    }
  }
  const int mh = w & 1, ksl = w >> 1;
  const u32 ecol = (u32)n64 * 64 + ksl * 16 + (lane >> 5) * 8;
  const u32 erow = (u32)m64 * 64 + mh * 32 + (lane & 31);
  const int trh = lane >> 5, trb = (lane >> 4) & 1, trq = (lane & 15) >> 2;
  u32 tb[2];
#pragma unroll
  for (int c = 0; c < 2; ++c) tb[c] = (u32)(8 * trh + trq) * 128 + ((((2 * c + trb + ksl) & 3) ^ (trq & 2)) * 32) + (lane & 3) * 8;
  u32x4 xres, gav, gbv, x1v;
  const int m128 = 4 * (xcd >> 1) + (bi & 3), nt = 8 * (xcd & 1) + (bi >> 2);
  constexpr u32 SC2 = 33280, SC3 = 34304;
  const int wn = wcg / 7, wfr = wcg - wn * 7;
  const u32 wdst = WS_WP + (u32)wn * (33 * 7168) + wfr * 1024;
  const u32 wmb = mbox(((lane >> 2) << 1) | (wn >> 3), ((wn & 7) << 2) | (lane & 3)) + wfr * 2 + wmat;
  {
    u32x4 g0, g1, g2, g3, g4, g5, g6, g7;
    const u32 kv = (u32)(tid >> 3) * 32, ev = ecol * 4;
#define G_LD() do { ld16_sc1(g0, kv, rws, modg + 6144); ld16_sc1(g1, kv + 16, rws, modg + 6144); ld16_sc1(g2, kv, rws, modg + 8192); ld16_sc1(g3, kv + 16, rws, modg + 8192); \
      ld16_sc1(g4, ev, rws, modg + 4096); ld16_sc1(g5, ev + 16, rws, modg + 4096); ld16_sc1(g6, ev, rws, modg + 10240); ld16_sc1(g7, ev + 16, rws, modg + 10240); } while (0)
#define G_WAIT() asm volatile("s_waitcnt vmcnt(0)" : "+v"(g0), "+v"(g1), "+v"(g2), "+v"(g3), "+v"(g4), "+v"(g5), "+v"(g6), "+v"(g7), "+v"(xres)::"memory")
    G_LD();
    ld16(xres, erow * 1024 + ecol * 2, rx, 0);
    asm volatile("s_waitcnt vmcnt(9) lgkmcnt(0)\n\ts_barrier"
                 : "+v"(wr[0]), "+v"(wr[1]), "+v"(wr[2]), "+v"(wr[3]), "+v"(wr[4]), "+v"(wr[5]), "+v"(wr[6]), "+v"(wr[7])::"memory");
    if (w == 0) {
      const int hd = 4 * grp + (lane & 3), rg = (m64a >> 2) + ((lane >> 2) & 1);
      post(ws, lane < 8 ? mbox(hd >> 2, (rg << 2) | (hd & 3)) + MB_QKV + (m64a & 3) + (lane < 4 ? 4 : 0) : WS_ARR / 4 + b, lane < 4 || (lane < 8 && rg < 8) || lane == 8, tag);
    }
    STAMP(4);
    if (tid < 16) sts16(16384 + tid * 1040 + 1024, (u32x4){0x3f803f80u, 0x3f803f80u, 0x3f803f80u, 0x3f803f80u});
    G_WAIT();
#pragma unroll 1
    for (u32 spins = 0; spins < SPIN_CAP; ++spins) {
      if (__all(tags_ok(g0, g1, tag) && tags_ok(g2, g3, tag) && tags_ok(g4, g5, tag) && tags_ok(g6, g7, tag))) break;
      __builtin_amdgcn_s_sleep(1);
      G_LD(); G_WAIT();
    }
    gav = gvals(g4, g5); gbv = gvals(g6, g7);
    if (bi < 28) {
      const u32x4 sb = gvals(g0, g1), cb = gvals(g2, g3);
      float u0 = 0.f, u1 = 0.f, b0 = 0.f, b1 = 0.f;
      u32x4 e0, e1;
#pragma unroll
      for (int e = 0; e < 4; ++e) {
        const u32 t = pk2(1.f + bflo(cb[e]), 1.f + bfhi(cb[e]));
        const float sl = bflo(t), sh = bfhi(t);
        e0[e] = pk2(bflo(wr[2 * e]) * sl, bflo(wr[2 * e + 1]) * sh);
        e1[e] = pk2(bfhi(wr[2 * e]) * sl, bfhi(wr[2 * e + 1]) * sh);
        u0 += bflo(e0[e]) + bfhi(e0[e]); u1 += bflo(e1[e]) + bfhi(e1[e]);
        b0 = __builtin_fmaf(bflo(wr[2 * e]), bflo(sb[e]), __builtin_fmaf(bflo(wr[2 * e + 1]), bfhi(sb[e]), b0));
        b1 = __builtin_fmaf(bfhi(wr[2 * e]), bflo(sb[e]), __builtin_fmaf(bfhi(wr[2 * e + 1]), bfhi(sb[e]), b1));
      }
      const u32 tw = 40960 + w * 2048;
      sts16(tw + lane * 32, e0); sts16(tw + lane * 32 + 16, e1);
      swap32f(u0, b0); u0 += b0;
      swap32f(u1, b1); u1 += b1;
      swap16f(u0, u1); u0 += u1;
      u0 += dppf<0x128>(u0);
      if ((lane & 8) == 0) sts4f(SC2 + (u32)((lane >> 5) * 16 + (lane & 7) * 2 + ((lane >> 4) & 1)) * 32 + w * 4, u0);
      asm volatile("s_waitcnt lgkmcnt(0)" ::: "memory");
      e0 = lds16u(tw + lv); e1 = lds16u(tw + 1024 + lv);
      const u32 vo = (u32)(w * 4 + (lane >> 5)) * 7168 + (u32)(((lane >> 4) & 1) * 32 + wmat * 16 + (lane & 15)) * 16;
      st16_sc1(e0, rws, vo, wdst);
      st16_sc1(e1, rws, vo + 2 * 7168, wdst);
    }
  }
  STAMP(5);
  {
    const int head = 4 * xcd + (bi & 3), range = bi >> 2, t0 = 8 * range - 8;
    u32 mk[8];
#pragma unroll
    for (int e = 0; e < 8; ++e) {
      const int k0 = ((2 * e) & 3) + 8 * ((2 * e) >> 2) + 4 * (lane >> 5);
      mk[e] = (k0 > (lane & 31) ? 0xffffu : 0u) | (k0 + 1 > (lane & 31) ? 0xffff0000u : 0u);
      PIN(mk[e]);
    }
    u32 kb = (u32)(w * 1024 + lane * 16);
    u32 vb0 = 16384 + w * 1040 + ((lane & 31) < 16 ? (lane >> 5) * 256 + (lane & 31) * 16 : 1024);
    u32 vb1 = 16384 + w * 1040 + ((lane & 31) < 16 ? 512 + (lane >> 5) * 256 + (lane & 31) * 16 : 1024);
    PIN(kb); PIN(vb0); PIN(vb1);
    poll(mb + MB_QKV + (w >> 1) + (lane & 1) * 4, lane < 2 && (range > 0 || lane == 1), tag);
    STAMP(6);
    u32x4 q;
    const u32 src = (u32)(head * 64 + t0 + w) * 1024;
    dma_sc1((w + 8) * 1024, lv, rws, WS_K + src + 8192);
    ld16_sc1(q, lv, rws, WS_Q + src + 8192);
    if (range > 0) dma_sc1(w * 1024, lv, rws, WS_K + src);
    dma_sc1(16384 + (w + 8) * 1040, lv, rws, WS_V + src + 8192);
    if (range > 0) dma_sc1(16384 + w * 1040, lv, rws, WS_V + src);
    asm volatile("s_waitcnt vmcnt(0) lgkmcnt(0)\n\ts_barrier" : "+v"(q)::"memory");
    STAMP(7);
    if (bi < 28 && w == 7) {
      post(ws, wmb + MB_W, lane < 16, tag);
      float t = sum8(lds16f(SC2 + (lane & 31) * 32), lds16f(SC2 + (lane & 31) * 32 + 16));
      float o = t;
      swap16f(t, o);
      const float uh = rbf(t), ul = t - uh, bh = rbf(o), bl = o - bh;
      u32x4 efr = {pk2(uh, ul), pk2(uh, bh), pk2(bl, bh), 0u};
      if (lane >= 32) efr = (u32x4){0u, 0u, 0u, 0u};
      if ((lane & 16) == 0) st16_sc1(efr, rws, (u32)((lane >> 5) * 32 + wmat * 16 + (lane & 15)) * 16, wdst + 32 * 7168);
    }
    u32x4 ov;
    const u32 tv = (u32)(w * 58 + (lane < 58 ? lane : 57)) * 128, tsrc = WS_WP + (u32)nt * (33 * 7168) + (u32)(bi & 3) * (462 * 128);
    if (range > 0) attn_wave<true>(w, lane, BF8(q), mk, kb, vb0, vb1, ov, tv, rws, tsrc);
    else attn_wave<false>(w, lane, BF8(q), mk, kb, vb0, vb1, ov, tv, rws, tsrc);
    st16_sc1(ov, rws, lv, WS_O + (u32)((8 * range + w) * 32 + head) * 1024);
    STAMP(8);
    u32 fw;
    asm volatile("buffer_load_dword %0, %1, %2, %3 offen sc1" : "=v"(fw) : "v"((lane & 15) * 4), "s"(rws), "s"(WS_MB + (u32)b * 1024 + MB_W * 4) : "memory");
    bf16x8 wf[8][2];
#pragma unroll
    for (int ks = 0; ks < 8; ++ks)
#pragma unroll
      for (int c = 0; c < 2; ++c) wf[ks][c] = ldtr(65536 + ksl * 16384 + tb[c] + ks * 2048);
    asm volatile("s_waitcnt vmcnt(0) lgkmcnt(0)\n\ts_barrier" : "+v"(fw)::"memory");
    if (w == 0) post(ws, mbox(range, lane) + MB_O + head, lane < 32, tag);
    if (bi < 28 && w == 7) post(ws, wmb + MB_E, lane < 16, tag);
    STAMP(21);
    if (!__all((lane & 15) >= 14 || fw == tag)) poll(mb + MB_W + lane, lane < 14, tag);
#pragma unroll
    for (int i = 0; i < 4; ++i) if (i < 3 || w < 4) dma_sc1(w * 1024 + i * 8192, lv, rws, WS_WP + (u32)nt * (33 * 7168) + w * 1024 + i * 8192);
    if (b == 255 && w == 1) {
#pragma unroll
      for (int i = 0; i < 4; ++i) poll((const GAS u32*)(ws + WS_ARR) + i * 64 + lane, true, tag);
      if (lane == 0) __hip_atomic_store((GAS u32*)(ws + WS_SEED), tag, RLX_AGENT);
    }
    const int tile = 2 * m64 + mh;
    poll(mb + MB_O + ksl * 8 + lane, lane < 8, tag);
    STAMP(9);
    u32x4 of[8];
#pragma unroll
    for (int ks = 0; ks < 8; ++ks) ld16_sc1(of[ks], lv, rws, WS_O + (u32)(tile * 32 + ksl * 8 + ks) * 1024);
    f32x16 acc[2] = {};
#pragma unroll
    for (int ks = 0; ks < 8; ++ks) {
      WAITV1(7 - ks, of[ks]);
      acc[0] = MFMA32(wf[ks][0], BF8(of[ks]), acc[0]);
      acc[1] = MFMA32(wf[ks][1], BF8(of[ks]), acc[1]);
    }
    const u32 rb = mh ? 36864 : 131072;
#pragma unroll
    for (int j = 1; j < 4; ++j) {
      const u32 a = rb + (u32)(((ksl + j) & 3) * 3 + j - 1) * 2048 + lv;
      const int o = (j & 1) * 8;
      sts16f(a, (f32x4){acc[j >> 1][o], acc[j >> 1][o + 1], acc[j >> 1][o + 2], acc[j >> 1][o + 3]});
      sts16f(a + 1024, (f32x4){acc[j >> 1][o + 4], acc[j >> 1][o + 5], acc[j >> 1][o + 6], acc[j >> 1][o + 7]});
    }
    LBAR();
    float y[8];
#pragma unroll
    for (int e = 0; e < 8; ++e) y[e] = acc[0][e];
#pragma unroll
    for (int j = 0; j < 3; ++j) {
      const f32x4 a = lds16f(rb + (u32)(ksl * 3 + j) * 2048 + lv), d = lds16f(rb + (u32)(ksl * 3 + j) * 2048 + 1024 + lv);
#pragma unroll
      for (int e = 0; e < 4; ++e) { y[e] += a[e]; y[4 + e] += d[e]; }
    }
    x1v = resid_epi(y, xres, gav);
    st16_sc1(x1v, rws, lv, WS_X1 + (u32)(tile * 32 + n64 * 4 + ksl) * 1024);
    STAMP(10);
    {
      float s = 0.f, q = 0.f;
#pragma unroll
      for (int e = 0; e < 4; ++e) { const float a = bflo(x1v[e]), c = bfhi(x1v[e]); s += a + c; q = __builtin_fmaf(a, a, __builtin_fmaf(c, c, q)); }
      swap32f(s, q);
      sts4f(SC3 + (u32)(w * 32 + (lane & 31)) * 8 + (lane >> 5) * 4, s + q);
    }
    LBAR();
    if (ksl == 0) {
      const u32 a = SC3 + (u32)(mh * 32 + (lane & 31)) * 8;
      const f32x2 t = (lds8f(a) + lds8f(a + 512)) + (lds8f(a + 1024) + lds8f(a + 1536));
      if (lane < 32) __builtin_amdgcn_raw_buffer_store_b64(__builtin_bit_cast(u32x2, t), rws, WS_ST + (u32)(tile * 8 + n64) * 256 + lane * 8, 0, 16);
    }
    WAITBAR(0);
    if (w == 0) post(ws, mbox(((m64 >> 3) << 1) | (lane >> 3), ((lane & 7) << 2) | ((m64 >> 1) & 3)) + MB_X1 + (m64 & 1) * 8 + n64, lane < 16, tag);
    STAMP(11);
  }
  const u32 tdv = (u32)((bi & 3) * 448 + (w < 7 ? w : 6) * 64 + lane) * 1024 + n64 * 128;
  if (w < 4) ffn_up<4>(w, lane, tag, ws, rws, rwd, pv, m128, nt, tdv, b);
  else ffn_up<3>(w, lane, tag, ws, rws, rwd, pv, m128, nt, tdv, b);
  STAMP(16);
  {
    u32 t0 = tb[0] + ksl * 40960, t1 = tb[1] + ksl * 40960;
    PIN(t0); PIN(t1);
    WAITBAR(0);
    if (w == 0) post(ws, mbox(m128 >> 1, ((lane >> 1) << 2) | ((2 * m128 + (lane & 1)) & 3)) + MB_ACT + nt, lane < 16, tag);
    bf16x8 wp[8][2];
#pragma unroll
    for (int s = 0; s < 8; ++s) { wp[s][0] = ldtr(t0 + s * 2048); wp[s][1] = ldtr(t1 + s * 2048); }
    LBAR();
#pragma unroll
    for (int i = 0; i < 8; ++i) dma((u32)((w >> 1) * 40 + (w & 1) * 8 + i) * 1024, pv, rwd, (u32)((w >> 1) * 448 + 320 + (w & 1) * 64 + i * 8) * 1024);
    STAMP(15);
    poll(mb + MB_ACT + ksl * 4 + lane, lane < 4, tag);
    STAMP(17);
    u32x4 af[28];
    const u32 asrc = WS_ACT + (u32)((2 * m64 + mh) * 112 + ksl * 28) * 1024;
#pragma unroll
    for (int ks = 0; ks < 8; ++ks) ld16_sc1(af[ks], lv, rws, asrc + ks * 1024);
    LBAR();
    f32x16 acc[2] = {};
    bf16x8 wa0, wa1, wb0, wb1;
#define DN_PG(s) ((s) < 20 ? (s) * 2048 : ((s) - 20) * 2048)
#define DN_RD(A, B, s) do { A = ldtr(t0 + DN_PG(s)); B = ldtr(t1 + DN_PG(s)); } while (0)
#define DN_MM(A, B, ks) do { if ((ks) + 8 < 28) ld16_sc1(af[(ks) + 8 < 28 ? (ks) + 8 : 0], lv, rws, asrc + ((ks) + 8) * 1024); WAITV1((ks) + 8 < 28 ? 8 : 27 - (ks), af[ks]); \
      acc[0] = MFMA32(A, BF8(af[ks]), acc[0]); acc[1] = MFMA32(B, BF8(af[ks]), acc[1]); SBAR(); } while (0)
#pragma unroll
    for (int ks = 0; ks < 7; ++ks) DN_MM(wp[ks][0], wp[ks][1], ks);
    DN_RD(wa0, wa1, 8); DN_MM(wp[7][0], wp[7][1], 7);
#pragma unroll
    for (int ks = 8; ks < 28; ks += 2) {
      if (ks == 20) LBAR();
      DN_RD(wb0, wb1, ks + 1); DN_MM(wa0, wa1, ks);
      if (ks + 2 < 28) DN_RD(wa0, wa1, ks + 2);
      DN_MM(wb0, wb1, ks + 1);
    }
    STAMP(18);
    const u32 sb = (u32)ksl * 40960 + 16384 + mh * 6144 + lv;
#pragma unroll
    for (int j = 1; j < 4; ++j) {
      const int o = (j & 1) * 8;
      sts16f(sb + (j - 1) * 2048, (f32x4){acc[j >> 1][o], acc[j >> 1][o + 1], acc[j >> 1][o + 2], acc[j >> 1][o + 3]});
      sts16f(sb + (j - 1) * 2048 + 1024, (f32x4){acc[j >> 1][o + 4], acc[j >> 1][o + 5], acc[j >> 1][o + 6], acc[j >> 1][o + 7]});
    }
    LBAR();
    float y[8];
#pragma unroll
    for (int e = 0; e < 8; ++e) y[e] = acc[0][e];
#pragma unroll
    for (int j = 1; j < 4; ++j) {
      const u32 a = (u32)((ksl - j) & 3) * 40960 + 16384 + mh * 6144 + (j - 1) * 2048 + lv;
      const f32x4 c = lds16f(a), d = lds16f(a + 1024);
#pragma unroll
      for (int e = 0; e < 4; ++e) { y[e] += c[e]; y[4 + e] += d[e]; }
    }
    const u32x4 ov = resid_epi(y, x1v, gbv);
    __builtin_amdgcn_raw_buffer_store_b128(ov, rout, erow * 1024 + ecol * 2, 0, 0);
    STAMP(19);
  }
}

extern "C" void kernel_launch(void* const* d_in, const int* in_sizes, int n_in, void* d_out, int out_size, void* d_ws, size_t ws_size,
                              hipStream_t stream) {
  fused_block<<<256, 512, 0, stream>>>(d_in[0], d_in[1], d_in[2], d_in[4], d_in[5], d_in[6], d_in[7], d_in[8], d_out, (char*)d_ws);
}
```

```cpp
#include <hip/hip_runtime.h>
#include <stdint.h>

typedef unsigned u32;
typedef unsigned long long u64;
typedef __attribute__((ext_vector_type(2))) u32 u32x2;
typedef __attribute__((ext_vector_type(4))) u32 u32x4;
typedef __attribute__((ext_vector_type(2))) float f32x2;
typedef __attribute__((ext_vector_type(4))) float f32x4;
typedef __attribute__((ext_vector_type(16))) float f32x16;
typedef __attribute__((ext_vector_type(8))) short bf16x8;
typedef __attribute__((ext_vector_type(2))) __bf16 bf16x2;
typedef __amdgpu_buffer_rsrc_t rsrc_t;
#define LAS __attribute__((address_space(3)))
#define GAS __attribute__((address_space(1)))
#define DI __device__ __forceinline__
#define RLX_AGENT __ATOMIC_RELAXED, __HIP_MEMORY_SCOPE_AGENT

constexpr u32 WS_SEED = 0;
constexpr u32 WS_ARR = 4096;
constexpr u32 WS_MODG = 65536;
constexpr u32 WS_MB = 262144;
constexpr u32 MB_QKV = 0, MB_O = 32, MB_X1 = 64, MB_E = 80, MB_W = 96, MB_ACT = 128;
constexpr u32 WS_ST = 786432;
constexpr u32 WS_Q = 1u << 20, WS_K = 3u << 20, WS_V = 5u << 20;
constexpr u32 WS_O = 7u << 20;
constexpr u32 WS_X1 = 9u << 20;
constexpr u32 WS_ACT = 11u << 20;
constexpr u32 WS_WP = 18u << 20;
constexpr u32 WS_BYTES = 22u << 20;
constexpr u32 WS_DBG = 24u << 20;
constexpr u32 SPIN_CAP = 1u << 15;

DI float bflo(u32 w) { return __uint_as_float(w << 16); }
DI float bfhi(u32 w) { return __uint_as_float(w & 0xffff0000u); }
DI u32 pk2(float a, float b) { f32x2 v = {a, b}; return __builtin_bit_cast(u32, __builtin_convertvector(v, bf16x2)); }
DI float rbf(float a) { return bflo(pk2(a, a)); }
template <int CTRL> DI float dppf(float x) { return __builtin_bit_cast(float, __builtin_amdgcn_update_dpp(0, __builtin_bit_cast(int, x), CTRL, 0xf, 0xf, true)); }
DI void swap32f(float& a, float& b) { auto r = __builtin_amdgcn_permlane32_swap(__float_as_uint(a), __float_as_uint(b), false, false); a = __uint_as_float(r[0]); b = __uint_as_float(r[1]); }
DI void swap16f(float& a, float& b) { auto r = __builtin_amdgcn_permlane16_swap(__float_as_uint(a), __float_as_uint(b), false, false); a = __uint_as_float(r[0]); b = __uint_as_float(r[1]); }
DI void swap32u(u32& a, u32& b) { auto r = __builtin_amdgcn_permlane32_swap(a, b, false, false); a = r[0]; b = r[1]; }
DI void swap16u(u32& a, u32& b) { auto r = __builtin_amdgcn_permlane16_swap(a, b, false, false); a = r[0]; b = r[1]; }
DI float row16_sum(float x) { x += dppf<0xB1>(x); x += dppf<0x4E>(x); x += dppf<0x141>(x); x += dppf<0x140>(x); return x; }
DI float rdlane(float x, int l) { return __uint_as_float(__builtin_amdgcn_readlane(__float_as_uint(x), l)); }

#define MFMA16(a, b, c) __builtin_amdgcn_mfma_f32_16x16x32_bf16((a), (b), (c), 0, 0, 0)
#define MFMA32(a, b, c) __builtin_amdgcn_mfma_f32_32x32x16_bf16((a), (b), (c), 0, 0, 0)
#define BF8(x) __builtin_bit_cast(bf16x8, (x))
#define WAITV(n) asm volatile("s_waitcnt vmcnt(%0)" ::"i"(n) : "memory")
#define WAITBAR(n) asm volatile("s_waitcnt vmcnt(%0) lgkmcnt(0)\n\ts_barrier" ::"i"(n) : "memory")
#define LBAR() asm volatile("s_waitcnt lgkmcnt(0)\n\ts_barrier" ::: "memory")
#define SBAR() __builtin_amdgcn_sched_barrier(0)
#define NOP_ do {} while (0)

DI void dma(u32 lds, u32 voff, rsrc_t rs, u32 soff) {
  asm volatile("s_mov_b32 m0, %0\n\ts_nop 0\n\tbuffer_load_dwordx4 %1, %2, %3 offen lds" ::"s"(lds), "v"(voff), "s"(rs), "s"(soff) : "memory");
}
DI void dma_sc1(u32 lds, u32 voff, rsrc_t rs, u32 soff) {
  asm volatile("s_mov_b32 m0, %0\n\ts_nop 0\n\tbuffer_load_dwordx4 %1, %2, %3 offen sc1 lds" ::"s"(lds), "v"(voff), "s"(rs), "s"(soff) : "memory");
}
DI void dma4(u32 lds, u32 voff, rsrc_t rs, u32 soff) {
  asm volatile("s_mov_b32 m0, %0\n\ts_nop 0\n\tbuffer_load_dword %1, %2, %3 offen lds" ::"s"(lds), "v"(voff), "s"(rs), "s"(soff) : "memory");
}
DI void ld16(u32x4& r, u32 voff, rsrc_t rs, u32 soff) { asm volatile("buffer_load_dwordx4 %0, %1, %2, %3 offen" : "=v"(r) : "v"(voff), "s"(rs), "s"(soff) : "memory"); }
DI void ld16_sc1(u32x4& r, u32 voff, rsrc_t rs, u32 soff) { asm volatile("buffer_load_dwordx4 %0, %1, %2, %3 offen sc1" : "=v"(r) : "v"(voff), "s"(rs), "s"(soff) : "memory"); }
DI void ld8_sc1(u32x2& r, u32 voff, rsrc_t rs, u32 soff) { asm volatile("buffer_load_dwordx2 %0, %1, %2, %3 offen sc1" : "=v"(r) : "v"(voff), "s"(rs), "s"(soff) : "memory"); }
DI void ld4(u32& r, u32 voff, rsrc_t rs, u32 soff) { asm volatile("buffer_load_dword %0, %1, %2, %3 offen" : "=v"(r) : "v"(voff), "s"(rs), "s"(soff) : "memory"); }
DI void ld2(u32& r, u32 voff, rsrc_t rs, u32 soff) { asm volatile("buffer_load_ushort %0, %1, %2, %3 offen" : "=v"(r) : "v"(voff), "s"(rs), "s"(soff) : "memory"); }
#define WAITV1(n, a) asm volatile("s_waitcnt vmcnt(%1)" : "+v"(a) : "i"(n) : "memory")
#define PIN(a) asm volatile("" : "+v"(a))
DI void st16_sc1(u32x4 v, rsrc_t rs, u32 voff, u32 soff) { __builtin_amdgcn_raw_buffer_store_b128(v, rs, voff + soff, 0, 16); }

DI bf16x8 lds16(u32 a) { return *(const LAS bf16x8*)(uintptr_t)a; }
DI u32x4 lds16u(u32 a) { return *(const LAS u32x4*)(uintptr_t)a; }
DI f32x4 lds16f(u32 a) { return *(const LAS f32x4*)(uintptr_t)a; }
DI f32x2 lds8f(u32 a) { return *(const LAS f32x2*)(uintptr_t)a; }
DI void sts16(u32 a, u32x4 v) { *(LAS u32x4*)(uintptr_t)a = v; }
DI void sts16f(u32 a, f32x4 v) { *(LAS f32x4*)(uintptr_t)a = v; }
DI void sts8f(u32 a, f32x2 v) { *(LAS f32x2*)(uintptr_t)a = v; }
DI void sts4f(u32 a, float v) { *(LAS float*)(uintptr_t)a = v; }
typedef short v4i16_t __attribute__((ext_vector_type(4)));
DI bf16x8 ldtr(u32 a) {
  v4i16_t lo = __builtin_amdgcn_ds_read_tr16_b64_v4i16((LAS v4i16_t*)(uintptr_t)a);
  v4i16_t hi = __builtin_amdgcn_ds_read_tr16_b64_v4i16((LAS v4i16_t*)(uintptr_t)(a + 512));
  return __builtin_shufflevector(lo, hi, 0, 1, 2, 3, 4, 5, 6, 7);
}

DI void poll(const GAS u32* p, bool active, u32 tag) {
#pragma unroll 1
  for (u32 spins = 0; spins < SPIN_CAP; ++spins) {
    u32 v = tag;
    if (active) v = __hip_atomic_load(p, RLX_AGENT);
    if (__all(v == tag)) break;
    __builtin_amdgcn_s_sleep(1);
  }
  asm volatile("" ::: "memory");
}
DI void post(char* ws, u32 word, bool active, u32 tag) { if (active) __hip_atomic_store((GAS u32*)ws + word, tag, RLX_AGENT); }
DI u32 mbox(int xcd, int bi) { return WS_MB / 4 + (u32)(bi * 8 + xcd) * 256; }
DI bool tags_ok(u32x4 a, u32x4 b, u32 tag) { return a[1] == tag && a[3] == tag && b[1] == tag && b[3] == tag; }
DI u32x4 gvals(u32x4 a, u32x4 b) { return (u32x4){a[0], a[2], b[0], b[2]}; }

#define STAMP(i)

DI float silu_bf(u32 cv) { const float c = bflo(cv); return rbf(c * __builtin_amdgcn_rcpf(1.f + __builtin_amdgcn_exp2f(c * -1.4426950408889634f))); }
DI void mod_part(u32 scr, u32 c0, u32 c1, u32x4 w0, u32x4 w1, int w, int lane) {
  const float s0 = silu_bf(c0), s1 = silu_bf(c1);
  float v[8];
#pragma unroll
  for (int j = 0; j < 4; ++j) {
    v[2 * j] = __builtin_fmaf(s0, bflo(w0[j]), s1 * bflo(w1[j]));
    v[2 * j + 1] = __builtin_fmaf(s0, bfhi(w0[j]), s1 * bfhi(w1[j]));
  }
#pragma unroll
  for (int j = 0; j < 4; ++j) { swap32f(v[j], v[j + 4]); v[j] += v[j + 4]; }
#pragma unroll
  for (int j = 0; j < 2; ++j) { swap16f(v[j], v[j + 2]); v[j] += v[j + 2]; }
  const bool o = lane & 8;
  float a = o ? v[1] : v[0], c = o ? v[0] : v[1];
  a += dppf<0x128>(c);
  a += dppf<0x4E>(a);
  a += dppf<0x104>(a);
  if ((lane & 6) == 0) sts4f(scr + ((lane & 1) * 8 + (lane >> 3)) * 32 + w * 4, a);
}
DI float sum8(f32x4 a, f32x4 d) { return ((a[0] + a[1]) + (a[2] + a[3])) + ((d[0] + d[1]) + (d[2] + d[3])); }
DI void mod_publish(u32 scr, char* ws, u32 col, u32 tag, int lane) {
  const u32 a = scr + (lane & 7) * 64;
  const float t0 = sum8(lds16f(a), lds16f(a + 16)), t1 = sum8(lds16f(a + 32), lds16f(a + 48));
  __hip_atomic_store((GAS u64*)(ws + WS_MODG) + (lane >> 3) * 1536 + (col >> 1) + (lane & 7), ((u64)tag << 32) | pk2(t0, t1), RLX_AGENT);
}

constexpr u64 upper_mask(int e) {
  u64 m = 0;
  for (int l = 0; l < 64; ++l) if ((e & 3) + 8 * (e >> 2) + 4 * (l >> 5) > (l & 31)) m |= 1ull << l;
  return m;
}
template <int E> DI bool upper() { constexpr u64 m = upper_mask(E); return __builtin_amdgcn_inverse_ballot_w64(m); }
#define MX3(a, b, c) __builtin_fmaxf(__builtin_fmaxf((a), (b)), (c))
#define AT_MAX(s) do { _Pragma("unroll") for (int e_ = 0; e_ < 16; e_ += 4) { m0 = MX3(m0, s[e_], s[e_ + 1]); m1 = MX3(m1, s[e_ + 2], s[e_ + 3]); } PIN(m0); PIN(m1); } while (0)
#define EX2(x) __builtin_amdgcn_exp2f(x)
template <int I> DI void sel_fill(f32x16& sel, const f32x16& sf, const f32x16& sd) {
  if constexpr (I < 16) { sel[I] = upper<I>() ? sf[I] : sd[I]; sel_fill<I + 1>(sel, sf, sd); }
}
template <bool FULL>
DI void attn_wave(int w, int lane, bf16x8 qf, const u32 (&mk)[8], u32 kb, u32 vb0, u32 vb1, u32x4& outv, u32 tv, rsrc_t rws, u32 tsrc) {
  const f32x16 zero = {};
  bf16x8 kf[9];
  float m0, m1;
  f32x16 sel, sa, sb;
  if constexpr (FULL) {
    kf[8] = lds16(kb + 8192); kf[0] = lds16(kb);
#pragma unroll
    for (int i = 1; i < 8; ++i) kf[i] = lds16(kb + i * 1024);
    SBAR();
    sa = MFMA32(kf[8], qf, zero); sb = MFMA32(kf[0], qf, zero);
    f32x16 sc = MFMA32(kf[1], qf, zero); SBAR();
    sel_fill<0>(sel, sb, sa); PIN(sel); SBAR();
    sa = MFMA32(kf[2], qf, zero); SBAR();
    m0 = __builtin_fmaxf(sel[0], sel[1]); m1 = __builtin_fmaxf(sel[2], sel[3]);
#pragma unroll
    for (int e = 4; e < 16; e += 4) { m0 = MX3(m0, sel[e], sel[e + 1]); m1 = MX3(m1, sel[e + 2], sel[e + 3]); }
    PIN(m0); PIN(m1); SBAR();
    sb = MFMA32(kf[3], qf, zero); SBAR(); AT_MAX(sc); SBAR();
    sc = MFMA32(kf[4], qf, zero); SBAR(); AT_MAX(sa); SBAR();
    sa = MFMA32(kf[5], qf, zero); SBAR(); AT_MAX(sb); SBAR();
    sb = MFMA32(kf[6], qf, zero); SBAR(); AT_MAX(sc); SBAR();
    sc = MFMA32(kf[7], qf, zero); SBAR(); AT_MAX(sa); SBAR();
    AT_MAX(sb); SBAR(); AT_MAX(sc);
  } else {
#pragma unroll
    for (int i = 1; i < 9; ++i) if (w + i >= 8) kf[i] = lds16(kb + i * 1024);
    sa = MFMA32(kf[8], qf, zero);
#pragma unroll
    for (int e = 0; e < 16; ++e) sb[e] = -INFINITY;
    sel_fill<0>(sel, sb, sa);
    m0 = __builtin_fmaxf(sel[0], sel[1]); m1 = __builtin_fmaxf(sel[2], sel[3]);
#pragma unroll
    for (int e = 4; e < 16; e += 4) { m0 = MX3(m0, sel[e], sel[e + 1]); m1 = MX3(m1, sel[e + 2], sel[e + 3]); }
#pragma unroll
    for (int i = 1; i < 8; ++i) if (w + i >= 8) { sa = MFMA32(kf[i], qf, zero); AT_MAX(sa); }
  }
  dma4(57344 + w * 256, tv, rws, tsrc);
  float m = __builtin_fmaxf(m0, m1), mo = m;
  swap32f(m, mo);
  m = __builtin_fmaxf(m, mo);
  f32x16 negm;
#pragma unroll
  for (int e = 0; e < 16; ++e) negm[e] = -m;
  f32x16 o = zero;
  u32x4 pd[2], pf[2], p0, p1;
  bf16x8 va, vc, vd, ve;
  if constexpr (FULL) {
    PIN(negm);
    va = lds16(vb0 + 8 * 1040); vc = lds16(vb1 + 8 * 1040); vd = lds16(vb0); ve = lds16(vb1);
    sa = MFMA32(kf[1], qf, negm); SBAR();
  }
#pragma unroll
  for (int e = 0; e < 8; ++e) {
    const u32 p = pk2(EX2(sel[2 * e] - m), EX2(sel[2 * e + 1] - m));
    pf[e >> 2][e & 3] = p & mk[e];
    pd[e >> 2][e & 3] = p & ~mk[e];
  }
  if constexpr (FULL) {
    PIN(pf[0]); PIN(pf[1]); PIN(pd[0]); PIN(pd[1]); SBAR();
#define AT_Q(P, S, o_) do { P[(o_) & 3] = pk2(EX2(S[2 * (o_)]), EX2(S[2 * (o_) + 1])); P[((o_) & 3) + 1] = pk2(EX2(S[2 * (o_) + 2]), EX2(S[2 * (o_) + 3])); PIN(P); SBAR(); } while (0)
#define AT_STEP(S, N, i, M0, M1, M2, M3) do { \
      if ((i) < 7) { N = MFMA32(kf[(i) < 7 ? (i) + 1 : 0], qf, negm); SBAR(); } \
      u32x4 q0_, q1_; \
      AT_Q(q0_, S, 0); M0; SBAR(); AT_Q(q0_, S, 2); M1; SBAR(); AT_Q(q1_, S, 4); M2; SBAR(); AT_Q(q1_, S, 6); M3; \
      va = lds16(vb0 + (i) * 1040); vc = lds16(vb1 + (i) * 1040); p0 = q0_; p1 = q1_; SBAR(); } while (0)
#define PV(V, P) o = MFMA32(V, BF8(P), o)
    AT_STEP(sa, sb, 1, PV(va, pd[0]), PV(vc, pd[1]), PV(vd, pf[0]), PV(ve, pf[1]));
    AT_STEP(sb, sa, 2, NOP_, PV(va, p0), PV(vc, p1), NOP_);
    AT_STEP(sa, sb, 3, NOP_, PV(va, p0), PV(vc, p1), NOP_);
    AT_STEP(sb, sa, 4, NOP_, PV(va, p0), PV(vc, p1), NOP_);
    AT_STEP(sa, sb, 5, NOP_, PV(va, p0), PV(vc, p1), NOP_);
    AT_STEP(sb, sa, 6, NOP_, PV(va, p0), PV(vc, p1), NOP_);
    AT_STEP(sa, sb, 7, NOP_, PV(va, p0), PV(vc, p1), NOP_);
    PV(va, p0); PV(vc, p1);
  } else {
    o = MFMA32(lds16(vb0 + 8 * 1040), BF8(pd[0]), o);
    o = MFMA32(lds16(vb1 + 8 * 1040), BF8(pd[1]), o);
#pragma unroll
    for (int i = 1; i < 8; ++i) if (w + i >= 8) {
      sa = MFMA32(kf[i], qf, negm);
#pragma unroll
      for (int e = 0; e < 4; ++e) {
        p0[e] = pk2(EX2(sa[2 * e]), EX2(sa[2 * e + 1]));
        p1[e] = pk2(EX2(sa[8 + 2 * e]), EX2(sa[8 + 2 * e + 1]));
      }
      o = MFMA32(lds16(vb0 + i * 1040), BF8(p0), o);
      o = MFMA32(lds16(vb1 + i * 1040), BF8(p1), o);
    }
  }
  const float rl = __builtin_amdgcn_rcpf(o[8]);
  u32 a0 = pk2(o[0] * rl, o[1] * rl), a1 = pk2(o[2] * rl, o[3] * rl), b0 = pk2(o[4] * rl, o[5] * rl), b1 = pk2(o[6] * rl, o[7] * rl);
  swap32u(a0, b0); swap32u(a1, b1);
  outv = (u32x4){a0, a1, b0, b1};
}

DI u32x4 resid_epi(float (&y)[8], u32x4 res, u32x4 gate) {
#pragma unroll
  for (int i = 0; i < 4; ++i) swap32f(y[i], y[4 + i]);
  u32x4 o;
#pragma unroll
  for (int i = 0; i < 4; ++i) {
    const u32 yb = pk2(y[2 * i], y[2 * i + 1]);
    const u32 tb = pk2(bflo(yb) * bflo(gate[i]), bfhi(yb) * bfhi(gate[i]));
    o[i] = pk2(bflo(res[i]) + bflo(tb), bfhi(res[i]) + bfhi(tb));
  }
  return o;
}

#define WD_DMA(base) dma((u32)((base) + w) * 1024, pv, rwd, (u32)(((base) / 40) * 448 + ((base) % 40) * 8 + w * 8) * 1024)

template <int NF>
DI void ffn_up(int w, int lane, u32 tag, char* ws, rsrc_t rws, rsrc_t rwd, u32 pv, int m128, int nt, u32 tdv, int b) {
  constexpr int NW = NF;
  constexpr int F0 = NF == 4 ? 0 : 4;
  const int p = w & 3, tile = 4 * m128 + p;
  const u32 lv = lane * 16;
  const GAS u32* mb = (const GAS u32*)(ws + WS_MB) + b * 256;
  const u32 wsrc = WS_WP + (u32)nt * (33 * 7168) + w * 1024;
  const u32 xsrc = WS_X1 + (u32)(tile * 32 + 2 * (w >> 2)) * 1024;
  const u32 xdst = 114688 + (p * 4 + 2 * (w >> 2)) * 1024;
#define UP_W1(g, i) dma_sc1(((g) & 3) * 28672 + w * 1024 + (i) * 8192, lv, rws, wsrc + (g) * 28672 + (i) * 8192)
#define UP_X1(g, i) dma_sc1(xdst + ((g) % 3) * 16384 + (i) * 1024, lv, rws, xsrc + (g) * 4096 + (i) * 1024)
#pragma unroll
  for (int g = 1; g < 4; ++g)
#pragma unroll
    for (int i = 0; i < NW; ++i) UP_W1(g, i);
  STAMP(12);
  poll(mb + MB_X1 + lane, lane < 16 ? (lane >> 3) == (p >> 1) : lane < 30, tag);
  STAMP(13);
#pragma unroll
  for (int g = 0; g < 3; ++g) { UP_X1(g, 0); UP_X1(g, 1); }
  f32x16 acc[NF];
#pragma unroll
  for (int f = 0; f < NF; ++f) acc[f] = (f32x16){};
  u32 wl = lv + F0 * 1024, wh = lv + F0 * 1024 + 57344, xl = lv + 114688 + p * 4096;
  PIN(wl); PIN(wh); PIN(xl);
  bf16x8 xa, wa[NF], xb, wb[NF];
  u32x2 st[4];
  const u32 stv = (u32)(lane >> 5) * 1024 + (lane & 31) * 8;
#define UP_ST(i) ld8_sc1(st[i], stv, rws, WS_ST + (u32)tile * 2048 + (i) * 256)
#define UP_RX(X, ks) X = lds16(xl + (((ks) >> 2) % 3) * 16384 + ((ks) & 3) * 1024)
#define UP_RW(W, f, ks) W[f] = lds16(((((ks) >> 2) & 2) ? wh : wl) + (((ks) >> 2) & 1) * 28672 + (((ks) & 3) * 7 + (f)) * 1024)
#define UP_STEP(XC, WC, XN, WN, ks, RD, MM, D0, D1) do { \
    if (RD) { UP_RX(XN, ks); UP_RW(WN, 0, ks); } SBAR(); if (MM) acc[0] = MFMA32(WC[0], XC, acc[0]); SBAR(); \
    if (RD) { UP_RW(WN, 1, ks); UP_RW(WN, 2, ks); } SBAR(); if (MM) acc[1] = MFMA32(WC[1], XC, acc[1]); SBAR(); \
    if (RD && NF == 4) UP_RW(WN, NF - 1, ks); D0; SBAR(); if (MM) acc[2] = MFMA32(WC[2], XC, acc[2]); SBAR(); \
    D1; if (MM && NF == 4) acc[NF - 1] = MFMA32(WC[NF - 1], XC, acc[NF - 1]); SBAR(); } while (0)
#define UP_WL(g) do { if (NF == 4) UP_W1(g, 3); } while (0)
#define UP_GROUP(g, MM0, A0, A1, B0, B1, C0, C1, E0, E1) do { \
    UP_STEP(xb, wb, xa, wa, 4 * (g), true, MM0, A0, A1); UP_STEP(xa, wa, xb, wb, 4 * (g) + 1, true, true, B0, B1); \
    UP_STEP(xb, wb, xa, wa, 4 * (g) + 2, true, true, C0, C1); UP_STEP(xa, wa, xb, wb, 4 * (g) + 3, true, true, E0, E1); } while (0)
#define UP_GREFILL(g) UP_GROUP(g, true, UP_X1((g) + 2, 0), UP_X1((g) + 2, 1), UP_W1((g) + 3, 0), NOP_, UP_W1((g) + 3, 1), UP_W1((g) + 3, 2), UP_WL((g) + 3), NOP_)
  u32 td;
  WAITBAR(4); UP_GROUP(0, false, NOP_, NOP_, UP_ST(0), UP_ST(1), UP_ST(2), UP_ST(3), ld4(td, tdv, rwd, 0), NOP_);
  WAITBAR(7); UP_GREFILL(1);
  WAITBAR(NW + 7); UP_GREFILL(2);
  WAITBAR(2 * NW + 2); UP_GREFILL(3);
  WAITBAR(2 * NW + 2); UP_GREFILL(4);
  WAITBAR(2 * NW + 2);
  UP_GROUP(5, true, UP_X1(7, 0), UP_X1(7, 1),
           dma_sc1(16384 + (w < 7 ? w : 6) * 1024, lv, rws, WS_WP + (u32)nt * (33 * 7168) + 32 * 7168 + (w < 7 ? w : 6) * 1024), NOP_,
           WD_DMA(0), NOP_, WD_DMA(8), NOP_);
  WAITBAR(NW + 5);
  asm volatile("" : "+v"(st[0]), "+v"(st[1]), "+v"(st[2]), "+v"(st[3]), "+v"(td));
  float s1 = (__uint_as_float(st[0][0]) + __uint_as_float(st[1][0])) + (__uint_as_float(st[2][0]) + __uint_as_float(st[3][0]));
  float s2 = (__uint_as_float(st[0][1]) + __uint_as_float(st[1][1])) + (__uint_as_float(st[2][1]) + __uint_as_float(st[3][1]));
  { float t1 = s1, t2 = s2; swap32f(s1, t1); swap32f(s2, t2); s1 += t1; s2 += t2; }
  const float mu = s1 * (1.f / 512.f);
  const float var = __builtin_fmaxf(s2 * (1.f / 512.f) - mu * mu, 0.f) + 1e-5f;
  float rstd = __builtin_amdgcn_rsqf(var);
  const float sig = var * rstd;
  const float mh = rbf(-mu), ml = -mu - mh, sh = rbf(sig), sl = sig - sh;
  u32x4 ev = {pk2(mh, mh), pk2(ml, sh), pk2(sh, sl), 0u};
  if (lane >= 32) ev = (u32x4){0u, 0u, 0u, 0u};
  PIN(ev); PIN(rstd); SBAR();
  UP_GROUP(6, true, WD_DMA(24), WD_DMA(32), WD_DMA(40), NOP_, WD_DMA(48), NOP_, WD_DMA(144), NOP_);
  WAITBAR(7);
  UP_GROUP(7, true, WD_DMA(56), WD_DMA(64), WD_DMA(72), NOP_, WD_DMA(112), NOP_, WD_DMA(120), NOP_);
#pragma unroll
  for (int f = 0; f < NF; ++f) wa[f] = lds16(wl + 16384 + f * 1024);
  UP_STEP(xb, wb, xa, wa, 0, false, true, NOP_, NOP_);
  LBAR();
  STAMP(14);
#pragma unroll
  for (int f = 0; f < NF; ++f) acc[f] = MFMA32(wa[f], BF8(ev), acc[f]);
  WD_DMA(80); WD_DMA(88);
  SBAR();
  constexpr float C1 = -2.f * 0.7978845608028654f * 1.4426950408889634f, C2 = C1 * 0.044715f;
#pragma unroll
  for (int f = 0; f < NF; ++f) {
    float a[8];
#pragma unroll
    for (int e = 0; e < 8; ++e) {
      const float g = acc[f][e] * rstd, u = acc[f][8 + e] * rstd;
      const float z = g * __builtin_fmaf(g * g, C2, C1);
      a[e] = g * u * __builtin_amdgcn_rcpf(1.f + __builtin_amdgcn_exp2f(z));
    }
    u32 a0 = pk2(a[0], a[1]), a1 = pk2(a[2], a[3]), b0 = pk2(a[4], a[5]), b1 = pk2(a[6], a[7]);
    swap32u(a0, b0); swap32u(a1, b1);
    st16_sc1((u32x4){a0, a1, b0, b1}, rws, lv, WS_ACT + (u32)(tile * 112 + nt * 7 + F0 + f) * 1024);
    if (f == 0) { WD_DMA(96); WD_DMA(104); }
    if (f == 1) { WD_DMA(128); WD_DMA(136); }
    if (f == 2) { WD_DMA(16); WD_DMA(152); }
    SBAR();
  }
}

__global__ void __launch_bounds__(512, 2)
fused_block(const void* __restrict__ px, const void* __restrict__ pwqkv, const void* __restrict__ pwo, const void* __restrict__ pwg,
            const void* __restrict__ pwu, const void* __restrict__ pwd, const void* __restrict__ pc, const void* __restrict__ pwm,
            void* pout, char* ws) {
  __shared__ __attribute__((aligned(16))) char smem[163840];
  asm volatile("" ::"v"((u32)(uintptr_t)(LAS char*)smem));
  const int tid = threadIdx.x, lane = tid & 63, w = __builtin_amdgcn_readfirstlane(tid >> 6);
  const int b = blockIdx.x, xcd = b & 7, bi = b >> 3;
  u32 seed;
  asm volatile("s_load_dword %0, %1, 0x0" : "=s"(seed) : "s"(ws) : "memory");
  const rsrc_t rx = __builtin_amdgcn_make_buffer_rsrc((void*)px, 0, 2048 * 512 * 2, 0x00020000);
  const rsrc_t rqkv = __builtin_amdgcn_make_buffer_rsrc((void*)pwqkv, 0, 512 * 1536 * 2, 0x00020000);
  const rsrc_t rwo = __builtin_amdgcn_make_buffer_rsrc((void*)pwo, 0, 512 * 512 * 2, 0x00020000);
  const rsrc_t rwd = __builtin_amdgcn_make_buffer_rsrc((void*)pwd, 0, 1792 * 512 * 2, 0x00020000);
  const rsrc_t rout = __builtin_amdgcn_make_buffer_rsrc(pout, 0, 2048 * 512 * 2, 0x00020000);
  const rsrc_t rws = __builtin_amdgcn_make_buffer_rsrc(ws, 0, WS_BYTES, 0x00020000);
  const u32 lv = lane * 16;
  const GAS u32* mb = (const GAS u32*)(ws + WS_MB) + b * 256;
  const u32 modg = WS_MODG + (u32)xcd * 12288;
  STAMP(0);
  if (w >= 6) {
    const u32 dl = (u32)w * 8192;
    if (w == 6) {
      dma4(dl, lane * 524288u, rws, 0);
      dma4(dl, lane * 65536u, rwd, 0);
      dma4(dl, lane * 65536u, rout, 0);
    } else {
      dma4(dl, lane * 65536u, rwo, 0);
      dma4(dl, lane * 65536u, __builtin_amdgcn_make_buffer_rsrc((void*)pwg, 0, 512 * 1792 * 2, 0x00020000), 0);
      dma4(dl, lane * 65536u, __builtin_amdgcn_make_buffer_rsrc((void*)pwu, 0, 512 * 1792 * 2, 0x00020000), 0);
    }
  }

  const int m64a = 8 * (xcd & 3) + (bi & 7), grp = 4 * (xcd >> 2) + (bi >> 3);
  const int mrole = bi & 3;
  const bool late = mrole == 1 || mrole == 2;
  const u32 mcol = mrole == 0 ? (u32)(xcd * 8 + (bi >> 2)) * 16 : 1024 + (u32)(xcd * 16 + (bi >> 2) * 2 + mrole - 1) * 16;
  constexpr u32 SCR = 155648;
#define MOD_LD(on, c0, c1, wm0, wm1) do { const rsrc_t rc_ = __builtin_amdgcn_make_buffer_rsrc((void*)pc, 0, (on) ? 512 * 2 : 0, 0x00020000); \
    const rsrc_t rm_ = __builtin_amdgcn_make_buffer_rsrc((void*)pwm, 0, (on) ? 512 * 3072 * 2 : 0, 0x00020000); \
    ld16(wm0, (u32)(tid >> 1) * 6144 + (tid & 1) * 16, rm_, mcol * 2); ld16(wm1, (u32)(tid >> 1) * 6144 + (tid & 1) * 16, rm_, mcol * 2 + 256 * 6144); \
    ld2(c0, (tid >> 1) * 2, rc_, 0); ld2(c1, (tid >> 1) * 2, rc_, 512); } while (0)
  u32 tag;
  if (mrole == 0) {
    u32 c0, c1; u32x4 wm0, wm1;
    MOD_LD(true, c0, c1, wm0, wm1);
    asm volatile("s_waitcnt vmcnt(0) lgkmcnt(0)" : "+v"(c0), "+v"(c1), "+v"(wm0), "+v"(wm1), "+s"(seed)::"memory");
    mod_part(SCR, c0, c1, wm0, wm1, w, lane);
    LBAR();
    if (w == 0) mod_publish(SCR, ws, mcol, seed + 1u, lane);
  }
  STAMP(1);
  u32x4 xr[8];
#pragma unroll
  for (int j = 0; j < 8; ++j) ld16(xr[j], lv, rx, (u32)(m64a * 64 + w * 8 + j) * 1024);
  const int wid = xcd * 28 + bi;
  const int wmat = wid >= 112, wcg = wid - wmat * 112;
  u32 wr[8];
#define WR_LD() do { const rsrc_t rg_ = __builtin_amdgcn_make_buffer_rsrc((void*)(wmat ? pwu : pwg), 0, bi < 28 ? 512 * 1792 * 2 : 0, 0x00020000); \
    _Pragma("unroll") for (int j = 0; j < 8; ++j) ld4(wr[j], (u32)(tid >> 3) * (8 * 3584) + (tid & 7) * 4, rg_, (u32)j * 3584 + (u32)wcg * 32); } while (0)
  const u32 qv = (u32)(w * 8 + (lane >> 3)) * 3072 + (((lane & 7) ^ ((((lane >> 4) & 1) | ((w & 1) << 1)) << 1)) * 16) + grp * 128;
#define QKV_DMA(s, seg) dma(65536 + ((s) & 3) * 24576 + (seg) * 8192 + w * 1024, qv, rqkv, (s) * 64 * 3072 + (seg) * 1024)
  if (w != 0) {
#pragma unroll
    for (int s = 0; s < 4; ++s) { QKV_DMA(s, 0); QKV_DMA(s, 1); if (s < 3) QKV_DMA(s, 2); }
    if (w == 1) {
      const u32 q0 = (u32)(lane >> 3) * 3072 + (((lane & 7) ^ (((lane >> 4) & 1) << 1)) * 16) + grp * 128;
#pragma unroll
      for (int s = 0; s < 4; ++s)
#pragma unroll
        for (int sg = 0; sg < 3; ++sg) if (s < 3 || sg < 2) dma(65536 + s * 24576 + sg * 8192, q0, rqkv, s * 64 * 3072 + sg * 1024);
    }
  }
  u32 l0, l1; u32x4 lm0, lm1;
  MOD_LD(late, l0, l1, lm0, lm1); WR_LD();
  if (w == 0) WAITV(12); else if (w == 1) WAITV(34); else WAITV(23);
  asm volatile("s_waitcnt lgkmcnt(0)" : "+s"(seed)::"memory");
  tag = seed + 1u;
  float xn[8][8];
  {
    asm volatile("" : "+v"(xr[0]), "+v"(xr[1]), "+v"(xr[2]), "+v"(xr[3]), "+v"(xr[4]), "+v"(xr[5]), "+v"(xr[6]), "+v"(xr[7])::"memory");
    float S[8], Q[8];
#pragma unroll
    for (int j = 0; j < 8; ++j) {
#pragma unroll
      for (int e = 0; e < 4; ++e) { xn[j][2 * e] = bflo(xr[j][e]); xn[j][2 * e + 1] = bfhi(xr[j][e]); }
      float q = xn[j][0] * xn[j][0];
#pragma unroll
      for (int e = 1; e < 8; ++e) q = __builtin_fmaf(xn[j][e], xn[j][e], q);
      S[j] = ((xn[j][0] + xn[j][1]) + (xn[j][2] + xn[j][3])) + ((xn[j][4] + xn[j][5]) + (xn[j][6] + xn[j][7])); Q[j] = q;
    }
#pragma unroll
    for (int j = 0; j < 4; ++j) { swap32f(S[j], S[j + 4]); S[j] += S[j + 4]; swap32f(Q[j], Q[j + 4]); Q[j] += Q[j + 4]; }
#pragma unroll
    for (int j = 0; j < 2; ++j) { swap16f(S[j], S[j + 2]); S[j] += S[j + 2]; swap16f(Q[j], Q[j + 2]); Q[j] += Q[j + 2]; }
    float ra[2], rb[2];
#pragma unroll
    for (int j = 0; j < 2; ++j) {
      const float mu = row16_sum(S[j]) * (1.f / 512.f);
      const float var = __builtin_fmaxf(row16_sum(Q[j]) * (1.f / 512.f) - mu * mu, 0.f) + 1e-5f;
      ra[j] = __builtin_amdgcn_rsqf(var); rb[j] = -mu * ra[j];
    }
#pragma unroll
    for (int j = 0; j < 8; ++j) {
      const float a = rdlane(ra[j & 1], (j >> 1) * 16), c = rdlane(rb[j & 1], (j >> 1) * 16);
#pragma unroll
      for (int e = 0; e < 8; ++e) { xn[j][e] = __builtin_fmaf(xn[j][e], a, c); PIN(xn[j][e]); }
    }
  }
  if (w == 0) {
    u32x4 a0, a1, a2, a3;
#pragma unroll 1
    for (u32 spins = 0; spins < SPIN_CAP; ++spins) {
      ld16_sc1(a0, lane * 32, rws, modg); ld16_sc1(a1, lane * 32 + 16, rws, modg);
      ld16_sc1(a2, lane * 32, rws, modg + 2048); ld16_sc1(a3, lane * 32 + 16, rws, modg + 2048);
      asm volatile("s_waitcnt vmcnt(0)" : "+v"(a0), "+v"(a1), "+v"(a2), "+v"(a3)::"memory");
      if (__all(tags_ok(a0, a1, tag) && tags_ok(a2, a3, tag))) break;
      __builtin_amdgcn_s_sleep(1);
    }
    sts16(SCR + 6144 + lv, gvals(a0, a1)); sts16(SCR + 7168 + lv, gvals(a2, a3));
  } else {
    if (w <= 3) dma4(SCR + w * 1024, (u32)(256 + 32 * (bi & 7) + (lane & 31)) * 3072 + grp * 128, rqkv, (w - 1) * 1024);
    else if (w <= 5) dma4(SCR + w * 1024, (u32)(16 * bi + 8 * (w - 4) + (lane >> 3)) * 1024 + (lane & 7) * 128, rwo, 0);
  }
  LBAR();
  STAMP(2);
  asm volatile("s_waitcnt vmcnt(8)" : "+v"(l0), "+v"(l1), "+v"(lm0), "+v"(lm1)::"memory");
  {
    const u32x4 sa = lds16u(SCR + 6144 + lv), ca = lds16u(SCR + 7168 + lv);
    float fs[8], fa[8];
#pragma unroll
    for (int e = 0; e < 4; ++e) {
      const u32 t = pk2(1.f + bflo(ca[e]), 1.f + bfhi(ca[e]));
      fs[2 * e] = bflo(t); fs[2 * e + 1] = bfhi(t); fa[2 * e] = bflo(sa[e]); fa[2 * e + 1] = bfhi(sa[e]);
    }
#pragma unroll
    for (int j = 0; j < 8; ++j) {
      u32x4 h;
#pragma unroll
      for (int e = 0; e < 4; ++e) h[e] = pk2(__builtin_fmaf(xn[j][2 * e], fs[2 * e], fa[2 * e]), __builtin_fmaf(xn[j][2 * e + 1], fs[2 * e + 1], fa[2 * e + 1]));
      const int r = w * 8 + j;
      sts16((u32)r * 1024 + ((lane ^ (r & 15)) * 16), h);
    }
  }
  if (late) mod_part(SCR, l0, l1, lm0, lm1, w, lane);
  const int n64 = bi >> 2, m64 = 4 * xcd + (bi & 3);
  const u32 pv = (u32)(lane >> 3) * 1024 + (((lane & 7) ^ (((lane >> 4) & 1) << 2)) * 16) + n64 * 128;
  {
    const int wm = w >> 2, hn = w & 3, fr = lane & 15, g = lane >> 4;
    u32 ab[4];
#pragma unroll
    for (int t = 0; t < 4; ++t) { ab[t] = (u32)(wm * 32 + fr) * 1024 + ((g ^ (fr & 3)) * 16) + ((t ^ (fr >> 2)) * 64); PIN(ab[t]); }
    u32 bl = 65536 + (u32)(8 * g + (fr >> 2)) * 128 + ((hn ^ (((fr >> 3) & 1) | ((g & 1) << 1))) * 32) + (lane & 3) * 8, bh = bl + 49152;
    PIN(bl); PIN(bh);
    f32x4 acc[2][3] = {};
    bf16x8 fa[2][2][2], fb[2][2][3];
#define Q_RA(S, s, kk, m) fa[S][kk][m] = lds16(ab[(2 * (s) + (kk)) & 3] + ((2 * (s) + (kk)) >> 2) * 256 + (m) * 16384)
#define Q_RB(S, s, kk, sg) fb[S][kk][sg] = ldtr((((s) & 2) ? bh : bl) + ((s) & 1) * 24576 + (sg) * 8192 + (kk) * 4096)
#define Q_M(S, kk, m, sg) acc[m][sg] = (sg) < 2 ? MFMA16(fb[S][kk][sg], fa[S][kk][m], acc[m][sg]) : MFMA16(fa[S][kk][m], fb[S][kk][sg], acc[m][sg])
#define Q_STAGE(C, N, s, RD, D0, D1, D2) do { \
      if (RD) { Q_RA(N, s, 0, 0); Q_RA(N, s, 0, 1); } Q_M(C, 0, 0, 0); SBAR(); \
      if (RD) Q_RB(N, s, 0, 0); Q_M(C, 0, 0, 1); SBAR(); \
      if (RD) Q_RB(N, s, 0, 1); Q_M(C, 0, 0, 2); SBAR(); \
      if (RD) Q_RB(N, s, 0, 2); Q_M(C, 0, 1, 0); SBAR(); \
      if (RD) { Q_RA(N, s, 1, 0); Q_RA(N, s, 1, 1); } Q_M(C, 0, 1, 1); SBAR(); \
      if (RD) Q_RB(N, s, 1, 0); Q_M(C, 0, 1, 2); SBAR(); \
      if (RD) Q_RB(N, s, 1, 1); Q_M(C, 1, 0, 0); SBAR(); \
      if (RD) Q_RB(N, s, 1, 2); Q_M(C, 1, 0, 1); SBAR(); \
      D0; Q_M(C, 1, 0, 2); SBAR(); D1; Q_M(C, 1, 1, 0); SBAR(); D2; Q_M(C, 1, 1, 1); SBAR(); Q_M(C, 1, 1, 2); SBAR(); } while (0)
#define WO_DMA(i) dma(65536 + ((i) * 8 + w) * 1024, pv, rwo, (u32)((i) * 8 + w) * 8192)
    LBAR();
    if (late && w == 0) { mod_publish(SCR, ws, mcol, tag, lane); asm volatile("s_waitcnt lgkmcnt(0)" ::: "memory"); }
    QKV_DMA(3, 2);
#pragma unroll
    for (int kk = 0; kk < 2; ++kk) { Q_RA(0, 0, kk, 0); Q_RA(0, 0, kk, 1); Q_RB(0, 0, kk, 0); Q_RB(0, 0, kk, 1); Q_RB(0, 0, kk, 2); }
    STAMP(20);
    LBAR(); Q_STAGE(0, 1, 1, true, QKV_DMA(4, 0), QKV_DMA(4, 1), QKV_DMA(4, 2));
    LBAR(); Q_STAGE(1, 0, 2, true, QKV_DMA(5, 0), QKV_DMA(5, 1), QKV_DMA(5, 2));
    WAITBAR(6); Q_STAGE(0, 1, 3, true, QKV_DMA(6, 0), QKV_DMA(6, 1), QKV_DMA(6, 2));
    WAITBAR(6); Q_STAGE(1, 0, 4, true, QKV_DMA(7, 0), QKV_DMA(7, 1), QKV_DMA(7, 2));
    WAITBAR(6); Q_STAGE(0, 1, 5, true, WO_DMA(0), WO_DMA(1), WO_DMA(2));
    WAITBAR(6); Q_STAGE(1, 0, 6, true, WO_DMA(3), WO_DMA(4), WO_DMA(5));
    WAITBAR(6); Q_STAGE(0, 1, 7, true, WO_DMA(6), WO_DMA(7), NOP_);
    Q_STAGE(1, 0, 0, false, NOP_, NOP_, NOP_);
    STAMP(3);
    constexpr float QS = 0.25f * 1.4426950408889634f;
    const int head = 4 * grp + hn, tile = 2 * m64a + wm;
    const u32 dst = (u32)(head * 64 + tile) * 1024;
    {
      u32 p0 = pk2(acc[0][0][0] * QS, acc[0][0][1] * QS), p1 = pk2(acc[0][0][2] * QS, acc[0][0][3] * QS);
      u32 r0 = pk2(acc[1][0][0] * QS, acc[1][0][1] * QS), r1 = pk2(acc[1][0][2] * QS, acc[1][0][3] * QS);
      swap16u(p0, r0); swap16u(p1, r1);
      st16_sc1((u32x4){p0, p1, r0, r1}, rws, lv, WS_Q + dst);
    }
    {
      u32 p0 = pk2(acc[0][1][0], acc[0][1][1]), p1 = pk2(acc[0][1][2], acc[0][1][3]);
      u32 r0 = pk2(acc[1][1][0], acc[1][1][1]), r1 = pk2(acc[1][1][2], acc[1][1][3]);
      swap16u(p0, r0); swap16u(p1, r1);
      st16_sc1((u32x4){p0, p1, r0, r1}, rws, lv, WS_K + dst);
    }
    {
      u32 p0 = pk2(acc[0][2][0], acc[0][2][1]), p1 = pk2(acc[0][2][2], acc[0][2][3]);
      u32 r0 = pk2(acc[1][2][0], acc[1][2][1]), r1 = pk2(acc[1][2][2], acc[1][2][3]);
      swap32u(p0, r0); swap32u(p1, r1);
      st16_sc1((u32x4){p0, p1, r0, r1}, rws, lv, WS_V + dst);
    }
  }
  const int mh = w & 1, ksl = w >> 1;
  const u32 ecol = (u32)n64 * 64 + ksl * 16 + (lane >> 5) * 8;
  const u32 erow = (u32)m64 * 64 + mh * 32 + (lane & 31);
  const int trh = lane >> 5, trb = (lane >> 4) & 1, trq = (lane & 15) >> 2;
  u32 tb[2];
#pragma unroll
  for (int c = 0; c < 2; ++c) tb[c] = (u32)(8 * trh + trq) * 128 + ((((2 * c + trb + ksl) & 3) ^ (trq & 2)) * 32) + (lane & 3) * 8;
  u32x4 xres, gav, gbv, x1v;
  const int m128 = 4 * (xcd >> 1) + (bi & 3), nt = 8 * (xcd & 1) + (bi >> 2);
  constexpr u32 SC2 = 33280, SC3 = 34304;
  const int wn = wcg / 7, wfr = wcg - wn * 7;
  const u32 wdst = WS_WP + (u32)wn * (33 * 7168) + wfr * 1024;
  const u32 wmb = mbox(((lane >> 2) << 1) | (wn >> 3), ((wn & 7) << 2) | (lane & 3)) + wfr * 2 + wmat;
  {
    u32x4 g0, g1, g2, g3, g4, g5, g6, g7;
    const u32 kv = (u32)(tid >> 3) * 32, ev = ecol * 4;
#define G_LD() do { ld16_sc1(g0, kv, rws, modg + 6144); ld16_sc1(g1, kv + 16, rws, modg + 6144); ld16_sc1(g2, kv, rws, modg + 8192); ld16_sc1(g3, kv + 16, rws, modg + 8192); \
      ld16_sc1(g4, ev, rws, modg + 4096); ld16_sc1(g5, ev + 16, rws, modg + 4096); ld16_sc1(g6, ev, rws, modg + 10240); ld16_sc1(g7, ev + 16, rws, modg + 10240); } while (0)
#define G_WAIT() asm volatile("s_waitcnt vmcnt(0)" : "+v"(g0), "+v"(g1), "+v"(g2), "+v"(g3), "+v"(g4), "+v"(g5), "+v"(g6), "+v"(g7), "+v"(xres)::"memory")
    G_LD();
    ld16(xres, erow * 1024 + ecol * 2, rx, 0);
    asm volatile("s_waitcnt vmcnt(9) lgkmcnt(0)\n\ts_barrier"
                 : "+v"(wr[0]), "+v"(wr[1]), "+v"(wr[2]), "+v"(wr[3]), "+v"(wr[4]), "+v"(wr[5]), "+v"(wr[6]), "+v"(wr[7])::"memory");
    if (w == 0) {
      const int hd = 4 * grp + (lane & 3), rg = (m64a >> 2) + ((lane >> 2) & 1);
      post(ws, lane < 8 ? mbox(hd >> 2, (rg << 2) | (hd & 3)) + MB_QKV + (m64a & 3) + (lane < 4 ? 4 : 0) : WS_ARR / 4 + b, lane < 4 || (lane < 8 && rg < 8) || lane == 8, tag);
    }
    STAMP(4);
    if (tid < 16) sts16(16384 + tid * 1040 + 1024, (u32x4){0x3f803f80u, 0x3f803f80u, 0x3f803f80u, 0x3f803f80u});
    G_WAIT();
#pragma unroll 1
    for (u32 spins = 0; spins < SPIN_CAP; ++spins) {
      if (__all(tags_ok(g0, g1, tag) && tags_ok(g2, g3, tag) && tags_ok(g4, g5, tag) && tags_ok(g6, g7, tag))) break;
      __builtin_amdgcn_s_sleep(1);
      G_LD(); G_WAIT();
    }
    gav = gvals(g4, g5); gbv = gvals(g6, g7);
    if (bi < 28) {
      const u32x4 sb = gvals(g0, g1), cb = gvals(g2, g3);
      float u0 = 0.f, u1 = 0.f, b0 = 0.f, b1 = 0.f;
      u32x4 e0, e1;
#pragma unroll
      for (int e = 0; e < 4; ++e) {
        const u32 t = pk2(1.f + bflo(cb[e]), 1.f + bfhi(cb[e]));
        const float sl = bflo(t), sh = bfhi(t);
        e0[e] = pk2(bflo(wr[2 * e]) * sl, bflo(wr[2 * e + 1]) * sh);
        e1[e] = pk2(bfhi(wr[2 * e]) * sl, bfhi(wr[2 * e + 1]) * sh);
        u0 += bflo(e0[e]) + bfhi(e0[e]); u1 += bflo(e1[e]) + bfhi(e1[e]);
        b0 = __builtin_fmaf(bflo(wr[2 * e]), bflo(sb[e]), __builtin_fmaf(bflo(wr[2 * e + 1]), bfhi(sb[e]), b0));
        b1 = __builtin_fmaf(bfhi(wr[2 * e]), bflo(sb[e]), __builtin_fmaf(bfhi(wr[2 * e + 1]), bfhi(sb[e]), b1));
      }
      const u32 tw = 40960 + w * 2048;
      sts16(tw + lane * 32, e0); sts16(tw + lane * 32 + 16, e1);
      swap32f(u0, b0); u0 += b0;
      swap32f(u1, b1); u1 += b1;
      swap16f(u0, u1); u0 += u1;
      u0 += dppf<0x128>(u0);
      if ((lane & 8) == 0) sts4f(SC2 + (u32)((lane >> 5) * 16 + (lane & 7) * 2 + ((lane >> 4) & 1)) * 32 + w * 4, u0);
      asm volatile("s_waitcnt lgkmcnt(0)" ::: "memory");
      e0 = lds16u(tw + lv); e1 = lds16u(tw + 1024 + lv);
      const u32 vo = (u32)(w * 4 + (lane >> 5)) * 7168 + (u32)(((lane >> 4) & 1) * 32 + wmat * 16 + (lane & 15)) * 16;
      st16_sc1(e0, rws, vo, wdst);
      st16_sc1(e1, rws, vo + 2 * 7168, wdst);
    }
  }
  STAMP(5);
  {
    const int head = 4 * xcd + (bi & 3), range = bi >> 2, t0 = 8 * range - 8;
    u32 mk[8];
#pragma unroll
    for (int e = 0; e < 8; ++e) {
      const int k0 = ((2 * e) & 3) + 8 * ((2 * e) >> 2) + 4 * (lane >> 5);
      mk[e] = (k0 > (lane & 31) ? 0xffffu : 0u) | (k0 + 1 > (lane & 31) ? 0xffff0000u : 0u);
      PIN(mk[e]);
    }
    u32 kb = (u32)(w * 1024 + lane * 16);
    u32 vb0 = 16384 + w * 1040 + ((lane & 31) < 16 ? (lane >> 5) * 256 + (lane & 31) * 16 : 1024);
    u32 vb1 = 16384 + w * 1040 + ((lane & 31) < 16 ? 512 + (lane >> 5) * 256 + (lane & 31) * 16 : 1024);
    PIN(kb); PIN(vb0); PIN(vb1);
    poll(mb + MB_QKV + (w >> 1) + (lane & 1) * 4, lane < 2 && (range > 0 || lane == 1), tag);
    STAMP(6);
    u32x4 q;
    const u32 src = (u32)(head * 64 + t0 + w) * 1024;
    dma_sc1((w + 8) * 1024, lv, rws, WS_K + src + 8192);
    ld16_sc1(q, lv, rws, WS_Q + src + 8192);
    if (range > 0) dma_sc1(w * 1024, lv, rws, WS_K + src);
    dma_sc1(16384 + (w + 8) * 1040, lv, rws, WS_V + src + 8192);
    if (range > 0) dma_sc1(16384 + w * 1040, lv, rws, WS_V + src);
    asm volatile("s_waitcnt vmcnt(0) lgkmcnt(0)\n\ts_barrier" : "+v"(q)::"memory");
    STAMP(7);
    if (bi < 28 && w == 7) {
      post(ws, wmb + MB_W, lane < 16, tag);
      float t = sum8(lds16f(SC2 + (lane & 31) * 32), lds16f(SC2 + (lane & 31) * 32 + 16));
      float o = t;
      swap16f(t, o);
      const float uh = rbf(t), ul = t - uh, bh = rbf(o), bl = o - bh;
      u32x4 efr = {pk2(uh, ul), pk2(uh, bh), pk2(bl, bh), 0u};
      if (lane >= 32) efr = (u32x4){0u, 0u, 0u, 0u};
      if ((lane & 16) == 0) st16_sc1(efr, rws, (u32)((lane >> 5) * 32 + wmat * 16 + (lane & 15)) * 16, wdst + 32 * 7168);
    }
    u32x4 ov;
    const u32 tv = (u32)(w * 58 + (lane < 58 ? lane : 57)) * 128, tsrc = WS_WP + (u32)nt * (33 * 7168) + (u32)(bi & 3) * (462 * 128);
    if (range > 0) attn_wave<true>(w, lane, BF8(q), mk, kb, vb0, vb1, ov, tv, rws, tsrc);
    else attn_wave<false>(w, lane, BF8(q), mk, kb, vb0, vb1, ov, tv, rws, tsrc);
    st16_sc1(ov, rws, lv, WS_O + (u32)((8 * range + w) * 32 + head) * 1024);
    STAMP(8);
    u32 fw;
    asm volatile("buffer_load_dword %0, %1, %2, %3 offen sc1" : "=v"(fw) : "v"((lane & 15) * 4), "s"(rws), "s"(WS_MB + (u32)b * 1024 + MB_W * 4) : "memory");
    bf16x8 wf[8][2];
#pragma unroll
    for (int ks = 0; ks < 8; ++ks)
#pragma unroll
      for (int c = 0; c < 2; ++c) wf[ks][c] = ldtr(65536 + ksl * 16384 + tb[c] + ks * 2048);
    asm volatile("s_waitcnt vmcnt(0) lgkmcnt(0)\n\ts_barrier" : "+v"(fw)::"memory");
    if (w == 0) post(ws, mbox(range, lane) + MB_O + head, lane < 32, tag);
    if (bi < 28 && w == 7) post(ws, wmb + MB_E, lane < 16, tag);
    STAMP(21);
    if (!__all((lane & 15) >= 14 || fw == tag)) poll(mb + MB_W + lane, lane < 14, tag);
#pragma unroll
    for (int i = 0; i < 4; ++i) if (i < 3 || w < 4) dma_sc1(w * 1024 + i * 8192, lv, rws, WS_WP + (u32)nt * (33 * 7168) + w * 1024 + i * 8192);
    if (b == 255 && w == 1) {
#pragma unroll
      for (int i = 0; i < 4; ++i) poll((const GAS u32*)(ws + WS_ARR) + i * 64 + lane, true, tag);
      if (lane == 0) __hip_atomic_store((GAS u32*)(ws + WS_SEED), tag, RLX_AGENT);
    }
    const int tile = 2 * m64 + mh;
    poll(mb + MB_O + ksl * 8 + lane, lane < 8, tag);
    STAMP(9);
    u32x4 of[8];
#pragma unroll
    for (int ks = 0; ks < 8; ++ks) ld16_sc1(of[ks], lv, rws, WS_O + (u32)(tile * 32 + ksl * 8 + ks) * 1024);
    f32x16 acc[2] = {};
#pragma unroll
    for (int ks = 0; ks < 8; ++ks) {
      WAITV1(7 - ks, of[ks]);
      acc[0] = MFMA32(wf[ks][0], BF8(of[ks]), acc[0]);
      acc[1] = MFMA32(wf[ks][1], BF8(of[ks]), acc[1]);
    }
    const u32 rb = mh ? 36864 : 131072;
#pragma unroll
    for (int j = 1; j < 4; ++j) {
      const u32 a = rb + (u32)(((ksl + j) & 3) * 3 + j - 1) * 2048 + lv;
      const int o = (j & 1) * 8;
      sts16f(a, (f32x4){acc[j >> 1][o], acc[j >> 1][o + 1], acc[j >> 1][o + 2], acc[j >> 1][o + 3]});
      sts16f(a + 1024, (f32x4){acc[j >> 1][o + 4], acc[j >> 1][o + 5], acc[j >> 1][o + 6], acc[j >> 1][o + 7]});
    }
    LBAR();
    float y[8];
#pragma unroll
    for (int e = 0; e < 8; ++e) y[e] = acc[0][e];
#pragma unroll
    for (int j = 0; j < 3; ++j) {
      const f32x4 a = lds16f(rb + (u32)(ksl * 3 + j) * 2048 + lv), d = lds16f(rb + (u32)(ksl * 3 + j) * 2048 + 1024 + lv);
#pragma unroll
      for (int e = 0; e < 4; ++e) { y[e] += a[e]; y[4 + e] += d[e]; }
    }
    x1v = resid_epi(y, xres, gav);
    st16_sc1(x1v, rws, lv, WS_X1 + (u32)(tile * 32 + n64 * 4 + ksl) * 1024);
    STAMP(10);
    {
      float s = 0.f, q = 0.f;
#pragma unroll
      for (int e = 0; e < 4; ++e) { const float a = bflo(x1v[e]), c = bfhi(x1v[e]); s += a + c; q = __builtin_fmaf(a, a, __builtin_fmaf(c, c, q)); }
      swap32f(s, q);
      sts4f(SC3 + (u32)(w * 32 + (lane & 31)) * 8 + (lane >> 5) * 4, s + q);
    }
    LBAR();
    if (ksl == 0) {
      const u32 a = SC3 + (u32)(mh * 32 + (lane & 31)) * 8;
      const f32x2 t = (lds8f(a) + lds8f(a + 512)) + (lds8f(a + 1024) + lds8f(a + 1536));
      if (lane < 32) __builtin_amdgcn_raw_buffer_store_b64(__builtin_bit_cast(u32x2, t), rws, WS_ST + (u32)(tile * 8 + n64) * 256 + lane * 8, 0, 16);
    }
    WAITBAR(0);
    if (w == 0) post(ws, mbox(((m64 >> 3) << 1) | (lane >> 3), ((lane & 7) << 2) | ((m64 >> 1) & 3)) + MB_X1 + (m64 & 1) * 8 + n64, lane < 16, tag);
    STAMP(11);
  }
  const u32 tdv = (u32)((bi & 3) * 448 + (w < 7 ? w : 6) * 64 + lane) * 1024 + n64 * 128;
  if (w < 4) ffn_up<4>(w, lane, tag, ws, rws, rwd, pv, m128, nt, tdv, b);
  else ffn_up<3>(w, lane, tag, ws, rws, rwd, pv, m128, nt, tdv, b);
  STAMP(16);
  {
    u32 t0 = tb[0] + ksl * 40960, t1 = tb[1] + ksl * 40960;
    PIN(t0); PIN(t1);
    WAITBAR(0);
    if (w == 0) post(ws, mbox(m128 >> 1, ((lane >> 1) << 2) | ((2 * m128 + (lane & 1)) & 3)) + MB_ACT + nt, lane < 16, tag);
    bf16x8 wp[8][2];
#pragma unroll
    for (int s = 0; s < 8; ++s) { wp[s][0] = ldtr(t0 + s * 2048); wp[s][1] = ldtr(t1 + s * 2048); }
    LBAR();
#pragma unroll
    for (int i = 0; i < 8; ++i) dma((u32)((w >> 1) * 40 + (w & 1) * 8 + i) * 1024, pv, rwd, (u32)((w >> 1) * 448 + 320 + (w & 1) * 64 + i * 8) * 1024);
    STAMP(15);
    poll(mb + MB_ACT + ksl * 4 + lane, lane < 4, tag);
    STAMP(17);
    u32x4 af[28];
    const u32 asrc = WS_ACT + (u32)((2 * m64 + mh) * 112 + ksl * 28) * 1024;
#pragma unroll
    for (int ks = 0; ks < 8; ++ks) ld16_sc1(af[ks], lv, rws, asrc + ks * 1024);
    LBAR();
    f32x16 acc[2] = {};
    bf16x8 wa0, wa1, wb0, wb1;
#define DN_PG(s) ((s) < 20 ? (s) * 2048 : ((s) - 20) * 2048)
#define DN_RD(A, B, s) do { A = ldtr(t0 + DN_PG(s)); B = ldtr(t1 + DN_PG(s)); } while (0)
#define DN_MM(A, B, ks) do { if ((ks) + 8 < 28) ld16_sc1(af[(ks) + 8 < 28 ? (ks) + 8 : 0], lv, rws, asrc + ((ks) + 8) * 1024); WAITV1((ks) + 8 < 28 ? 8 : 27 - (ks), af[ks]); \
      acc[0] = MFMA32(A, BF8(af[ks]), acc[0]); acc[1] = MFMA32(B, BF8(af[ks]), acc[1]); SBAR(); } while (0)
#pragma unroll
    for (int ks = 0; ks < 7; ++ks) DN_MM(wp[ks][0], wp[ks][1], ks);
    DN_RD(wa0, wa1, 8); DN_MM(wp[7][0], wp[7][1], 7);
#pragma unroll
    for (int ks = 8; ks < 28; ks += 2) {
      if (ks == 20) LBAR();
      DN_RD(wb0, wb1, ks + 1); DN_MM(wa0, wa1, ks);
      if (ks + 2 < 28) DN_RD(wa0, wa1, ks + 2);
      DN_MM(wb0, wb1, ks + 1);
    }
    STAMP(18);
    const u32 sb = (u32)ksl * 40960 + 16384 + mh * 6144 + lv;
#pragma unroll
    for (int j = 1; j < 4; ++j) {
      const int o = (j & 1) * 8;
      sts16f(sb + (j - 1) * 2048, (f32x4){acc[j >> 1][o], acc[j >> 1][o + 1], acc[j >> 1][o + 2], acc[j >> 1][o + 3]});
      sts16f(sb + (j - 1) * 2048 + 1024, (f32x4){acc[j >> 1][o + 4], acc[j >> 1][o + 5], acc[j >> 1][o + 6], acc[j >> 1][o + 7]});
    }
    LBAR();
    float y[8];
#pragma unroll
    for (int e = 0; e < 8; ++e) y[e] = acc[0][e];
#pragma unroll
    for (int j = 1; j < 4; ++j) {
      const u32 a = (u32)((ksl - j) & 3) * 40960 + 16384 + mh * 6144 + (j - 1) * 2048 + lv;
      const f32x4 c = lds16f(a), d = lds16f(a + 1024);
#pragma unroll
      for (int e = 0; e < 4; ++e) { y[e] += c[e]; y[4 + e] += d[e]; }
    }
    const u32x4 ov = resid_epi(y, x1v, gbv);
    __builtin_amdgcn_raw_buffer_store_b128(ov, rout, erow * 1024 + ecol * 2, 0, 0);
    STAMP(19);
  }
}

extern "C" void kernel_launch(void* const* d_in, const int* in_sizes, int n_in, void* d_out, int out_size, void* d_ws, size_t ws_size,
                              hipStream_t stream) {
  fused_block<<<256, 512, 0, stream>>>(d_in[0], d_in[1], d_in[2], d_in[4], d_in[5], d_in[6], d_in[7], d_in[8], d_out, (char*)d_ws);
}
```

```cpp
#include <hip/hip_runtime.h>
#include <stdint.h>

typedef unsigned u32;
typedef unsigned long long u64;
typedef __attribute__((ext_vector_type(2))) u32 u32x2;
typedef __attribute__((ext_vector_type(4))) u32 u32x4;
typedef __attribute__((ext_vector_type(2))) float f32x2;
typedef __attribute__((ext_vector_type(4))) float f32x4;
typedef __attribute__((ext_vector_type(16))) float f32x16;
typedef __attribute__((ext_vector_type(8))) short bf16x8;
typedef __attribute__((ext_vector_type(2))) __bf16 bf16x2;
typedef __amdgpu_buffer_rsrc_t rsrc_t;
#define LAS __attribute__((address_space(3)))
#define GAS __attribute__((address_space(1)))
#define DI __device__ __forceinline__
#define RLX_AGENT __ATOMIC_RELAXED, __HIP_MEMORY_SCOPE_AGENT

constexpr u32 WS_SEED = 0;
constexpr u32 WS_ARR = 4096;
constexpr u32 WS_MODL = 65536;
constexpr u32 WS_MODC = 524288;
constexpr u32 WS_MB = 262144;
constexpr u32 MB_QKV = 0, MB_O = 32, MB_X1 = 64, MB_E = 96, MB_W = 128, MB_ACT = 160;
constexpr u32 WS_ST = 786432;
constexpr u32 WS_Q = 1u << 20, WS_K = 3u << 20, WS_V = 5u << 20;
constexpr u32 WS_O = 7u << 20;
constexpr u32 WS_X1 = 9u << 20;
constexpr u32 WS_ACT = 11u << 20;
constexpr u32 WS_WP = 18u << 20;
constexpr u32 WPN = 34 * 7168;
constexpr u32 WS_BYTES = 22u << 20;
constexpr u32 WS_DBG = 24u << 20;
#define DEP 10
#define PRE_ARGS rws, WS_WP + (u32)nt * WPN + (u32)((bi & 7) * 30 + w) * 1024
constexpr u32 SPIN_CAP = 1u << 15;
#define START_DELAY 8

DI float bflo(u32 w) { return __uint_as_float(w << 16); }
DI float bfhi(u32 w) { return __uint_as_float(w & 0xffff0000u); }
DI u32 pk2(float a, float b) { f32x2 v = {a, b}; return __builtin_bit_cast(u32, __builtin_convertvector(v, bf16x2)); }
DI float rbf(float a) { return bflo(pk2(a, a)); }
template <int CTRL> DI float dppf(float x) { return __builtin_bit_cast(float, __builtin_amdgcn_update_dpp(0, __builtin_bit_cast(int, x), CTRL, 0xf, 0xf, true)); }
DI void swap32f(float& a, float& b) { auto r = __builtin_amdgcn_permlane32_swap(__float_as_uint(a), __float_as_uint(b), false, false); a = __uint_as_float(r[0]); b = __uint_as_float(r[1]); }
DI void swap16f(float& a, float& b) { auto r = __builtin_amdgcn_permlane16_swap(__float_as_uint(a), __float_as_uint(b), false, false); a = __uint_as_float(r[0]); b = __uint_as_float(r[1]); }
DI void swap32u(u32& a, u32& b) { auto r = __builtin_amdgcn_permlane32_swap(a, b, false, false); a = r[0]; b = r[1]; }
DI void swap16u(u32& a, u32& b) { auto r = __builtin_amdgcn_permlane16_swap(a, b, false, false); a = r[0]; b = r[1]; }
DI float row16_sum(float x) { x += dppf<0xB1>(x); x += dppf<0x4E>(x); x += dppf<0x141>(x); x += dppf<0x140>(x); return x; }
DI float rdlane(float x, int l) { return __uint_as_float(__builtin_amdgcn_readlane(__float_as_uint(x), l)); }

#define MFMA16(a, b, c) __builtin_amdgcn_mfma_f32_16x16x32_bf16((a), (b), (c), 0, 0, 0)
#define MFMA32(a, b, c) __builtin_amdgcn_mfma_f32_32x32x16_bf16((a), (b), (c), 0, 0, 0)
#define BF8(x) __builtin_bit_cast(bf16x8, (x))
#define WAITV(n) asm volatile("s_waitcnt vmcnt(%0)" ::"i"(n) : "memory")
#define WAITBAR(n) asm volatile("s_waitcnt vmcnt(%0) lgkmcnt(0)\n\ts_barrier" ::"i"(n) : "memory")
#define LBAR() asm volatile("s_waitcnt lgkmcnt(0)\n\ts_barrier" ::: "memory")
#define SBAR() __builtin_amdgcn_sched_barrier(0)
#define NOP_ do {} while (0)

DI void dma(u32 lds, u32 voff, rsrc_t rs, u32 soff) {
  asm volatile("s_mov_b32 m0, %0\n\ts_nop 0\n\tbuffer_load_dwordx4 %1, %2, %3 offen lds" ::"s"(lds), "v"(voff), "s"(rs), "s"(soff) : "memory");
}
DI void dma_sc1(u32 lds, u32 voff, rsrc_t rs, u32 soff) {
  asm volatile("s_mov_b32 m0, %0\n\ts_nop 0\n\tbuffer_load_dwordx4 %1, %2, %3 offen sc1 lds" ::"s"(lds), "v"(voff), "s"(rs), "s"(soff) : "memory");
}
DI void dma4(u32 lds, u32 voff, rsrc_t rs, u32 soff) {
  asm volatile("s_mov_b32 m0, %0\n\ts_nop 0\n\tbuffer_load_dword %1, %2, %3 offen lds" ::"s"(lds), "v"(voff), "s"(rs), "s"(soff) : "memory");
}
DI void ld16(u32x4& r, u32 voff, rsrc_t rs, u32 soff) { asm volatile("buffer_load_dwordx4 %0, %1, %2, %3 offen" : "=v"(r) : "v"(voff), "s"(rs), "s"(soff) : "memory"); }
DI void ld16_sc1(u32x4& r, u32 voff, rsrc_t rs, u32 soff) { asm volatile("buffer_load_dwordx4 %0, %1, %2, %3 offen sc1" : "=v"(r) : "v"(voff), "s"(rs), "s"(soff) : "memory"); }
DI void ld8_sc1(u32x2& r, u32 voff, rsrc_t rs, u32 soff) { asm volatile("buffer_load_dwordx2 %0, %1, %2, %3 offen sc1" : "=v"(r) : "v"(voff), "s"(rs), "s"(soff) : "memory"); }
DI void ld4(u32& r, u32 voff, rsrc_t rs, u32 soff) { asm volatile("buffer_load_dword %0, %1, %2, %3 offen" : "=v"(r) : "v"(voff), "s"(rs), "s"(soff) : "memory"); }
DI void ld2(u32& r, u32 voff, rsrc_t rs, u32 soff) { asm volatile("buffer_load_ushort %0, %1, %2, %3 offen" : "=v"(r) : "v"(voff), "s"(rs), "s"(soff) : "memory"); }
#define WAITV1(n, a) asm volatile("s_waitcnt vmcnt(%1)" : "+v"(a) : "i"(n) : "memory")
#define PIN(a) asm volatile("" : "+v"(a))
DI void st16_sc1(u32x4 v, rsrc_t rs, u32 voff) { __builtin_amdgcn_raw_buffer_store_b128(v, rs, voff, 0, 16); }

DI bf16x8 lds16(u32 a) { return *(const LAS bf16x8*)(uintptr_t)a; }
DI u32x4 lds16u(u32 a) { return *(const LAS u32x4*)(uintptr_t)a; }
DI f32x4 lds16f(u32 a) { return *(const LAS f32x4*)(uintptr_t)a; }
DI f32x2 lds8f(u32 a) { return *(const LAS f32x2*)(uintptr_t)a; }
DI float lds4f(u32 a) { return *(const LAS float*)(uintptr_t)a; }
DI void sts16(u32 a, u32x4 v) { *(LAS u32x4*)(uintptr_t)a = v; }
DI void sts16f(u32 a, f32x4 v) { *(LAS f32x4*)(uintptr_t)a = v; }
DI void sts8f(u32 a, f32x2 v) { *(LAS f32x2*)(uintptr_t)a = v; }
DI void sts4f(u32 a, float v) { *(LAS float*)(uintptr_t)a = v; }
typedef short v4i16_t __attribute__((ext_vector_type(4)));
DI bf16x8 ldtr(u32 a) {
  v4i16_t lo = __builtin_amdgcn_ds_read_tr16_b64_v4i16((LAS v4i16_t*)(uintptr_t)a);
  v4i16_t hi = __builtin_amdgcn_ds_read_tr16_b64_v4i16((LAS v4i16_t*)(uintptr_t)(a + 512));
  return __builtin_shufflevector(lo, hi, 0, 1, 2, 3, 4, 5, 6, 7);
}

DI void poll(u32 voff, rsrc_t rws, u32 tag) {
#pragma unroll 1
  for (u32 spins = 0; spins < SPIN_CAP; ++spins) {
    u32 v;
    asm volatile("buffer_load_dword %0, %1, %2, 0 offen sc1\n\ts_waitcnt vmcnt(0)" : "=v"(v) : "v"(voff), "s"(rws) : "memory");
    if (__all(v == tag)) break;
    __builtin_amdgcn_s_sleep(1);
  }
}
DI void post(u32 voff, rsrc_t rws, u32 tag) { __builtin_amdgcn_raw_buffer_store_b32(tag, rws, voff, 0, 16); }
constexpr u32 NOWHERE = 0x7ffffff0u;
DI u32 mbox(int xcd, int bi, u32 word) { return WS_MB + (u32)(bi * 8 + xcd) * 1024 + word * 4; }
DI bool tags_ok(u32x4 a, u32x4 b, u32 tag) { return a[1] == tag && a[3] == tag && b[1] == tag && b[3] == tag; }
DI float gsum(u32x4 a, u32x4 b, u32x4 c, u32x4 d, int i) { return (__uint_as_float(a[i]) + __uint_as_float(b[i])) + (__uint_as_float(c[i]) + __uint_as_float(d[i])); }
DI void st_gran(char* ws, u32 byte, float v, u32 tag) { __hip_atomic_store((GAS u64*)(ws + byte), ((u64)tag << 32) | __float_as_uint(v), RLX_AGENT); }
DI void ld4_sc1(u32& r, u32 voff, rsrc_t rs, u32 soff) { asm volatile("buffer_load_dword %0, %1, %2, %3 offen sc1" : "=v"(r) : "v"(voff), "s"(rs), "s"(soff) : "memory"); }
#define LD4X(a, b, c, d, voff, base, step) do { ld16_sc1(a, voff, rws, base); ld16_sc1(b, voff, rws, (base) + (step)); ld16_sc1(c, voff, rws, (base) + 2 * (step)); ld16_sc1(d, voff, rws, (base) + 3 * (step)); } while (0)
#define WAIT4X(n, a, b, c, d) asm volatile("s_waitcnt vmcnt(%4)" : "+v"(a), "+v"(b), "+v"(c), "+v"(d) : "i"(n) : "memory")

#define STAMP(i)
#define STAMP_END()
#define DBGARG
#define DBGPASS

DI float silu_bf(float c) { return rbf(c * __builtin_amdgcn_rcpf(1.f + __builtin_amdgcn_exp2f(c * -1.4426950408889634f))); }
DI float sum8(f32x4 a, f32x4 d) { return ((a[0] + a[1]) + (a[2] + a[3])) + ((d[0] + d[1]) + (d[2] + d[3])); }

constexpr u64 upper_mask(int e) {
  u64 m = 0;
  for (int l = 0; l < 64; ++l) if ((e & 3) + 8 * (e >> 2) + 4 * (l >> 5) > (l & 31)) m |= 1ull << l;
  return m;
}
template <int E> DI bool upper() { constexpr u64 m = upper_mask(E); return __builtin_amdgcn_inverse_ballot_w64(m); }
#define MX3(a, b, c) __builtin_fmaxf(__builtin_fmaxf((a), (b)), (c))
#define EX2(x) __builtin_amdgcn_exp2f(x)
template <int I> DI void sel_fill(f32x16& sel, const f32x16& sf, const f32x16& sd) {
  if constexpr (I < 16) { sel[I] = upper<I>() ? sf[I] : sd[I]; sel_fill<I + 1>(sel, sf, sd); }
}
#define WP_PRE(i) dma(40960 + w * 1024, lane * 16, rws, psrc + (i) * 8192)
#define PV(V, P) do { o = MFMA32(V, BF8(P), o); PIN(o); } while (0)
DI float half_max(const f32x16& s) {
  float m0 = __builtin_fmaxf(s[0], s[1]), m1 = __builtin_fmaxf(s[2], s[3]);
#pragma unroll
  for (int e = 4; e < 16; e += 4) { m0 = MX3(m0, s[e], s[e + 1]); m1 = MX3(m1, s[e + 2], s[e + 3]); }
  return __builtin_fmaxf(m0, m1);
}
DI float both_max(float m) { float mo = m; swap32f(m, mo); return __builtin_fmaxf(m, mo); }
DI void boundary_p(const f32x16& sel, float m, const u32 (&mk)[8], u32x4 (&pf)[2], u32x4 (&pd)[2]) {
#pragma unroll
  for (int e = 0; e < 8; ++e) {
    const u32 p = pk2(EX2(sel[2 * e] - m), EX2(sel[2 * e + 1] - m));
    pf[e >> 2][e & 3] = p & mk[e];
    pd[e >> 2][e & 3] = p & ~mk[e];
  }
}
DI f32x16 attn_exact(int ilo, bf16x8 qf, const u32 (&mk)[8], u32 kb, u32 vb0, u32 vb1) {
  const f32x16 zero = {};
  f32x16 sel, sf, sd = MFMA32(lds16(kb + 8192), qf, zero);
  if (ilo == 0) sf = MFMA32(lds16(kb), qf, zero);
  else {
#pragma unroll
    for (int e = 0; e < 16; ++e) sf[e] = -INFINITY;
  }
  sel_fill<0>(sel, sf, sd);
  float m = half_max(sel);
#pragma unroll 1
  for (int i = ilo > 1 ? ilo : 1; i < 8; ++i) m = __builtin_fmaxf(m, half_max(MFMA32(lds16(kb + i * 1024), qf, zero)));
  m = both_max(m);
  f32x16 negm;
#pragma unroll
  for (int e = 0; e < 16; ++e) negm[e] = -m;
  u32x4 pf[2], pd[2];
  boundary_p(sel, m, mk, pf, pd);
  f32x16 o = MFMA32(lds16(vb0 + 8 * 1040), BF8(pd[0]), zero);
  o = MFMA32(lds16(vb1 + 8 * 1040), BF8(pd[1]), o);
  if (ilo == 0) { o = MFMA32(lds16(vb0), BF8(pf[0]), o); o = MFMA32(lds16(vb1), BF8(pf[1]), o); }
#pragma unroll 1
  for (int i = ilo > 1 ? ilo : 1; i < 8; ++i) {
    const f32x16 sc = MFMA32(lds16(kb + i * 1024), qf, negm);
    u32x4 p0, p1;
#pragma unroll
    for (int e = 0; e < 4; ++e) { p0[e] = pk2(EX2(sc[2 * e]), EX2(sc[2 * e + 1])); p1[e] = pk2(EX2(sc[8 + 2 * e]), EX2(sc[8 + 2 * e + 1])); }
    o = MFMA32(lds16(vb0 + i * 1040), BF8(p0), o);
    o = MFMA32(lds16(vb1 + i * 1040), BF8(p1), o);
  }
  return o;
}
DI f32x16 attn_fast(int w, int lane, bf16x8 qf, const u32 (&mk)[8], const u32 (&ms)[16], u32 kb, u32 vb0, u32 vb1, rsrc_t rws, u32 psrc,
                    u32 pv, rsrc_t rwo, u32 r3) {
#define WO_DMA(i) dma(98304 + ((((i) ^ (r3 >> 8)) * 8 + w) * 1024), pv, rwo, (u32)((((i) ^ (r3 >> 8)) * 8 + w) ^ ((r3 & 7) << 1)) * 8192)
#define TWO(a, b) do { a; b; } while (0)
  const f32x16 zero = {};
  bf16x8 k8 = lds16(kb + 8192), k0 = lds16(kb), ka = lds16(kb + 1024), kc = lds16(kb + 2048);
  bf16x8 va = lds16(vb0 + 8 * 1040), vc = lds16(vb1 + 8 * 1040), vd = lds16(vb0), ve = lds16(vb1);
  SBAR();
  f32x16 sa = MFMA32(k8, qf, zero);
  PIN(sa); WO_DMA(0); SBAR();
  const float m = both_max(half_max(sa));
  f32x16 negm;
#pragma unroll
  for (int e = 0; e < 16; ++e) negm[e] = -m;
  PIN(negm); SBAR();
  f32x16 sd = MFMA32(k8, qf, negm), sb = MFMA32(k0, qf, negm);
  sa = MFMA32(ka, qf, negm);
  PIN(sd); PIN(sb); PIN(sa); WO_DMA(1); SBAR();
  f32x16 o = zero;
  u32x4 pd[2], pf[2], p0, p1;
#pragma unroll
  for (int e = 0; e < 8; ++e) {
    const float x0 = __uint_as_float((__float_as_uint(sb[2 * e]) & ms[2 * e]) | (__float_as_uint(sd[2 * e]) & ~ms[2 * e]));
    const float x1 = __uint_as_float((__float_as_uint(sb[2 * e + 1]) & ms[2 * e + 1]) | (__float_as_uint(sd[2 * e + 1]) & ~ms[2 * e + 1]));
    const u32 p = pk2(EX2(x0), EX2(x1));
    pf[e >> 2][e & 3] = p & mk[e];
    pd[e >> 2][e & 3] = p & ~mk[e];
  }
  PIN(pf[0]); PIN(pf[1]); PIN(pd[0]); PIN(pd[1]); WO_DMA(2); SBAR();
#define AT_Q(P, S, o_) do { P[(o_) & 3] = pk2(EX2(S[2 * (o_)]), EX2(S[2 * (o_) + 1])); P[((o_) & 3) + 1] = pk2(EX2(S[2 * (o_) + 2]), EX2(S[2 * (o_) + 3])); PIN(P); SBAR(); } while (0)
#define AT_STEP(S, N, KN, KR, i, M0, M1, M2, M3) do { \
    if ((i) < 7) { N = MFMA32(KN, qf, negm); PIN(N); if ((i) < 6) KR = lds16(kb + ((i) + 2) * 1024); SBAR(); } \
    u32x4 q0_, q1_; \
    AT_Q(q0_, S, 0); M0; SBAR(); AT_Q(q0_, S, 2); M1; SBAR(); AT_Q(q1_, S, 4); M2; SBAR(); AT_Q(q1_, S, 6); M3; \
    va = lds16(vb0 + (i) * 1040); vc = lds16(vb1 + (i) * 1040); p0 = q0_; p1 = q1_; SBAR(); } while (0)
  AT_STEP(sa, sb, kc, ka, 1, PV(va, pd[0]), PV(vc, pd[1]), PV(vd, pf[0]), PV(ve, pf[1]));
  AT_STEP(sb, sa, ka, kc, 2, WP_PRE(0), PV(va, p0), PV(vc, p1), WO_DMA(3));
  AT_STEP(sa, sb, kc, ka, 3, WP_PRE(1), PV(va, p0), PV(vc, p1), WO_DMA(4));
  AT_STEP(sb, sa, ka, kc, 4, WP_PRE(2), PV(va, p0), PV(vc, p1), WO_DMA(5));
  AT_STEP(sa, sb, kc, ka, 5, WP_PRE(3), PV(va, p0), PV(vc, p1), WO_DMA(6));
  AT_STEP(sb, sa, ka, kc, 6, WO_DMA(7), PV(va, p0), PV(vc, p1), NOP_);
  AT_STEP(sa, sb, kc, ka, 7, NOP_, PV(va, p0), PV(vc, p1), NOP_);
  PV(va, p0); PV(vc, p1);
  return o;
}
DI u32x4 attn_wave(bool full, int w, int lane, bf16x8 qf, const u32 (&mk)[8], const u32 (&ms)[16], u32 kb, u32 vb0, u32 vb1, rsrc_t rws, u32 psrc,
                   u32 pv, rsrc_t rwo, u32 r3) {
  f32x16 o = attn_fast(w, lane, qf, mk, ms, kb, vb0, vb1, rws, psrc, pv, rwo, r3);
  if (!__all(o[8] < 1.8446744e19f && o[8] > 5.4210109e-20f))
    o = attn_exact(full ? 0 : 8 - w, qf, mk, kb, vb0, vb1);
  const float rl = __builtin_amdgcn_rcpf(o[8]);
  u32 a0 = pk2(o[0] * rl, o[1] * rl), a1 = pk2(o[2] * rl, o[3] * rl), b0 = pk2(o[4] * rl, o[5] * rl), b1 = pk2(o[6] * rl, o[7] * rl);
  swap32u(a0, b0); swap32u(a1, b1);
  return (u32x4){a0, a1, b0, b1};
}

DI u32x4 resid_epi(float (&y)[8], u32x4 res, u32x4 gate) {
#pragma unroll
  for (int i = 0; i < 4; ++i) swap32f(y[i], y[4 + i]);
  u32x4 o;
#pragma unroll
  for (int i = 0; i < 4; ++i) {
    const u32 yb = pk2(y[2 * i], y[2 * i + 1]);
    const u32 tb = pk2(bflo(yb) * bflo(gate[i]), bfhi(yb) * bfhi(gate[i]));
    o[i] = pk2(bflo(res[i]) + bflo(tb), bfhi(res[i]) + bfhi(tb));
  }
  return o;
}

DI u32 rot28(u32 s, u32 r) { const u32 t = s + r; return t >= 28 ? t - 28 : t; }
#define WD_DMA(base) dma((u32)((base) + w) * 1024, pv, rwd, (u32)(((base) / 40) * 448 + (w & 1) * 8 + rot28((((base) % 40) + w) >> 1, r5) * 16) * 1024)

template <int NF>
DI void ffn_up(int w, int lane, u32 tag, char* ws, rsrc_t rws, rsrc_t rwd, u32 pv, int m128, int nt, u32 r5, u32 pollx, u32 wl, u32 wh, u32 xl, u32 stv, u32 adst, int b DBGARG) {
  constexpr int NW = NF;
  constexpr int F0 = NF == 4 ? 0 : 4;
  const int p = w & 3, tile = 4 * m128 + p;
  const u32 lv = lane * 16;
  const u32 wsrc = WS_WP + (u32)nt * WPN + w * 1024;
  const u32 xsrc = WS_X1 + (u32)(tile * 32 + 2 * (w >> 2)) * 1024;
  const u32 xdst = 114688 + (p * 4 + 2 * (w >> 2)) * 1024;
  const u32 r4 = (2 * nt + m128) & 7;
#define UP_W1(g, i) dma_sc1(((g) & 3) * 28672 + w * 1024 + (i) * 8192, lv, rws, wsrc + ((g) ^ r4) * 28672 + (i) * 8192)
#define UP_X1(g, i) dma_sc1(xdst + ((g) % 3) * 16384 + (i) * 1024, lv, rws, xsrc + ((g) ^ r4) * 4096 + (i) * 1024)
#pragma unroll
  for (int g = 1; g < 4; ++g)
#pragma unroll
    for (int i = 0; i < NW; ++i) UP_W1(g, i);
  STAMP(12);
  poll(pollx, rws, tag);
  STAMP(13);
#pragma unroll
  for (int g = 0; g < 3; ++g) { UP_X1(g, 0); UP_X1(g, 1); }
  f32x16 acc[NF];
#pragma unroll
  for (int f = 0; f < NF; ++f) acc[f] = (f32x16){};
  bf16x8 xa, wa[NF], xb, wb[NF];
  u32x4 s0, s1_, s2_, s3;
#define UP_RX(X, ks) X = lds16(xl + (((ks) >> 2) % 3) * 16384 + ((ks) & 3) * 1024)
#define UP_RW(W, f, ks) W[f] = lds16(((((ks) >> 2) & 2) ? wh : wl) + (((ks) >> 2) & 1) * 28672 + (((ks) & 3) * 7 + (f)) * 1024)
#define UP_STEP(XC, WC, XN, WN, ks, RD, MM, D0, D1) do { \
    if (RD) { UP_RX(XN, ks); UP_RW(WN, 0, ks); } SBAR(); if (MM) { acc[0] = MFMA32(WC[0], XC, acc[0]); PIN(acc[0]); } SBAR(); \
    if (RD) { UP_RW(WN, 1, ks); UP_RW(WN, 2, ks); } SBAR(); if (MM) { acc[1] = MFMA32(WC[1], XC, acc[1]); PIN(acc[1]); } SBAR(); \
    if (RD && NF == 4) UP_RW(WN, NF - 1, ks); D0; SBAR(); if (MM) { acc[2] = MFMA32(WC[2], XC, acc[2]); PIN(acc[2]); } SBAR(); \
    D1; if (MM && NF == 4) { acc[NF - 1] = MFMA32(WC[NF - 1], XC, acc[NF - 1]); PIN(acc[NF - 1]); } SBAR(); } while (0)
#define UP_WL(g) do { if (NF == 4) UP_W1(g, 3); } while (0)
#define UP_GROUP(g, MM0, A0, A1, B0, B1, C0, C1, E0, E1) do { \
    UP_STEP(xb, wb, xa, wa, 4 * (g), true, MM0, A0, A1); UP_STEP(xa, wa, xb, wb, 4 * (g) + 1, true, true, B0, B1); \
    UP_STEP(xb, wb, xa, wa, 4 * (g) + 2, true, true, C0, C1); UP_STEP(xa, wa, xb, wb, 4 * (g) + 3, true, true, E0, E1); } while (0)
#define UP_GREFILL(g, S0, S1) UP_GROUP(g, true, UP_X1((g) + 2, 0), UP_X1((g) + 2, 1), UP_W1((g) + 3, 0), S0, UP_W1((g) + 3, 1), UP_W1((g) + 3, 2), UP_WL((g) + 3), S1)
#define UP_E(i) dma_sc1(((i) * 8 + w) * 1024, lv, rws, WS_WP + (u32)nt * WPN + 32 * 7168 + ((i) * 8 + w) * 1024)
  WAITBAR(4); STAMP(24); UP_GROUP(0, false, NOP_, NOP_, NOP_, NOP_, NOP_, NOP_, NOP_, NOP_);
  WAITBAR(2); STAMP(25); UP_GREFILL(1, NOP_, NOP_);
  WAITBAR(NW + 2); STAMP(26); UP_GREFILL(2, NOP_, NOP_);
  WAITBAR(2 * NW + 2); STAMP(27); UP_GREFILL(3, NOP_, LD4X(s0, s1_, s2_, s3, stv, WS_ST + (u32)tile * 4096, 512));
  WAITBAR(2 * NW + 6); STAMP(28); UP_GREFILL(4, NOP_, NOP_);
  WAITBAR(2 * NW + 6); STAMP(29);
  UP_GROUP(5, true, UP_X1(7, 0), UP_X1(7, 1), UP_E(0), NOP_, UP_E(1), NOP_, WD_DMA(16), NOP_);
  asm volatile("s_waitcnt vmcnt(%4) lgkmcnt(0)\n\ts_barrier" : "+v"(s0), "+v"(s1_), "+v"(s2_), "+v"(s3) : "i"(NW + 5) : "memory");
  STAMP(30);
#pragma unroll 1
  for (u32 spins = 0; spins < SPIN_CAP; ++spins) {
    if (__all(tags_ok(s0, s1_, tag) && tags_ok(s2_, s3, tag))) break;
    LD4X(s0, s1_, s2_, s3, stv, WS_ST + (u32)tile * 4096, 512);
    WAIT4X(0, s0, s1_, s2_, s3);
  }
  float s1 = gsum(s0, s1_, s2_, s3, 0), s2 = gsum(s0, s1_, s2_, s3, 2);
  { float t1 = s1, t2 = s2; swap32f(s1, t1); swap32f(s2, t2); s1 += t1; s2 += t2; }
  const float mu = s1 * (1.f / 512.f);
  const float var = __builtin_fmaxf(s2 * (1.f / 512.f) - mu * mu, 0.f) + 1e-5f;
  float rstd = __builtin_amdgcn_rsqf(var);
  const float sig = var * rstd;
  const float mh = rbf(-mu), ml = -mu - mh, sh = rbf(sig), sl = sig - sh;
  u32x4 ev = {pk2(mh, mh), pk2(ml, sh), pk2(sh, sl), 0u};
  PIN(ev); PIN(rstd); SBAR();
  UP_GROUP(6, true, WD_DMA(24), WD_DMA(32), WD_DMA(40), NOP_, WD_DMA(48), NOP_, WD_DMA(144), NOP_);
  WAITBAR(6); STAMP(31);
  UP_GROUP(7, true, WD_DMA(56), WD_DMA(64), WD_DMA(72), NOP_, WD_DMA(112), NOP_, WD_DMA(120), NOP_);
#pragma unroll
  for (int f = 0; f < NF; ++f) wa[f] = lds16(wl + f * 1024);
  UP_STEP(xb, wb, xa, wa, 0, false, true, NOP_, NOP_);
#pragma unroll
  for (int f = 0; f < NF; ++f) wb[f] = lds16(wl + 7168 + f * 1024);
#pragma unroll
  for (int f = 0; f < NF; ++f) { acc[f] = MFMA32(wa[f], BF8(ev), acc[f]); PIN(acc[f]); }
  LBAR();
  STAMP(14);
#pragma unroll
  for (int f = 0; f < NF; ++f) acc[f] = MFMA32(wb[f], BF8(ev), acc[f]);
  WD_DMA(0); WD_DMA(8); WD_DMA(80); WD_DMA(88);
  SBAR();
  constexpr float C1 = -2.f * 0.7978845608028654f * 1.4426950408889634f, C2 = C1 * 0.044715f;
  const float k1 = C1 * rstd, k3 = C2 * rstd * rstd * rstd;
#pragma unroll
  for (int f = 0; f < NF; ++f) {
    float a[8];
#pragma unroll
    for (int e = 0; e < 8; ++e) {
      const float g = acc[f][e];
      const float z = g * __builtin_fmaf(g * g, k3, k1);
      a[e] = (g * acc[f][8 + e]) * __builtin_amdgcn_rcpf(__builtin_fmaf(__builtin_amdgcn_exp2f(z), var, var));
    }
    u32 a0 = pk2(a[0], a[1]), a1 = pk2(a[2], a[3]), b0 = pk2(a[4], a[5]), b1 = pk2(a[6], a[7]);
    swap32u(a0, b0); swap32u(a1, b1);
    st16_sc1((u32x4){a0, a1, b0, b1}, rws, adst + f * 1024);
    if (f == 0) { WD_DMA(128); WD_DMA(96); }
    if (f == 1) { WD_DMA(104); WD_DMA(136); }
    if (f == 2) WD_DMA(152);
    SBAR();
  }

}

__global__ void __launch_bounds__(512, 2)
fused_block(const void* __restrict__ px, const void* __restrict__ pwqkv, const void* __restrict__ pwo, const void* __restrict__ pwg,
            const void* __restrict__ pwu, const void* __restrict__ pwd, const void* __restrict__ pc, const void* __restrict__ pwm,
            void* pout, char* ws) {
  __shared__ __attribute__((aligned(16))) char smem[163840];
  asm volatile("" ::"v"((u32)(uintptr_t)(LAS char*)smem));
  const int tid = threadIdx.x, lane = tid & 63, w = __builtin_amdgcn_readfirstlane(tid >> 6);
  const int b = blockIdx.x, xcd = b & 7, bi = b >> 3;
  const rsrc_t rx = __builtin_amdgcn_make_buffer_rsrc((void*)px, 0, 2048 * 512 * 2, 0x00020000);
  const rsrc_t rqkv = __builtin_amdgcn_make_buffer_rsrc((void*)pwqkv, 0, 512 * 1536 * 2, 0x00020000);
  const rsrc_t rwo = __builtin_amdgcn_make_buffer_rsrc((void*)pwo, 0, 512 * 512 * 2, 0x00020000);
  const rsrc_t rwd = __builtin_amdgcn_make_buffer_rsrc((void*)pwd, 0, 1792 * 512 * 2, 0x00020000);
  const rsrc_t rout = __builtin_amdgcn_make_buffer_rsrc(pout, 0, 2048 * 512 * 2, 0x00020000);
  const rsrc_t rws = __builtin_amdgcn_make_buffer_rsrc(ws, 0, WS_BYTES, 0x00020000);
  const u32 lv = lane * 16;
  STAMP(0);

  const int m64a = 8 * (xcd & 3) + (bi & 7), grp = 4 * (xcd >> 2) + (bi >> 3);
  constexpr u32 SCR = 155648;
  const rsrc_t rc = __builtin_amdgcn_make_buffer_rsrc((void*)pc, 0, 512 * 2, 0x00020000);
  const rsrc_t rwm = __builtin_amdgcn_make_buffer_rsrc((void*)pwm, 0, 512 * 3072 * 2, 0x00020000);
  if (w == 0) {
    __builtin_amdgcn_s_setprio(2);
    u32 cv[4], seed; u32x4 m[4];
    asm volatile("s_load_dword %0, %1, 0x0" : "=s"(seed) : "s"(ws) : "memory");
    const u32 r0 = (u32)(bi >> 3) * 128 + (lane >> 1);
#pragma unroll
    for (int i = 0; i < 4; ++i) ld16(m[i], r0 * 6144 + (lane & 1) * 16, rwm, (u32)(xcd * 8 + (bi & 7)) * 32 + i * (32 * 6144));
#pragma unroll
    for (int i = 0; i < 4; ++i) ld2(cv[i], r0 * 2, rc, i * 64);
    asm volatile("s_waitcnt vmcnt(0) lgkmcnt(0)" : "+v"(cv[0]), "+v"(cv[1]), "+v"(cv[2]), "+v"(cv[3]), "+v"(m[0]), "+v"(m[1]), "+v"(m[2]), "+v"(m[3]), "+s"(seed)::"memory");
    float v[8] = {};
#pragma unroll
    for (int i = 0; i < 4; ++i) {
      const float sc = silu_bf(bflo(cv[i]));
#pragma unroll
      for (int e = 0; e < 4; ++e) { v[2 * e] = __builtin_fmaf(sc, bflo(m[i][e]), v[2 * e]); v[2 * e + 1] = __builtin_fmaf(sc, bfhi(m[i][e]), v[2 * e + 1]); }
    }
#pragma unroll
    for (int j = 0; j < 4; ++j) { swap32f(v[j], v[j + 4]); v[j] += v[j + 4]; }
#pragma unroll
    for (int j = 0; j < 2; ++j) { swap16f(v[j], v[j + 2]); v[j] += v[j + 2]; }
    const bool o = lane & 8;
    float t = (o ? v[1] : v[0]) + dppf<0x128>(o ? v[0] : v[1]);
    t += dppf<0x4E>(t);
    t += __builtin_bit_cast(float, __builtin_amdgcn_update_dpp(__builtin_amdgcn_update_dpp(0, __builtin_bit_cast(int, t), 0x104, 0xf, 0x5, false), __builtin_bit_cast(int, t), 0x114, 0xf, 0xa, false));
    const u32 a = WS_MODC + (u32)((lane >> 1) & 3) * 32768 + (u32)(bi >> 3) * 8192 + (u32)((xcd * 8 + (bi & 7)) * 16 + (lane & 1) * 8 + (lane >> 3)) * 8;
    st_gran(ws, a, t, seed + 1u); st_gran(ws, a + 131072, t, seed + 1u);
  }
  STAMP(1);
  u32 seed;
  asm volatile("s_load_dword %0, %1, 0x0" : "=s"(seed) : "s"(ws) : "memory");
  u32x4 xr[8];
#pragma unroll
  for (int j = 0; j < 8; ++j) ld16(xr[j], lv, rx, (u32)(m64a * 64 + w * 8 + j) * 1024);
  u32 cv1; u32x4 ml;
  {
    const u32 r1 = (u32)(bi >> 2) * 64 + w * 8 + (lane >> 3);
    ld2(cv1, r1 * 2, rc, 0);
    ld16(ml, r1 * 6144 + (lane & 7) * 16, rwm, 2048 + (u32)(xcd * 4 + (bi & 3)) * 128);
  }
  const u32 r1 = bi & 7;
  const int n64 = bi >> 2, m64 = 4 * xcd + (bi & 3);
  const int mh = w & 1, ksl = w >> 1, tile3 = 2 * m64 + mh;
  const int head2 = 4 * xcd + (bi & 3), range = bi >> 2;
  const int m128 = 8 * (xcd >> 2) + (bi & 7), nt = 4 * (xcd & 3) + (bi >> 3);
  const int wid = xcd * 28 + bi, wq = wid & 3, wmat = wid >= 112, wg = (wid >> 2) - wmat * 28;
  constexpr u32 SC2 = 53248, SC3 = 94208;
  u32 qv, ab[4], bl, bh, aw[8], pv, pg, gvo, xro, tb[2], mk[8], ms[16], kb, vb0, vb1, wdst, ta, qdst, odst, xdst3, adst;
  u32 postq, posto, postw, postx, posta, pollq, pollo, pollw, pollx, polla, rbw[3], rbr, wl, xl, stv;
  const int wm = w >> 2, hn = w & 3, fr = lane & 15, g = lane >> 4;
  {
    qv = (u32)(w * 8 + (lane >> 3)) * 3072 + (((lane & 7) ^ ((((lane >> 4) & 1) | ((w & 1) << 1)) << 1)) * 16) + grp * 128;
#pragma unroll
    for (int t = 0; t < 4; ++t) ab[t] = (u32)(wm * 32 + fr) * 1024 + ((g ^ (fr & 3)) * 16) + ((t ^ (fr >> 2)) * 64);
    bl = 65536 + (u32)(8 * g + (fr >> 2)) * 128 + ((hn ^ (((fr >> 3) & 1) | ((g & 1) << 1))) * 32) + (lane & 3) * 8; bh = bl + 49152;
#pragma unroll
    for (int j = 0; j < 8; ++j) aw[j] = (u32)(w * 8 + j) * 1024 + ((lane ^ (r1 << 3) ^ ((w * 8 + j) & 15)) * 16);
    pg = (u32)(lane >> 3) * 3584 + (((lane & 7) ^ (((lane >> 4) & 1) << 2)) * 16) + (u32)(wq * 128 + w * 16) * 3584 + wg * 128;
    gvo = (u32)((lane >> 3) & 1) * 65536 + (lane & 7) * 16 + 8 * (lane < 32 ? 512 + (lane >> 4) * 512 + wq * 128 + w * 16 : (lane < 48 ? 0 : 1536) + n64 * 64 + ksl * 16);
    xro = (u32)(tile3 * 32 + (lane & 31)) * 1024 + (u32)(n64 * 64 + ksl * 16 + (lane >> 5) * 8) * 2;
    qdst = (u32)((4 * grp + hn) * 64 + 2 * m64a + wm) * 1024 + lv;
    PIN(qv); PIN(bl); PIN(bh); PIN(pg); PIN(gvo); PIN(xro); PIN(qdst);
#pragma unroll
    for (int t = 0; t < 4; ++t) PIN(ab[t]);
#pragma unroll
    for (int j = 0; j < 8; ++j) PIN(aw[j]);
  }
#define QKV_DMA(s, seg) dma(65536 + ((s) & 3) * 24576 + (seg) * 8192 + w * 1024, qv, rqkv, ((s) ^ r1) * (64 * 3072) + (seg) * 1024)
  asm volatile("s_waitcnt vmcnt(2) lgkmcnt(0)" : "+s"(seed)::"memory");
  const u32 tag = seed + 1u;
  u32x4 a0, a1, a2, a3;
  f32x2 xn[8][4];
  {
    asm volatile("" : "+v"(xr[0]), "+v"(xr[1]), "+v"(xr[2]), "+v"(xr[3]), "+v"(xr[4]), "+v"(xr[5]), "+v"(xr[6]), "+v"(xr[7])::"memory");
    float S[8], Q[8];
#pragma unroll
    for (int j = 0; j < 8; ++j) {
#pragma unroll
      for (int e = 0; e < 4; ++e) xn[j][e] = (f32x2){bflo(xr[j][e]), bfhi(xr[j][e])};
      const f32x2 s2 = (xn[j][0] + xn[j][1]) + (xn[j][2] + xn[j][3]);
      const f32x2 q2 = __builtin_elementwise_fma(xn[j][0], xn[j][0], __builtin_elementwise_fma(xn[j][1], xn[j][1], __builtin_elementwise_fma(xn[j][2], xn[j][2], xn[j][3] * xn[j][3])));
      S[j] = s2[0] + s2[1]; Q[j] = q2[0] + q2[1];
    }
#pragma unroll
    for (int j = 0; j < 4; ++j) { swap32f(S[j], S[j + 4]); S[j] += S[j + 4]; swap32f(Q[j], Q[j + 4]); Q[j] += Q[j + 4]; }
#pragma unroll
    for (int j = 0; j < 2; ++j) { swap16f(S[j], S[j + 2]); S[j] += S[j + 2]; swap16f(Q[j], Q[j + 2]); Q[j] += Q[j + 2]; }
    float ra[2], rb[2];
#pragma unroll
    for (int j = 0; j < 2; ++j) {
      const float mu = row16_sum(S[j]) * (1.f / 512.f);
      const float var = __builtin_fmaxf(row16_sum(Q[j]) * (1.f / 512.f) - mu * mu, 0.f) + 1e-5f;
      ra[j] = __builtin_amdgcn_rsqf(var); rb[j] = -mu * ra[j];
    }
    SBAR();
    LD4X(a0, a1, a2, a3, lv, WS_MODC + (u32)xcd * 32768 + w * 1024, 8192);
    SBAR();
#pragma unroll
    for (int j = 0; j < 8; ++j) {
      const float a = rdlane(ra[j & 1], (j >> 1) * 16), c = rdlane(rb[j & 1], (j >> 1) * 16);
#pragma unroll
      for (int e = 0; e < 4; ++e) { xn[j][e] = __builtin_elementwise_fma(xn[j][e], (f32x2){a, a}, (f32x2){c, c}); PIN(xn[j][e]); }
    }
  }
  asm volatile("s_waitcnt vmcnt(4)" : "+v"(cv1), "+v"(ml)::"memory");
  {
    const float sc = silu_bf(bflo(cv1));
    float v[8];
#pragma unroll
    for (int e = 0; e < 4; ++e) { v[2 * e] = sc * bflo(ml[e]); v[2 * e + 1] = sc * bfhi(ml[e]); }
#pragma unroll
    for (int j = 0; j < 4; ++j) { swap32f(v[j], v[j + 4]); v[j] += v[j + 4]; }
#pragma unroll
    for (int j = 0; j < 2; ++j) { swap16f(v[j], v[j + 2]); v[j] += v[j + 2]; }
    const bool o = lane & 8;
    const float a = (o ? v[1] : v[0]) + dppf<0x128>(o ? v[0] : v[1]);
    sts4f(SCR + 1024 + ((lane & 7) * 8 + (lane >> 3)) * 32 + w * 4, a);
  }
  STAMP(22);
  QKV_DMA(0, 0); QKV_DMA(0, 1); QKV_DMA(0, 2); QKV_DMA(1, 0); QKV_DMA(1, 1); QKV_DMA(1, 2);
  {
    WAIT4X(6, a0, a1, a2, a3);
#pragma unroll 1
    for (u32 spins = 0; spins < SPIN_CAP; ++spins) {
      if (__all(tags_ok(a0, a1, tag) && tags_ok(a2, a3, tag))) break;
      LD4X(a0, a1, a2, a3, lv, WS_MODC + (u32)xcd * 32768 + w * 1024, 8192);
      WAIT4X(0, a0, a1, a2, a3);
    }
    if (w == 0) __builtin_amdgcn_s_setprio(0);
    *(LAS u32*)(uintptr_t)(SCR + 4096 + tid * 4) = pk2(gsum(a0, a1, a2, a3, 0), gsum(a0, a1, a2, a3, 2));
  }
  STAMP(23);
  LBAR();
  STAMP(2);
  {
    const u32x4 sa = lds16u(SCR + 4096 + lv), ca = lds16u(SCR + 5120 + lv);
    f32x2 fs[4], fa[4];
#pragma unroll
    for (int e = 0; e < 4; ++e) {
      const u32 t = pk2(1.f + bflo(ca[e]), 1.f + bfhi(ca[e]));
      fs[e] = (f32x2){bflo(t), bfhi(t)}; fa[e] = (f32x2){bflo(sa[e]), bfhi(sa[e])};
    }
#pragma unroll
    for (int j = 0; j < 8; ++j) {
      u32x4 h;
#pragma unroll
      for (int e = 0; e < 4; ++e) { const f32x2 t = __builtin_elementwise_fma(xn[j][e], fs[e], fa[e]); h[e] = pk2(t[0], t[1]); }
      sts16(aw[j], h);
    }
    if (w == 1) st_gran(ws, WS_MODL + (u32)(bi >> 2) * 16384 + (u32)((xcd * 4 + (bi & 3)) * 64 + lane) * 8,
                        sum8(lds16f(SCR + 1024 + lane * 32), lds16f(SCR + 1024 + lane * 32 + 16)), tag);
  }
  const rsrc_t rwg = __builtin_amdgcn_make_buffer_rsrc((void*)(wmat ? pwu : pwg), 0, bi < 28 ? 512 * 1792 * 2 : 0, 0x00020000);
#define WG_DMA(i) dma(65536 + w * 2048 + (i) * 1024, pg, rwg, (i) * (8 * 3584))
  u32x4 g0, g1, g2, g3, xres;
  {
    f32x4 acc[2][3] = {};
    bf16x8 fa[2][2][2], fb[2][2][3];
#define Q_RA(S, s, kk, m) fa[S][kk][m] = lds16(ab[(2 * (s) + (kk)) & 3] + ((2 * (s) + (kk)) >> 2) * 256 + (m) * 16384)
#define Q_RB(S, s, kk, sg) fb[S][kk][sg] = ldtr((((s) & 2) ? bh : bl) + ((s) & 1) * 24576 + (sg) * 8192 + (kk) * 4096)
#define Q_M(S, kk, m, sg) do { acc[m][sg] = (sg) < 2 ? MFMA16(fb[S][kk][sg], fa[S][kk][m], acc[m][sg]) : MFMA16(fa[S][kk][m], fb[S][kk][sg], acc[m][sg]); PIN(acc[m][sg]); } while (0)
#define Q_STAGE(C, N, s, RD, D0, D1, D2, D3) do { \
      if (RD) { Q_RA(N, s, 0, 0); Q_RA(N, s, 0, 1); } Q_M(C, 0, 0, 0); SBAR(); \
      if (RD) Q_RB(N, s, 0, 0); Q_M(C, 0, 0, 1); SBAR(); \
      if (RD) Q_RB(N, s, 0, 1); Q_M(C, 0, 0, 2); SBAR(); \
      if (RD) Q_RB(N, s, 0, 2); Q_M(C, 0, 1, 0); SBAR(); \
      if (RD) { Q_RA(N, s, 1, 0); Q_RA(N, s, 1, 1); } Q_M(C, 0, 1, 1); SBAR(); \
      if (RD) Q_RB(N, s, 1, 0); Q_M(C, 0, 1, 2); SBAR(); \
      if (RD) Q_RB(N, s, 1, 1); Q_M(C, 1, 0, 0); SBAR(); \
      if (RD) Q_RB(N, s, 1, 2); Q_M(C, 1, 0, 1); SBAR(); \
      D0; Q_M(C, 1, 0, 2); SBAR(); D1; Q_M(C, 1, 1, 0); SBAR(); D2; Q_M(C, 1, 1, 1); SBAR(); D3; Q_M(C, 1, 1, 2); SBAR(); } while (0)
    WAITBAR(0);
    QKV_DMA(2, 0); QKV_DMA(2, 1); QKV_DMA(2, 2);
#pragma unroll
    for (int kk = 0; kk < 2; ++kk) { Q_RA(0, 0, kk, 0); Q_RA(0, 0, kk, 1); Q_RB(0, 0, kk, 0); Q_RB(0, 0, kk, 1); Q_RB(0, 0, kk, 2); }
    STAMP(20);
    LBAR();
    Q_STAGE(0, 1, 1, true, QKV_DMA(3, 0), QKV_DMA(3, 1), QKV_DMA(3, 2), QKV_DMA(4, 0));
    WAITBAR(4); Q_STAGE(1, 0, 2, true, QKV_DMA(4, 1), QKV_DMA(4, 2), QKV_DMA(5, 0), QKV_DMA(5, 1));
    WAITBAR(5); Q_STAGE(0, 1, 3, true, QKV_DMA(5, 2), QKV_DMA(6, 0), QKV_DMA(6, 1), QKV_DMA(6, 2));
    WAITBAR(6); Q_STAGE(1, 0, 4, true, QKV_DMA(7, 0), QKV_DMA(7, 1), QKV_DMA(7, 2), NOP_);
    WAITBAR(6); Q_STAGE(0, 1, 5, true, WG_DMA(0), WG_DMA(1), ld16(xres, xro, rx, 0), NOP_);
    WAITBAR(6); Q_STAGE(1, 0, 6, true, LD4X(g0, g1, g2, g3, gvo, WS_MODL, 16384), NOP_, NOP_, NOP_);
    WAITBAR(7); Q_STAGE(0, 1, 7, true, NOP_, NOP_, NOP_, NOP_);
    Q_STAGE(1, 0, 0, false, NOP_, NOP_, NOP_, NOP_);
    STAMP(3);
    constexpr float QS = 0.25f * 1.4426950408889634f;
    {
      u32 p0 = pk2(acc[0][0][0] * QS, acc[0][0][1] * QS), p1 = pk2(acc[0][0][2] * QS, acc[0][0][3] * QS);
      u32 r0 = pk2(acc[1][0][0] * QS, acc[1][0][1] * QS), r1 = pk2(acc[1][0][2] * QS, acc[1][0][3] * QS);
      swap16u(p0, r0); swap16u(p1, r1);
      st16_sc1((u32x4){p0, p1, r0, r1}, rws, WS_Q + qdst);
    }
    {
      u32 p0 = pk2(acc[0][1][0], acc[0][1][1]), p1 = pk2(acc[0][1][2], acc[0][1][3]);
      u32 r0 = pk2(acc[1][1][0], acc[1][1][1]), r1 = pk2(acc[1][1][2], acc[1][1][3]);
      swap16u(p0, r0); swap16u(p1, r1);
      st16_sc1((u32x4){p0, p1, r0, r1}, rws, WS_K + qdst);
    }
    {
      u32 p0 = pk2(acc[0][2][0], acc[0][2][1]), p1 = pk2(acc[0][2][2], acc[0][2][3]);
      u32 r0 = pk2(acc[1][2][0], acc[1][2][1]), r1 = pk2(acc[1][2][2], acc[1][2][3]);
      swap32u(p0, r0); swap32u(p1, r1);
      st16_sc1((u32x4){p0, p1, r0, r1}, rws, WS_V + qdst);
    }
  }
  u32x4 gav, gbv, x1v, q;
  {
    pv = (u32)(lane >> 3) * 1024 + (((lane & 7) ^ (((lane >> 4) & 1) << 2)) * 16) + n64 * 128;
#pragma unroll
    for (int e = 0; e < 16; ++e) ms[e] = (e & 3) + 8 * (e >> 2) + 4 * (lane >> 5) > (lane & 31) ? 0xffffffffu : 0u;
#pragma unroll
    for (int e = 0; e < 8; ++e) mk[e] = (ms[2 * e] & 0xffffu) | (ms[2 * e + 1] & 0xffff0000u);
    kb = (u32)(w * 1024 + lane * 16);
    vb0 = 16384 + w * 1040 + ((lane & 31) < 16 ? (lane >> 5) * 256 + (lane & 31) * 16 : 1024);
    vb1 = 16384 + w * 1040 + ((lane & 31) < 16 ? 512 + (lane >> 5) * 256 + (lane & 31) * 16 : 1024);
    const int wcu = wg * 4 + g, wn = wcu / 7, wfr = wcu - wn * 7;
    wdst = WS_WP + (u32)wn * WPN + wfr * 1024 + (u32)(wmat * 16 + fr) * 16;
    ta = 65536 + w * 2048 + (u32)(fr >> 2) * 128 + ((g ^ ((fr >> 2) & 2)) * 32) + (lane & 3) * 8;
    odst = WS_O + (u32)((8 * range + w) * 32 + head2) * 1024 + lv;
    const int hd = 4 * grp + (lane & 3), rg = (m64a >> 2) + ((lane >> 2) & 1);
    postq = lane < 4 || (lane < 8 && rg < 8) ? mbox(hd >> 2, (rg << 2) | (hd & 3), MB_QKV + (m64a & 3) + (lane < 4 ? 4 : 0)) : lane == 8 ? WS_ARR + b * 4 : NOWHERE;
    posto = lane < 32 ? mbox(range, lane, MB_O + head2) : NOWHERE;
    const int wnp = (wg * 4 + (lane < 16 ? 0 : 3)) / 7;
    postw = lane < 32 ? mbox(((fr >> 3) << 2) | (wnp >> 2), ((wnp & 3) << 3) | (lane & 7), wmat * 12 + (wg - ((7 * wnp) >> 2)) * 4 + wq) : NOWHERE;
    pollq = mbox(xcd, bi, MB_QKV + (w >> 1) + ((lane & 1) || range == 0 ? 4 : 0));
    const int l24 = (lane & 31) < 24 ? (lane & 31) : 0, l12 = l24 < 12 ? l24 : l24 - 12;
    const int wslot = l12 < 4 * (((7 * nt + 6) >> 2) - ((7 * nt) >> 2) + 1) ? l24 : 0;
    pollw = mbox(xcd, bi, MB_W + wslot);
    pollx = mbox(xcd, bi, lane < 32 ? MB_X1 + ((w >> 1) & 1) * 8 + (lane & 7) : MB_E + wslot);
    PIN(pv); PIN(kb); PIN(vb0); PIN(vb1); PIN(wdst); PIN(ta); PIN(odst); PIN(postq); PIN(posto); PIN(postw); PIN(pollq); PIN(pollw); PIN(pollx);
#pragma unroll
    for (int j = 0; j < 8; ++j) PIN(mk[j]);
#pragma unroll
    for (int e = 0; e < 16; ++e) PIN(ms[e]);
  }
  {
    asm volatile("s_waitcnt vmcnt(0) lgkmcnt(0)\n\ts_barrier" : "+v"(g0), "+v"(g1), "+v"(g2), "+v"(g3), "+v"(xres)::"memory");
    if (w == 0) post(postq, rws, tag);
    STAMP(4);
    if (tid < 16) { const u32 o1 = tid >= 8 || range > 0 ? 0x3f803f80u : 0u; sts16(16384 + tid * 1040 + 1024, (u32x4){o1, o1, o1, o1}); }
#pragma unroll 1
    for (u32 spins = 0; spins < SPIN_CAP; ++spins) {
      if (__all(tags_ok(g0, g1, tag) && tags_ok(g2, g3, tag))) break;
      __builtin_amdgcn_s_sleep(1);
      LD4X(g0, g1, g2, g3, gvo, WS_MODL, 16384);
      WAIT4X(0, g0, g1, g2, g3);
    }
    const u32 gs = 49152 + w * 128;
    {
      float t0 = gsum(g0, g1, g2, g3, 0), t1 = gsum(g0, g1, g2, g3, 2);
      t0 += dppf<0x128>(t0); t1 += dppf<0x128>(t1);
      u32 t = pk2(t0, t1);
      if ((lane >> 4) == 1) t = pk2(1.f + bflo(t), 1.f + bfhi(t));
      if ((lane & 8) == 0) *(LAS u32*)(uintptr_t)(gs + (lane >> 4) * 32 + (lane & 7) * 4) = t;
      asm volatile("s_waitcnt lgkmcnt(0)" ::: "memory");
    }
    gav = lds16u(gs + 64 + (lane >> 5) * 16); gbv = lds16u(gs + 96 + (lane >> 5) * 16);
    if (bi < 28) {
      float u = 0.f, bs = 0.f;
#pragma unroll
      for (int h = 0; h < 2; ++h) {
        const u32x4 wv = __builtin_bit_cast(u32x4, ldtr(ta + h * 1024)), sb = lds16u(gs + h * 16), cb = lds16u(gs + 32 + h * 16);
        u32x4 e;
#pragma unroll
        for (int j = 0; j < 4; ++j) {
          e[j] = pk2(bflo(wv[j]) * bflo(cb[j]), bfhi(wv[j]) * bfhi(cb[j]));
          u += bflo(e[j]) + bfhi(e[j]);
          bs = __builtin_fmaf(bflo(wv[j]), bflo(sb[j]), __builtin_fmaf(bfhi(wv[j]), bfhi(sb[j]), bs));
        }
        st16_sc1(e, rws, wdst + h * 512 + (u32)(wq * 8 + w) * 7168);
      }
      sts4f(SC2 + lane * 32 + w * 4, u); sts4f(SC2 + 2048 + lane * 32 + w * 4, bs);
    }
    STAMP(5);
    poll(pollq, rws, tag);
    STAMP(6);
    const u32 src = (u32)(head2 * 64 + 8 * range + w) * 1024;
    const rsrc_t rh = __builtin_amdgcn_make_buffer_rsrc(ws, 0, range > 0 ? WS_BYTES : 0, 0x00020000);
    dma_sc1((w + 8) * 1024, lv, rws, WS_K + src);
    ld16_sc1(q, lv, rws, WS_Q + src);
    dma_sc1(w * 1024, lv, rh, WS_K + src - 8192);
    dma_sc1(16384 + (w + 8) * 1040, lv, rws, WS_V + src);
    dma_sc1(16384 + w * 1040, lv, rh, WS_V + src - 8192);
  }
  {
    asm volatile("s_waitcnt vmcnt(0) lgkmcnt(0)\n\ts_barrier" : "+v"(q)::"memory");
    STAMP(7);
    if (bi < 28 && w == 7) {
      post(postw + MB_W * 4, rws, tag);
      const float t = sum8(lds16f(SC2 + lane * 32), lds16f(SC2 + lane * 32 + 16)), o = sum8(lds16f(SC2 + 2048 + lane * 32), lds16f(SC2 + 2048 + lane * 32 + 16));
      const float uh = rbf(t), ul = t - uh, bh = rbf(o), bl = o - bh;
      st16_sc1((u32x4){pk2(uh, ul), pk2(uh, bh), pk2(bl, bh), 0u}, rws, wdst + (wq & 1) * 512 + (u32)(32 + (wq >> 1)) * 7168);
    }
    const u32x4 ov = attn_wave(range > 0, w, lane, BF8(q), mk, ms, kb, vb0, vb1, PRE_ARGS,
                               pv, rwo, (u32)n64 | (u32)(bi & 3) << 9);
    st16_sc1(ov, rws, odst);
    STAMP(8);
    u32 fw;
    ld4_sc1(fw, pollw, rws, 0);
    {
#pragma unroll
      for (int c = 0; c < 2; ++c) tb[c] = (u32)(8 * (lane >> 5) + (fr >> 2)) * 128 + ((((2 * c + (g & 1) + ksl) & 3) ^ ((fr >> 2) & 2)) * 32) + (lane & 3) * 8;
      xdst3 = WS_X1 + (u32)(tile3 * 32 + n64 * 4 + ksl) * 1024 + lv;
      adst = WS_ACT + (u32)((4 * m128 + hn) * 112 + nt * 7 + (w & 4)) * 1024 + lv;
      postx = lane < 16 ? mbox(((m64 >> 4) << 2) | (lane >> 2), ((lane & 3) << 3) | ((m64 >> 1) & 7), MB_X1 + (m64 & 1) * 8 + n64) : NOWHERE;
      posta = lane < 16 ? mbox(m128 >> 1, ((lane >> 1) << 2) | ((2 * m128 + (lane & 1)) & 3), MB_ACT + nt) : NOWHERE;
      pollo = mbox(xcd, bi, MB_O + ksl * 8 + (lane & 7));
      polla = mbox(xcd, bi, MB_ACT + ksl * 4 + (lane & 3));
#pragma unroll
      for (int j = 1; j < 4; ++j) rbw[j - 1] = (mh ? 36864 : 65536) + (u32)(((ksl + j) & 3) * 3 + j - 1) * 2048 + lv;
      rbr = (mh ? 36864 : 65536) + (u32)ksl * 6144 + lv;
      wl = lv + (w & 4) * 1024; xl = lv + 114688 + hn * 4096;
      stv = (u32)(lane >> 5) * 2048 + (lane & 31) * 16;
      PIN(tb[0]); PIN(tb[1]); PIN(xdst3); PIN(adst); PIN(postx); PIN(posta); PIN(pollo); PIN(polla); PIN(rbw[0]); PIN(rbw[1]); PIN(rbw[2]); PIN(rbr); PIN(wl); PIN(xl); PIN(stv);
    }
    asm volatile("s_waitcnt vmcnt(0) lgkmcnt(0)\n\ts_barrier" : "+v"(fw)::"memory");
    if (w == 0) post(posto, rws, tag);
    if (bi < 28 && w == 7) post(postw + MB_E * 4, rws, tag);
    STAMP(21);
    if (!__all(fw == tag)) poll(pollw, rws, tag);
#pragma unroll
    for (int i = 0; i < 4; ++i) if (i < 3 || w < 4) dma_sc1(w * 1024 + i * 8192, lv, rws, WS_WP + (u32)nt * WPN + ((2 * nt + m128) & 7) * 28672 + w * 1024 + i * 8192);
    bf16x8 wf[8][2];
#pragma unroll
    for (int ks = 0; ks < 8; ++ks)
#pragma unroll
      for (int c = 0; c < 2; ++c) wf[ks][c] = ldtr(98304 + ksl * 16384 + tb[c] + ks * 2048);
    if (b == 255 && w == 1) {
#pragma unroll 1
      for (int i = 0; i < 4; ++i) poll(WS_ARR + i * 256 + lane * 4, rws, tag);
      if (lane == 0) __hip_atomic_store((GAS u32*)(ws + WS_SEED), tag, RLX_AGENT);
    }
    poll(pollo, rws, tag);
    STAMP(9);
    u32x4 of[8];
#pragma unroll
    for (int ks = 0; ks < 8; ++ks) ld16_sc1(of[ks], lv, rws, WS_O + (u32)(tile3 * 32 + ksl * 8 + (ks ^ n64)) * 1024);
    f32x16 acc[2] = {};
#pragma unroll
    for (int ks = 0; ks < 8; ++ks) {
      WAITV1(7 - ks, of[ks]);
      acc[0] = MFMA32(wf[ks][0], BF8(of[ks]), acc[0]);
      acc[1] = MFMA32(wf[ks][1], BF8(of[ks]), acc[1]);
      PIN(acc[0]); PIN(acc[1]);
    }
#pragma unroll
    for (int j = 1; j < 4; ++j) {
      const u32 a = rbw[j - 1];
      const int o = (j & 1) * 8;
      sts16f(a, (f32x4){acc[j >> 1][o], acc[j >> 1][o + 1], acc[j >> 1][o + 2], acc[j >> 1][o + 3]});
      sts16f(a + 1024, (f32x4){acc[j >> 1][o + 4], acc[j >> 1][o + 5], acc[j >> 1][o + 6], acc[j >> 1][o + 7]});
    }
    LBAR();
    float y[8];
#pragma unroll
    for (int e = 0; e < 8; ++e) y[e] = acc[0][e];
#pragma unroll
    for (int j = 0; j < 3; ++j) {
      const f32x4 a = lds16f(rbr + j * 2048), d = lds16f(rbr + j * 2048 + 1024);
#pragma unroll
      for (int e = 0; e < 4; ++e) { y[e] += a[e]; y[4 + e] += d[e]; }
    }
    x1v = resid_epi(y, xres, gav);
    st16_sc1(x1v, rws, xdst3);
    STAMP(10);
    {
      float s = 0.f, q2 = 0.f;
#pragma unroll
      for (int e = 0; e < 4; ++e) { const float a = bflo(x1v[e]), c = bfhi(x1v[e]); s += a + c; q2 = __builtin_fmaf(a, a, __builtin_fmaf(c, c, q2)); }
      swap32f(s, q2);
      sts4f(SC3 + (u32)(mh * 1024 + ksl * 256 + (lane & 31) * 8 + (lane >> 5) * 4), s + q2);
    }
    WAITBAR(0);
    if (w == 2) post(postx, rws, tag);
    if (ksl == 0) {
      const u32 a = SC3 + (u32)(mh * 1024 + (lane & 31) * 8 + (lane >> 5) * 4);
      const float t = (lds4f(a) + lds4f(a + 256)) + (lds4f(a + 512) + lds4f(a + 768));
      st_gran(ws, WS_ST + (u32)(tile3 * 8 + n64) * 512 + (lane & 31) * 16 + (lane >> 5) * 8, t, tag);
      asm volatile("s_waitcnt lgkmcnt(0)" ::: "memory");
    }
    STAMP(11);
  }
  const u32 r5 = (u32)(((n64 * 7) >> 1) + 7 * (bi & 3)) % 28;
  u32 wh = wl + 57344;
  PIN(wh);
  if (w < 4) ffn_up<4>(w, lane, tag, ws, rws, rwd, pv, m128, nt, r5, pollx, wl, wh, xl, stv, adst, b DBGPASS);
  else ffn_up<3>(w, lane, tag, ws, rws, rwd, pv, m128, nt, r5, pollx, wl, wh, xl, stv, adst, b DBGPASS);
  STAMP(16);
  {
    u32 t0 = tb[0] + ksl * 40960, t1 = tb[1] + ksl * 40960;
    PIN(t0); PIN(t1);
    WAITBAR(0);
    if (w == 0) post(posta, rws, tag);
    bf16x8 wp[8][2];
#pragma unroll
    for (int s = 0; s < 8; ++s) { wp[s][0] = ldtr(t0 + s * 2048); wp[s][1] = ldtr(t1 + s * 2048); }
    LBAR();
#define DN_RF(i) dma((u32)((w >> 1) * 40 + (w & 1) * 8 + (i)) * 1024, pv, rwd, (u32)((w >> 1) * 448 + ((i) & 1) * 8 + rot28(20 + (w & 1) * 4 + ((i) >> 1), r5) * 16) * 1024)
    DN_RF(0); DN_RF(1); DN_RF(2); DN_RF(3);
    DN_RF(4); DN_RF(5); DN_RF(6); DN_RF(7);
    STAMP(15);
    poll(polla, rws, tag);
    STAMP(17);
    u32x4 af[28];
    const u32 asrc = WS_ACT + (u32)(tile3 * 112 + ksl * 28) * 1024;
#pragma unroll
    for (int ks = 0; ks < DEP; ++ks) ld16_sc1(af[ks], lv, rws, asrc + rot28(ks, r5) * 1024);
    f32x16 acc[2] = {};
    bf16x8 wa0, wa1, wb0, wb1;
#define DN_PG(s) ((s) < 20 ? (s) * 2048 : ((s) - 20) * 2048)
#define DN_RD(A, B, s) do { A = ldtr(t0 + DN_PG(s)); B = ldtr(t1 + DN_PG(s)); } while (0)
#define DN_MM(A, B, ks) do { if ((ks) + DEP < 28) ld16_sc1(af[(ks) + DEP < 28 ? (ks) + DEP : 0], lv, rws, asrc + rot28((ks) + DEP, r5) * 1024); \
      WAITV1((ks) + DEP < 28 ? DEP : 27 - (ks), af[ks]); \
      acc[0] = MFMA32(A, BF8(af[ks]), acc[0]); PIN(acc[0]); acc[1] = MFMA32(B, BF8(af[ks]), acc[1]); PIN(acc[1]); SBAR(); } while (0)
#pragma unroll
    for (int ks = 0; ks < 7; ++ks) DN_MM(wp[ks][0], wp[ks][1], ks);
    DN_RD(wa0, wa1, 8); DN_MM(wp[7][0], wp[7][1], 7);
#pragma unroll
    for (int ks = 8; ks < 28; ks += 2) {
      DN_RD(wb0, wb1, ks + 1); DN_MM(wa0, wa1, ks);
      if (ks == 18) LBAR();
      if (ks + 2 < 28) DN_RD(wa0, wa1, ks + 2);
      DN_MM(wb0, wb1, ks + 1);
    }
    STAMP(18);
    const u32 sb = (u32)ksl * 40960 + 16384 + mh * 6144 + lv;
#pragma unroll
    for (int j = 1; j < 4; ++j) {
      const int o = (j & 1) * 8;
      sts16f(sb + (j - 1) * 2048, (f32x4){acc[j >> 1][o], acc[j >> 1][o + 1], acc[j >> 1][o + 2], acc[j >> 1][o + 3]});
      sts16f(sb + (j - 1) * 2048 + 1024, (f32x4){acc[j >> 1][o + 4], acc[j >> 1][o + 5], acc[j >> 1][o + 6], acc[j >> 1][o + 7]});
    }
    LBAR();
    float y[8];
#pragma unroll
    for (int e = 0; e < 8; ++e) y[e] = acc[0][e];
#pragma unroll
    for (int j = 1; j < 4; ++j) {
      const u32 a = (u32)((ksl - j) & 3) * 40960 + 16384 + mh * 6144 + (j - 1) * 2048 + lv;
      const f32x4 c = lds16f(a), d = lds16f(a + 1024);
#pragma unroll
      for (int e = 0; e < 4; ++e) { y[e] += c[e]; y[4 + e] += d[e]; }
    }
    const u32x4 ov = resid_epi(y, x1v, gbv);
    __builtin_amdgcn_raw_buffer_store_b128(ov, rout, xro, 0, 0);
    STAMP(19);
    STAMP_END();
  }
}

extern "C" void kernel_launch(void* const* d_in, const int* in_sizes, int n_in, void* d_out, int out_size, void* d_ws, size_t ws_size,
                              hipStream_t stream) {
  fused_block<<<256, 512, 0, stream>>>(d_in[0], d_in[1], d_in[2], d_in[4], d_in[5], d_in[6], d_in[7], d_in[8], d_out, (char*)d_ws);
}
```

```cpp
#include <hip/hip_runtime.h>
#include <stdint.h>

typedef unsigned u32;
typedef unsigned long long u64;
typedef __attribute__((ext_vector_type(2))) u32 u32x2;
typedef __attribute__((ext_vector_type(4))) u32 u32x4;
typedef __attribute__((ext_vector_type(2))) float f32x2;
typedef __attribute__((ext_vector_type(4))) float f32x4;
typedef __attribute__((ext_vector_type(16))) float f32x16;
typedef __attribute__((ext_vector_type(8))) short bf16x8;
typedef __attribute__((ext_vector_type(2))) __bf16 bf16x2;
typedef __amdgpu_buffer_rsrc_t rsrc_t;
#define LAS __attribute__((address_space(3)))
#define GAS __attribute__((address_space(1)))
#define DI __device__ __forceinline__
#define RLX_AGENT __ATOMIC_RELAXED, __HIP_MEMORY_SCOPE_AGENT

constexpr u32 WS_SEED = 0;
constexpr u32 WS_ARR = 4096;
constexpr u32 WS_MODL = 65536;
constexpr u32 WS_MODC = 524288;
constexpr u32 WS_MB = 262144;
constexpr u32 MB_QKV = 0, MB_O = 32, MB_X1 = 64, MB_E = 96, MB_W = 128, MB_ACT = 160;
constexpr u32 WS_ST = 786432;
constexpr u32 WS_Q = 1u << 20, WS_K = 3u << 20, WS_V = 5u << 20;
constexpr u32 WS_O = 7u << 20;
constexpr u32 WS_X1 = 9u << 20;
constexpr u32 WS_ACT = 11u << 20;
constexpr u32 WS_WP = 18u << 20;
constexpr u32 WPN = 34 * 7168;
constexpr u32 WS_BYTES = 22u << 20;
constexpr u32 WS_DBG = 24u << 20;
#define DEP 10
#define PRE_ARGS rws, WS_WP + (u32)nt * WPN + (u32)((bi & 7) * 30 + w) * 1024
constexpr u32 SPIN_CAP = 1u << 15;
#define START_DELAY 8

DI float bflo(u32 w) { return __uint_as_float(w << 16); }
DI float bfhi(u32 w) { return __uint_as_float(w & 0xffff0000u); }
DI u32 pk2(float a, float b) { f32x2 v = {a, b}; return __builtin_bit_cast(u32, __builtin_convertvector(v, bf16x2)); }
DI float rbf(float a) { return bflo(pk2(a, a)); }
template <int CTRL> DI float dppf(float x) { return __builtin_bit_cast(float, __builtin_amdgcn_update_dpp(0, __builtin_bit_cast(int, x), CTRL, 0xf, 0xf, true)); }
DI void swap32f(float& a, float& b) { auto r = __builtin_amdgcn_permlane32_swap(__float_as_uint(a), __float_as_uint(b), false, false); a = __uint_as_float(r[0]); b = __uint_as_float(r[1]); }
DI void swap16f(float& a, float& b) { auto r = __builtin_amdgcn_permlane16_swap(__float_as_uint(a), __float_as_uint(b), false, false); a = __uint_as_float(r[0]); b = __uint_as_float(r[1]); }
DI void swap32u(u32& a, u32& b) { auto r = __builtin_amdgcn_permlane32_swap(a, b, false, false); a = r[0]; b = r[1]; }
DI void swap16u(u32& a, u32& b) { auto r = __builtin_amdgcn_permlane16_swap(a, b, false, false); a = r[0]; b = r[1]; }
DI float row16_sum(float x) { x += dppf<0xB1>(x); x += dppf<0x4E>(x); x += dppf<0x141>(x); x += dppf<0x140>(x); return x; }
DI float rdlane(float x, int l) { return __uint_as_float(__builtin_amdgcn_readlane(__float_as_uint(x), l)); }

#define MFMA16(a, b, c) __builtin_amdgcn_mfma_f32_16x16x32_bf16((a), (b), (c), 0, 0, 0)
#define MFMA32(a, b, c) __builtin_amdgcn_mfma_f32_32x32x16_bf16((a), (b), (c), 0, 0, 0)
#define BF8(x) __builtin_bit_cast(bf16x8, (x))
#define WAITV(n) asm volatile("s_waitcnt vmcnt(%0)" ::"i"(n) : "memory")
#define WAITBAR(n) asm volatile("s_waitcnt vmcnt(%0) lgkmcnt(0)\n\ts_barrier" ::"i"(n) : "memory")
#define LBAR() asm volatile("s_waitcnt lgkmcnt(0)\n\ts_barrier" ::: "memory")
#define SBAR() __builtin_amdgcn_sched_barrier(0)
#define NOP_ do {} while (0)

DI void dma(u32 lds, u32 voff, rsrc_t rs, u32 soff) {
  asm volatile("s_mov_b32 m0, %0\n\ts_nop 0\n\tbuffer_load_dwordx4 %1, %2, %3 offen lds" ::"s"(lds), "v"(voff), "s"(rs), "s"(soff) : "memory");
}
DI void dma_sc1(u32 lds, u32 voff, rsrc_t rs, u32 soff) {
  asm volatile("s_mov_b32 m0, %0\n\ts_nop 0\n\tbuffer_load_dwordx4 %1, %2, %3 offen sc1 lds" ::"s"(lds), "v"(voff), "s"(rs), "s"(soff) : "memory");
}
DI void dma4(u32 lds, u32 voff, rsrc_t rs, u32 soff) {
  asm volatile("s_mov_b32 m0, %0\n\ts_nop 0\n\tbuffer_load_dword %1, %2, %3 offen lds" ::"s"(lds), "v"(voff), "s"(rs), "s"(soff) : "memory");
}
DI void ld16(u32x4& r, u32 voff, rsrc_t rs, u32 soff) { asm volatile("buffer_load_dwordx4 %0, %1, %2, %3 offen" : "=v"(r) : "v"(voff), "s"(rs), "s"(soff) : "memory"); }
DI void ld16_sc1(u32x4& r, u32 voff, rsrc_t rs, u32 soff) { asm volatile("buffer_load_dwordx4 %0, %1, %2, %3 offen sc1" : "=v"(r) : "v"(voff), "s"(rs), "s"(soff) : "memory"); }
DI void ld8_sc1(u32x2& r, u32 voff, rsrc_t rs, u32 soff) { asm volatile("buffer_load_dwordx2 %0, %1, %2, %3 offen sc1" : "=v"(r) : "v"(voff), "s"(rs), "s"(soff) : "memory"); }
DI void ld4(u32& r, u32 voff, rsrc_t rs, u32 soff) { asm volatile("buffer_load_dword %0, %1, %2, %3 offen" : "=v"(r) : "v"(voff), "s"(rs), "s"(soff) : "memory"); }
DI void ld2(u32& r, u32 voff, rsrc_t rs, u32 soff) { asm volatile("buffer_load_ushort %0, %1, %2, %3 offen" : "=v"(r) : "v"(voff), "s"(rs), "s"(soff) : "memory"); }
#define WAITV1(n, a) asm volatile("s_waitcnt vmcnt(%1)" : "+v"(a) : "i"(n) : "memory")
#define PIN(a) asm volatile("" : "+v"(a))
DI void st16_sc1(u32x4 v, rsrc_t rs, u32 voff) { __builtin_amdgcn_raw_buffer_store_b128(v, rs, voff, 0, 16); }

DI bf16x8 lds16(u32 a) { return *(const LAS bf16x8*)(uintptr_t)a; }
DI u32x4 lds16u(u32 a) { return *(const LAS u32x4*)(uintptr_t)a; }
DI f32x4 lds16f(u32 a) { return *(const LAS f32x4*)(uintptr_t)a; }
DI f32x2 lds8f(u32 a) { return *(const LAS f32x2*)(uintptr_t)a; }
DI float lds4f(u32 a) { return *(const LAS float*)(uintptr_t)a; }
DI void sts16(u32 a, u32x4 v) { *(LAS u32x4*)(uintptr_t)a = v; }
DI void sts16f(u32 a, f32x4 v) { *(LAS f32x4*)(uintptr_t)a = v; }
DI void sts8f(u32 a, f32x2 v) { *(LAS f32x2*)(uintptr_t)a = v; }
DI void sts4f(u32 a, float v) { *(LAS float*)(uintptr_t)a = v; }
typedef short v4i16_t __attribute__((ext_vector_type(4)));
DI bf16x8 ldtr(u32 a) {
  v4i16_t lo = __builtin_amdgcn_ds_read_tr16_b64_v4i16((LAS v4i16_t*)(uintptr_t)a);
  v4i16_t hi = __builtin_amdgcn_ds_read_tr16_b64_v4i16((LAS v4i16_t*)(uintptr_t)(a + 512));
  return __builtin_shufflevector(lo, hi, 0, 1, 2, 3, 4, 5, 6, 7);
}

DI void poll(u32 voff, rsrc_t rws, u32 tag) {
#pragma unroll 1
  for (u32 spins = 0; spins < SPIN_CAP; ++spins) {
    u32 v;
    asm volatile("buffer_load_dword %0, %1, %2, 0 offen sc1\n\ts_waitcnt vmcnt(0)" : "=v"(v) : "v"(voff), "s"(rws) : "memory");
    if (__all(v == tag)) break;
    __builtin_amdgcn_s_sleep(1);
  }
}
DI void post(u32 voff, rsrc_t rws, u32 tag) { __builtin_amdgcn_raw_buffer_store_b32(tag, rws, voff, 0, 16); }
constexpr u32 NOWHERE = 0x7ffffff0u;
DI u32 mbox(int xcd, int bi, u32 word) { return WS_MB + (u32)(bi * 8 + xcd) * 1024 + word * 4; }
DI bool tags_ok(u32x4 a, u32x4 b, u32 tag) { return a[1] == tag && a[3] == tag && b[1] == tag && b[3] == tag; }
DI float gsum(u32x4 a, u32x4 b, u32x4 c, u32x4 d, int i) { return (__uint_as_float(a[i]) + __uint_as_float(b[i])) + (__uint_as_float(c[i]) + __uint_as_float(d[i])); }
DI void st_gran(char* ws, u32 byte, float v, u32 tag) { __hip_atomic_store((GAS u64*)(ws + byte), ((u64)tag << 32) | __float_as_uint(v), RLX_AGENT); }
DI void ld4_sc1(u32& r, u32 voff, rsrc_t rs, u32 soff) { asm volatile("buffer_load_dword %0, %1, %2, %3 offen sc1" : "=v"(r) : "v"(voff), "s"(rs), "s"(soff) : "memory"); }
#define LD4X(a, b, c, d, voff, base, step) do { ld16_sc1(a, voff, rws, base); ld16_sc1(b, voff, rws, (base) + (step)); ld16_sc1(c, voff, rws, (base) + 2 * (step)); ld16_sc1(d, voff, rws, (base) + 3 * (step)); } while (0)
#define WAIT4X(n, a, b, c, d) asm volatile("s_waitcnt vmcnt(%4)" : "+v"(a), "+v"(b), "+v"(c), "+v"(d) : "i"(n) : "memory")

#define STAMP(i)
#define STAMP_END()
#define DBGARG
#define DBGPASS

DI float silu_bf(float c) { return rbf(c * __builtin_amdgcn_rcpf(1.f + __builtin_amdgcn_exp2f(c * -1.4426950408889634f))); }
DI float sum8(f32x4 a, f32x4 d) { return ((a[0] + a[1]) + (a[2] + a[3])) + ((d[0] + d[1]) + (d[2] + d[3])); }

constexpr u64 upper_mask(int e) {
  u64 m = 0;
  for (int l = 0; l < 64; ++l) if ((e & 3) + 8 * (e >> 2) + 4 * (l >> 5) > (l & 31)) m |= 1ull << l;
  return m;
}
template <int E> DI bool upper() { constexpr u64 m = upper_mask(E); return __builtin_amdgcn_inverse_ballot_w64(m); }
#define MX3(a, b, c) __builtin_fmaxf(__builtin_fmaxf((a), (b)), (c))
#define EX2(x) __builtin_amdgcn_exp2f(x)
template <int I> DI void sel_fill(f32x16& sel, const f32x16& sf, const f32x16& sd) {
  if constexpr (I < 16) { sel[I] = upper<I>() ? sf[I] : sd[I]; sel_fill<I + 1>(sel, sf, sd); }
}
#define WP_PRE(i) dma(40960 + w * 1024, lane * 16, rws, psrc + (i) * 8192)
#define PV(V, P) do { o = MFMA32(V, BF8(P), o); PIN(o); } while (0)
DI float half_max(const f32x16& s) {
  float m0 = __builtin_fmaxf(s[0], s[1]), m1 = __builtin_fmaxf(s[2], s[3]);
#pragma unroll
  for (int e = 4; e < 16; e += 4) { m0 = MX3(m0, s[e], s[e + 1]); m1 = MX3(m1, s[e + 2], s[e + 3]); }
  return __builtin_fmaxf(m0, m1);
}
DI float both_max(float m) { float mo = m; swap32f(m, mo); return __builtin_fmaxf(m, mo); }
DI void boundary_p(const f32x16& sel, float m, const u32 (&mk)[8], u32x4 (&pf)[2], u32x4 (&pd)[2]) {
#pragma unroll
  for (int e = 0; e < 8; ++e) {
    const u32 p = pk2(EX2(sel[2 * e] - m), EX2(sel[2 * e + 1] - m));
    pf[e >> 2][e & 3] = p & mk[e];
    pd[e >> 2][e & 3] = p & ~mk[e];
  }
}
DI f32x16 attn_exact(int ilo, bf16x8 qf, const u32 (&mk)[8], u32 kb, u32 vb0, u32 vb1) {
  const f32x16 zero = {};
  f32x16 sel, sf, sd = MFMA32(lds16(kb + 8192), qf, zero);
  if (ilo == 0) sf = MFMA32(lds16(kb), qf, zero);
  else {
#pragma unroll
    for (int e = 0; e < 16; ++e) sf[e] = -INFINITY;
  }
  sel_fill<0>(sel, sf, sd);
  float m = half_max(sel);
#pragma unroll 1
  for (int i = ilo > 1 ? ilo : 1; i < 8; ++i) m = __builtin_fmaxf(m, half_max(MFMA32(lds16(kb + i * 1024), qf, zero)));
  m = both_max(m);
  f32x16 negm;
#pragma unroll
  for (int e = 0; e < 16; ++e) negm[e] = -m;
  u32x4 pf[2], pd[2];
  boundary_p(sel, m, mk, pf, pd);
  f32x16 o = MFMA32(lds16(vb0 + 8 * 1040), BF8(pd[0]), zero);
  o = MFMA32(lds16(vb1 + 8 * 1040), BF8(pd[1]), o);
  if (ilo == 0) { o = MFMA32(lds16(vb0), BF8(pf[0]), o); o = MFMA32(lds16(vb1), BF8(pf[1]), o); }
#pragma unroll 1
  for (int i = ilo > 1 ? ilo : 1; i < 8; ++i) {
    const f32x16 sc = MFMA32(lds16(kb + i * 1024), qf, negm);
    u32x4 p0, p1;
#pragma unroll
    for (int e = 0; e < 4; ++e) { p0[e] = pk2(EX2(sc[2 * e]), EX2(sc[2 * e + 1])); p1[e] = pk2(EX2(sc[8 + 2 * e]), EX2(sc[8 + 2 * e + 1])); }
    o = MFMA32(lds16(vb0 + i * 1040), BF8(p0), o);
    o = MFMA32(lds16(vb1 + i * 1040), BF8(p1), o);
  }
  return o;
}
DI f32x16 attn_fast(int w, int lane, bf16x8 qf, const u32 (&mk)[8], const u32 (&ms)[16], u32 kb, u32 vb0, u32 vb1, rsrc_t rws, u32 psrc,
                    u32 pv, rsrc_t rwo, u32 r3) {
#define WO_DMA(i) dma(98304 + ((((i) ^ (r3 >> 8)) * 8 + w) * 1024), pv, rwo, (u32)((((i) ^ (r3 >> 8)) * 8 + w) ^ ((r3 & 7) << 1)) * 8192)
#define TWO(a, b) do { a; b; } while (0)
  const f32x16 zero = {};
  bf16x8 k8 = lds16(kb + 8192), k0 = lds16(kb), ka = lds16(kb + 1024), kc = lds16(kb + 2048);
  bf16x8 va = lds16(vb0 + 8 * 1040), vc = lds16(vb1 + 8 * 1040), vd = lds16(vb0), ve = lds16(vb1);
  SBAR();
  f32x16 sa = MFMA32(k8, qf, zero);
  PIN(sa); WO_DMA(0); SBAR();
  const float m = both_max(half_max(sa));
  f32x16 negm;
#pragma unroll
  for (int e = 0; e < 16; ++e) negm[e] = -m;
  PIN(negm); SBAR();
  f32x16 sd = MFMA32(k8, qf, negm), sb = MFMA32(k0, qf, negm);
  sa = MFMA32(ka, qf, negm);
  PIN(sd); PIN(sb); PIN(sa); WO_DMA(1); SBAR();
  f32x16 o = zero;
  u32x4 pd[2], pf[2], p0, p1;
#pragma unroll
  for (int e = 0; e < 8; ++e) {
    const float x0 = __uint_as_float((__float_as_uint(sb[2 * e]) & ms[2 * e]) | (__float_as_uint(sd[2 * e]) & ~ms[2 * e]));
    const float x1 = __uint_as_float((__float_as_uint(sb[2 * e + 1]) & ms[2 * e + 1]) | (__float_as_uint(sd[2 * e + 1]) & ~ms[2 * e + 1]));
    const u32 p = pk2(EX2(x0), EX2(x1));
    pf[e >> 2][e & 3] = p & mk[e];
    pd[e >> 2][e & 3] = p & ~mk[e];
  }
  PIN(pf[0]); PIN(pf[1]); PIN(pd[0]); PIN(pd[1]); WO_DMA(2); SBAR();
#define AT_Q(P, S, o_) do { P[(o_) & 3] = pk2(EX2(S[2 * (o_)]), EX2(S[2 * (o_) + 1])); P[((o_) & 3) + 1] = pk2(EX2(S[2 * (o_) + 2]), EX2(S[2 * (o_) + 3])); PIN(P); SBAR(); } while (0)
#define AT_STEP(S, N, KN, KR, i, M0, M1, M2, M3) do { \
    if ((i) < 7) { N = MFMA32(KN, qf, negm); PIN(N); if ((i) < 6) KR = lds16(kb + ((i) + 2) * 1024); SBAR(); } \
    u32x4 q0_, q1_; \
    AT_Q(q0_, S, 0); M0; SBAR(); AT_Q(q0_, S, 2); M1; SBAR(); AT_Q(q1_, S, 4); M2; SBAR(); AT_Q(q1_, S, 6); M3; \
    va = lds16(vb0 + (i) * 1040); vc = lds16(vb1 + (i) * 1040); p0 = q0_; p1 = q1_; SBAR(); } while (0)
  AT_STEP(sa, sb, kc, ka, 1, PV(va, pd[0]), PV(vc, pd[1]), PV(vd, pf[0]), PV(ve, pf[1]));
  AT_STEP(sb, sa, ka, kc, 2, WP_PRE(0), PV(va, p0), PV(vc, p1), WO_DMA(3));
  AT_STEP(sa, sb, kc, ka, 3, WP_PRE(1), PV(va, p0), PV(vc, p1), WO_DMA(4));
  AT_STEP(sb, sa, ka, kc, 4, WP_PRE(2), PV(va, p0), PV(vc, p1), WO_DMA(5));
  AT_STEP(sa, sb, kc, ka, 5, WP_PRE(3), PV(va, p0), PV(vc, p1), WO_DMA(6));
  AT_STEP(sb, sa, ka, kc, 6, WO_DMA(7), PV(va, p0), PV(vc, p1), NOP_);
  AT_STEP(sa, sb, kc, ka, 7, NOP_, PV(va, p0), PV(vc, p1), NOP_);
  PV(va, p0); PV(vc, p1);
  return o;
}
DI u32x4 attn_wave(bool full, int w, int lane, bf16x8 qf, const u32 (&mk)[8], const u32 (&ms)[16], u32 kb, u32 vb0, u32 vb1, rsrc_t rws, u32 psrc,
                   u32 pv, rsrc_t rwo, u32 r3) {
  f32x16 o = attn_fast(w, lane, qf, mk, ms, kb, vb0, vb1, rws, psrc, pv, rwo, r3);
  if (!__all(o[8] < 1.8446744e19f && o[8] > 5.4210109e-20f))
    o = attn_exact(full ? 0 : 8 - w, qf, mk, kb, vb0, vb1);
  const float rl = __builtin_amdgcn_rcpf(o[8]);
  u32 a0 = pk2(o[0] * rl, o[1] * rl), a1 = pk2(o[2] * rl, o[3] * rl), b0 = pk2(o[4] * rl, o[5] * rl), b1 = pk2(o[6] * rl, o[7] * rl);
  swap32u(a0, b0); swap32u(a1, b1);
  return (u32x4){a0, a1, b0, b1};
}

DI u32x4 resid_epi(float (&y)[8], u32x4 res, u32x4 gate) {
#pragma unroll
  for (int i = 0; i < 4; ++i) swap32f(y[i], y[4 + i]);
  u32x4 o;
#pragma unroll
  for (int i = 0; i < 4; ++i) {
    const u32 yb = pk2(y[2 * i], y[2 * i + 1]);
    const u32 tb = pk2(bflo(yb) * bflo(gate[i]), bfhi(yb) * bfhi(gate[i]));
    o[i] = pk2(bflo(res[i]) + bflo(tb), bfhi(res[i]) + bfhi(tb));
  }
  return o;
}

DI u32 rot28(u32 s, u32 r) { const u32 t = s + r; return t >= 28 ? t - 28 : t; }
#define WD_DMA(base) dma((u32)((base) + w) * 1024, pv, rwd, (u32)(((base) / 40) * 448 + (w & 1) * 8 + rot28((((base) % 40) + w) >> 1, r5) * 16) * 1024)

template <int NF>
DI void ffn_up(int w, int lane, u32 tag, char* ws, rsrc_t rws, rsrc_t rwd, u32 pv, int m128, int nt, u32 r5, u32 pollx, u32 wl, u32 wh, u32 xl, u32 stv, u32 adst, int b DBGARG) {
  constexpr int NW = NF;
  constexpr int F0 = NF == 4 ? 0 : 4;
  const int p = w & 3, tile = 4 * m128 + p;
  const u32 lv = lane * 16;
  const u32 wsrc = WS_WP + (u32)nt * WPN + w * 1024;
  const u32 xsrc = WS_X1 + (u32)(tile * 32 + 2 * (w >> 2)) * 1024;
  const u32 xdst = 114688 + (p * 4 + 2 * (w >> 2)) * 1024;
  const u32 r4 = (2 * nt + m128) & 7;
#define UP_W1(g, i) dma_sc1(((g) & 3) * 28672 + w * 1024 + (i) * 8192, lv, rws, wsrc + ((g) ^ r4) * 28672 + (i) * 8192)
#define UP_X1(g, i) dma_sc1(xdst + ((g) % 3) * 16384 + (i) * 1024, lv, rws, xsrc + ((g) ^ r4) * 4096 + (i) * 1024)
#pragma unroll
  for (int g = 1; g < 4; ++g)
#pragma unroll
    for (int i = 0; i < NW; ++i) UP_W1(g, i);
  STAMP(12);
  poll(pollx, rws, tag);
  STAMP(13);
#pragma unroll
  for (int g = 0; g < 3; ++g) { UP_X1(g, 0); UP_X1(g, 1); }
  f32x16 acc[NF];
#pragma unroll
  for (int f = 0; f < NF; ++f) acc[f] = (f32x16){};
  bf16x8 xa, wa[NF], xb, wb[NF];
  u32x4 s0, s1_, s2_, s3;
#define UP_RX(X, ks) X = lds16(xl + (((ks) >> 2) % 3) * 16384 + ((ks) & 3) * 1024)
#define UP_RW(W, f, ks) W[f] = lds16(((((ks) >> 2) & 2) ? wh : wl) + (((ks) >> 2) & 1) * 28672 + (((ks) & 3) * 7 + (f)) * 1024)
#define UP_STEP(XC, WC, XN, WN, ks, RD, MM, D0, D1) do { \
    if (RD) { UP_RX(XN, ks); UP_RW(WN, 0, ks); } SBAR(); if (MM) { acc[0] = MFMA32(WC[0], XC, acc[0]); PIN(acc[0]); } SBAR(); \
    if (RD) { UP_RW(WN, 1, ks); UP_RW(WN, 2, ks); } SBAR(); if (MM) { acc[1] = MFMA32(WC[1], XC, acc[1]); PIN(acc[1]); } SBAR(); \
    if (RD && NF == 4) UP_RW(WN, NF - 1, ks); D0; SBAR(); if (MM) { acc[2] = MFMA32(WC[2], XC, acc[2]); PIN(acc[2]); } SBAR(); \
    D1; if (MM && NF == 4) { acc[NF - 1] = MFMA32(WC[NF - 1], XC, acc[NF - 1]); PIN(acc[NF - 1]); } SBAR(); } while (0)
#define UP_WL(g) do { if (NF == 4) UP_W1(g, 3); } while (0)
#define UP_GROUP(g, MM0, A0, A1, B0, B1, C0, C1, E0, E1) do { \
    UP_STEP(xb, wb, xa, wa, 4 * (g), true, MM0, A0, A1); UP_STEP(xa, wa, xb, wb, 4 * (g) + 1, true, true, B0, B1); \
    UP_STEP(xb, wb, xa, wa, 4 * (g) + 2, true, true, C0, C1); UP_STEP(xa, wa, xb, wb, 4 * (g) + 3, true, true, E0, E1); } while (0)
#define UP_GREFILL(g, S0, S1) UP_GROUP(g, true, UP_X1((g) + 2, 0), UP_X1((g) + 2, 1), UP_W1((g) + 3, 0), S0, UP_W1((g) + 3, 1), UP_W1((g) + 3, 2), UP_WL((g) + 3), S1)
#define UP_E(i) dma_sc1(((i) * 8 + w) * 1024, lv, rws, WS_WP + (u32)nt * WPN + 32 * 7168 + ((i) * 8 + w) * 1024)
  WAITBAR(4); STAMP(24); UP_GROUP(0, false, NOP_, NOP_, NOP_, NOP_, NOP_, NOP_, NOP_, NOP_);
  WAITBAR(2); STAMP(25); UP_GREFILL(1, NOP_, NOP_);
  WAITBAR(NW + 2); STAMP(26); UP_GREFILL(2, NOP_, NOP_);
  WAITBAR(2 * NW + 2); STAMP(27); UP_GREFILL(3, NOP_, LD4X(s0, s1_, s2_, s3, stv, WS_ST + (u32)tile * 4096, 512));
  WAITBAR(2 * NW + 6); STAMP(28); UP_GREFILL(4, NOP_, NOP_);
  WAITBAR(2 * NW + 6); STAMP(29);
  UP_GROUP(5, true, UP_X1(7, 0), UP_X1(7, 1), UP_E(0), NOP_, UP_E(1), NOP_, WD_DMA(16), NOP_);
  asm volatile("s_waitcnt vmcnt(%4) lgkmcnt(0)\n\ts_barrier" : "+v"(s0), "+v"(s1_), "+v"(s2_), "+v"(s3) : "i"(NW + 5) : "memory");
  STAMP(30);
#pragma unroll 1
  for (u32 spins = 0; spins < SPIN_CAP; ++spins) {
    if (__all(tags_ok(s0, s1_, tag) && tags_ok(s2_, s3, tag))) break;
    LD4X(s0, s1_, s2_, s3, stv, WS_ST + (u32)tile * 4096, 512);
    WAIT4X(0, s0, s1_, s2_, s3);
  }
  float s1 = gsum(s0, s1_, s2_, s3, 0), s2 = gsum(s0, s1_, s2_, s3, 2);
  { float t1 = s1, t2 = s2; swap32f(s1, t1); swap32f(s2, t2); s1 += t1; s2 += t2; }
  const float mu = s1 * (1.f / 512.f);
  const float var = __builtin_fmaxf(s2 * (1.f / 512.f) - mu * mu, 0.f) + 1e-5f;
  float rstd = __builtin_amdgcn_rsqf(var);
  const float sig = var * rstd;
  const float mh = rbf(-mu), ml = -mu - mh, sh = rbf(sig), sl = sig - sh;
  u32x4 ev = {pk2(mh, mh), pk2(ml, sh), pk2(sh, sl), 0u};
  PIN(ev); PIN(rstd); SBAR();
  UP_GROUP(6, true, WD_DMA(24), WD_DMA(32), WD_DMA(40), NOP_, WD_DMA(48), NOP_, WD_DMA(144), NOP_);
  WAITBAR(6); STAMP(31);
  UP_GROUP(7, true, WD_DMA(56), WD_DMA(64), WD_DMA(72), NOP_, WD_DMA(112), NOP_, WD_DMA(120), NOP_);
#pragma unroll
  for (int f = 0; f < NF; ++f) wa[f] = lds16(wl + f * 1024);
  UP_STEP(xb, wb, xa, wa, 0, false, true, NOP_, NOP_);
#pragma unroll
  for (int f = 0; f < NF; ++f) wb[f] = lds16(wl + 7168 + f * 1024);
#pragma unroll
  for (int f = 0; f < NF; ++f) { acc[f] = MFMA32(wa[f], BF8(ev), acc[f]); PIN(acc[f]); }
  LBAR();
  STAMP(14);
#pragma unroll
  for (int f = 0; f < NF; ++f) acc[f] = MFMA32(wb[f], BF8(ev), acc[f]);
  WD_DMA(0); WD_DMA(8); WD_DMA(80); WD_DMA(88);
  SBAR();
  constexpr float C1 = -2.f * 0.7978845608028654f * 1.4426950408889634f, C2 = C1 * 0.044715f;
  const float k1 = C1 * rstd, k3 = C2 * rstd * rstd * rstd;
#pragma unroll
  for (int f = 0; f < NF; ++f) {
    float a[8];
#pragma unroll
    for (int e = 0; e < 8; ++e) {
      const float g = acc[f][e];
      const float z = g * __builtin_fmaf(g * g, k3, k1);
      a[e] = (g * acc[f][8 + e]) * __builtin_amdgcn_rcpf(__builtin_fmaf(__builtin_amdgcn_exp2f(z), var, var));
    }
    u32 a0 = pk2(a[0], a[1]), a1 = pk2(a[2], a[3]), b0 = pk2(a[4], a[5]), b1 = pk2(a[6], a[7]);
    swap32u(a0, b0); swap32u(a1, b1);
    st16_sc1((u32x4){a0, a1, b0, b1}, rws, adst + f * 1024);
    if (f == 0) { WD_DMA(128); WD_DMA(96); }
    if (f == 1) { WD_DMA(104); WD_DMA(136); }
    if (f == 2) WD_DMA(152);
    SBAR();
  }

}

__global__ void __launch_bounds__(576, 3)
fused_block(const void* __restrict__ px, const void* __restrict__ pwqkv, const void* __restrict__ pwo, const void* __restrict__ pwg,
            const void* __restrict__ pwu, const void* __restrict__ pwd, const void* __restrict__ pc, const void* __restrict__ pwm,
            void* pout, char* ws) {
  __shared__ __attribute__((aligned(16))) char smem[163840];
  asm volatile("" ::"v"((u32)(uintptr_t)(LAS char*)smem));
  const int tid = threadIdx.x, lane = tid & 63, w = __builtin_amdgcn_readfirstlane(tid >> 6);
  const int b = blockIdx.x, xcd = b & 7, bi = b >> 3;
  if (w == 8) {
    if (bi == 0) { asm volatile("buffer_wbl2 sc0 sc1\n\ts_waitcnt vmcnt(0)" ::: "memory"); }
    return;
  }
  const rsrc_t rx = __builtin_amdgcn_make_buffer_rsrc((void*)px, 0, 2048 * 512 * 2, 0x00020000);
  const rsrc_t rqkv = __builtin_amdgcn_make_buffer_rsrc((void*)pwqkv, 0, 512 * 1536 * 2, 0x00020000);
  const rsrc_t rwo = __builtin_amdgcn_make_buffer_rsrc((void*)pwo, 0, 512 * 512 * 2, 0x00020000);
  const rsrc_t rwd = __builtin_amdgcn_make_buffer_rsrc((void*)pwd, 0, 1792 * 512 * 2, 0x00020000);
  const rsrc_t rout = __builtin_amdgcn_make_buffer_rsrc(pout, 0, 2048 * 512 * 2, 0x00020000);
  const rsrc_t rws = __builtin_amdgcn_make_buffer_rsrc(ws, 0, WS_BYTES, 0x00020000);
  const u32 lv = lane * 16;
  STAMP(0);

  const int m64a = 8 * (xcd & 3) + (bi & 7), grp = 4 * (xcd >> 2) + (bi >> 3);
  constexpr u32 SCR = 155648;
  const rsrc_t rc = __builtin_amdgcn_make_buffer_rsrc((void*)pc, 0, 512 * 2, 0x00020000);
  const rsrc_t rwm = __builtin_amdgcn_make_buffer_rsrc((void*)pwm, 0, 512 * 3072 * 2, 0x00020000);
  if (w == 0) {
    __builtin_amdgcn_s_setprio(2);
    u32 cv[4], seed; u32x4 m[4];
    asm volatile("s_load_dword %0, %1, 0x0" : "=s"(seed) : "s"(ws) : "memory");
    const u32 r0 = (u32)(bi >> 3) * 128 + (lane >> 1);
#pragma unroll
    for (int i = 0; i < 4; ++i) ld16(m[i], r0 * 6144 + (lane & 1) * 16, rwm, (u32)(xcd * 8 + (bi & 7)) * 32 + i * (32 * 6144));
#pragma unroll
    for (int i = 0; i < 4; ++i) ld2(cv[i], r0 * 2, rc, i * 64);
    asm volatile("s_waitcnt vmcnt(0) lgkmcnt(0)" : "+v"(cv[0]), "+v"(cv[1]), "+v"(cv[2]), "+v"(cv[3]), "+v"(m[0]), "+v"(m[1]), "+v"(m[2]), "+v"(m[3]), "+s"(seed)::"memory");
    float v[8] = {};
#pragma unroll
    for (int i = 0; i < 4; ++i) {
      const float sc = silu_bf(bflo(cv[i]));
#pragma unroll
      for (int e = 0; e < 4; ++e) { v[2 * e] = __builtin_fmaf(sc, bflo(m[i][e]), v[2 * e]); v[2 * e + 1] = __builtin_fmaf(sc, bfhi(m[i][e]), v[2 * e + 1]); }
    }
#pragma unroll
    for (int j = 0; j < 4; ++j) { swap32f(v[j], v[j + 4]); v[j] += v[j + 4]; }
#pragma unroll
    for (int j = 0; j < 2; ++j) { swap16f(v[j], v[j + 2]); v[j] += v[j + 2]; }
    const bool o = lane & 8;
    float t = (o ? v[1] : v[0]) + dppf<0x128>(o ? v[0] : v[1]);
    t += dppf<0x4E>(t);
    t += __builtin_bit_cast(float, __builtin_amdgcn_update_dpp(__builtin_amdgcn_update_dpp(0, __builtin_bit_cast(int, t), 0x104, 0xf, 0x5, false), __builtin_bit_cast(int, t), 0x114, 0xf, 0xa, false));
    const u32 a = WS_MODC + (u32)((lane >> 1) & 3) * 32768 + (u32)(bi >> 3) * 8192 + (u32)((xcd * 8 + (bi & 7)) * 16 + (lane & 1) * 8 + (lane >> 3)) * 8;
    st_gran(ws, a, t, seed + 1u); st_gran(ws, a + 131072, t, seed + 1u);
  }
  STAMP(1);
  u32 seed;
  asm volatile("s_load_dword %0, %1, 0x0" : "=s"(seed) : "s"(ws) : "memory");
  u32x4 xr[8];
#pragma unroll
  for (int j = 0; j < 8; ++j) ld16(xr[j], lv, rx, (u32)(m64a * 64 + w * 8 + j) * 1024);
  u32 cv1; u32x4 ml;
  {
    const u32 r1 = (u32)(bi >> 2) * 64 + w * 8 + (lane >> 3);
    ld2(cv1, r1 * 2, rc, 0);
    ld16(ml, r1 * 6144 + (lane & 7) * 16, rwm, 2048 + (u32)(xcd * 4 + (bi & 3)) * 128);
  }
  const u32 r1 = bi & 7;
  const int n64 = bi >> 2, m64 = 4 * xcd + (bi & 3);
  const int mh = w & 1, ksl = w >> 1, tile3 = 2 * m64 + mh;
  const int head2 = 4 * xcd + (bi & 3), range = bi >> 2;
  const int m128 = 8 * (xcd >> 2) + (bi & 7), nt = 4 * (xcd & 3) + (bi >> 3);
  const int wid = xcd * 28 + bi, wq = wid & 3, wmat = wid >= 112, wg = (wid >> 2) - wmat * 28;
  constexpr u32 SC2 = 53248, SC3 = 94208;
  u32 qv, ab[4], bl, bh, aw[8], pv, pg, gvo, xro, tb[2], mk[8], ms[16], kb, vb0, vb1, wdst, ta, qdst, odst, xdst3, adst;
  u32 postq, posto, postw, postx, posta, pollq, pollo, pollw, pollx, polla, rbw[3], rbr, wl, xl, stv;
  const int wm = w >> 2, hn = w & 3, fr = lane & 15, g = lane >> 4;
  {
    qv = (u32)(w * 8 + (lane >> 3)) * 3072 + (((lane & 7) ^ ((((lane >> 4) & 1) | ((w & 1) << 1)) << 1)) * 16) + grp * 128;
#pragma unroll
    for (int t = 0; t < 4; ++t) ab[t] = (u32)(wm * 32 + fr) * 1024 + ((g ^ (fr & 3)) * 16) + ((t ^ (fr >> 2)) * 64);
    bl = 65536 + (u32)(8 * g + (fr >> 2)) * 128 + ((hn ^ (((fr >> 3) & 1) | ((g & 1) << 1))) * 32) + (lane & 3) * 8; bh = bl + 49152;
#pragma unroll
    for (int j = 0; j < 8; ++j) aw[j] = (u32)(w * 8 + j) * 1024 + ((lane ^ (r1 << 3) ^ ((w * 8 + j) & 15)) * 16);
    pg = (u32)(lane >> 3) * 3584 + (((lane & 7) ^ (((lane >> 4) & 1) << 2)) * 16) + (u32)(wq * 128 + w * 16) * 3584 + wg * 128;
    gvo = (u32)((lane >> 3) & 1) * 65536 + (lane & 7) * 16 + 8 * (lane < 32 ? 512 + (lane >> 4) * 512 + wq * 128 + w * 16 : (lane < 48 ? 0 : 1536) + n64 * 64 + ksl * 16);
    xro = (u32)(tile3 * 32 + (lane & 31)) * 1024 + (u32)(n64 * 64 + ksl * 16 + (lane >> 5) * 8) * 2;
    qdst = (u32)((4 * grp + hn) * 64 + 2 * m64a + wm) * 1024 + lv;
    PIN(qv); PIN(bl); PIN(bh); PIN(pg); PIN(gvo); PIN(xro); PIN(qdst);
#pragma unroll
    for (int t = 0; t < 4; ++t) PIN(ab[t]);
#pragma unroll
    for (int j = 0; j < 8; ++j) PIN(aw[j]);
  }
#define QKV_DMA(s, seg) dma(65536 + ((s) & 3) * 24576 + (seg) * 8192 + w * 1024, qv, rqkv, ((s) ^ r1) * (64 * 3072) + (seg) * 1024)
  asm volatile("s_waitcnt vmcnt(2) lgkmcnt(0)" : "+s"(seed)::"memory");
  const u32 tag = seed + 1u;
  u32x4 a0, a1, a2, a3;
  f32x2 xn[8][4];
  {
    asm volatile("" : "+v"(xr[0]), "+v"(xr[1]), "+v"(xr[2]), "+v"(xr[3]), "+v"(xr[4]), "+v"(xr[5]), "+v"(xr[6]), "+v"(xr[7])::"memory");
    float S[8], Q[8];
#pragma unroll
    for (int j = 0; j < 8; ++j) {
#pragma unroll
      for (int e = 0; e < 4; ++e) xn[j][e] = (f32x2){bflo(xr[j][e]), bfhi(xr[j][e])};
      const f32x2 s2 = (xn[j][0] + xn[j][1]) + (xn[j][2] + xn[j][3]);
      const f32x2 q2 = __builtin_elementwise_fma(xn[j][0], xn[j][0], __builtin_elementwise_fma(xn[j][1], xn[j][1], __builtin_elementwise_fma(xn[j][2], xn[j][2], xn[j][3] * xn[j][3])));
      S[j] = s2[0] + s2[1]; Q[j] = q2[0] + q2[1];
    }
#pragma unroll
    for (int j = 0; j < 4; ++j) { swap32f(S[j], S[j + 4]); S[j] += S[j + 4]; swap32f(Q[j], Q[j + 4]); Q[j] += Q[j + 4]; }
#pragma unroll
    for (int j = 0; j < 2; ++j) { swap16f(S[j], S[j + 2]); S[j] += S[j + 2]; swap16f(Q[j], Q[j + 2]); Q[j] += Q[j + 2]; }
    float ra[2], rb[2];
#pragma unroll
    for (int j = 0; j < 2; ++j) {
      const float mu = row16_sum(S[j]) * (1.f / 512.f);
      const float var = __builtin_fmaxf(row16_sum(Q[j]) * (1.f / 512.f) - mu * mu, 0.f) + 1e-5f;
      ra[j] = __builtin_amdgcn_rsqf(var); rb[j] = -mu * ra[j];
    }
    SBAR();
    LD4X(a0, a1, a2, a3, lv, WS_MODC + (u32)xcd * 32768 + w * 1024, 8192);
    SBAR();
#pragma unroll
    for (int j = 0; j < 8; ++j) {
      const float a = rdlane(ra[j & 1], (j >> 1) * 16), c = rdlane(rb[j & 1], (j >> 1) * 16);
#pragma unroll
      for (int e = 0; e < 4; ++e) { xn[j][e] = __builtin_elementwise_fma(xn[j][e], (f32x2){a, a}, (f32x2){c, c}); PIN(xn[j][e]); }
    }
  }
  asm volatile("s_waitcnt vmcnt(4)" : "+v"(cv1), "+v"(ml)::"memory");
  {
    const float sc = silu_bf(bflo(cv1));
    float v[8];
#pragma unroll
    for (int e = 0; e < 4; ++e) { v[2 * e] = sc * bflo(ml[e]); v[2 * e + 1] = sc * bfhi(ml[e]); }
#pragma unroll
    for (int j = 0; j < 4; ++j) { swap32f(v[j], v[j + 4]); v[j] += v[j + 4]; }
#pragma unroll
    for (int j = 0; j < 2; ++j) { swap16f(v[j], v[j + 2]); v[j] += v[j + 2]; }
    const bool o = lane & 8;
    const float a = (o ? v[1] : v[0]) + dppf<0x128>(o ? v[0] : v[1]);
    sts4f(SCR + 1024 + ((lane & 7) * 8 + (lane >> 3)) * 32 + w * 4, a);
  }
  STAMP(22);
  QKV_DMA(0, 0); QKV_DMA(0, 1); QKV_DMA(0, 2); QKV_DMA(1, 0); QKV_DMA(1, 1); QKV_DMA(1, 2);
  {
    WAIT4X(6, a0, a1, a2, a3);
#pragma unroll 1
    for (u32 spins = 0; spins < SPIN_CAP; ++spins) {
      if (__all(tags_ok(a0, a1, tag) && tags_ok(a2, a3, tag))) break;
      LD4X(a0, a1, a2, a3, lv, WS_MODC + (u32)xcd * 32768 + w * 1024, 8192);
      WAIT4X(0, a0, a1, a2, a3);
    }
    if (w == 0) __builtin_amdgcn_s_setprio(0);
    *(LAS u32*)(uintptr_t)(SCR + 4096 + tid * 4) = pk2(gsum(a0, a1, a2, a3, 0), gsum(a0, a1, a2, a3, 2));
  }
  STAMP(23);
  LBAR();
  STAMP(2);
  {
    const u32x4 sa = lds16u(SCR + 4096 + lv), ca = lds16u(SCR + 5120 + lv);
    f32x2 fs[4], fa[4];
#pragma unroll
    for (int e = 0; e < 4; ++e) {
      const u32 t = pk2(1.f + bflo(ca[e]), 1.f + bfhi(ca[e]));
      fs[e] = (f32x2){bflo(t), bfhi(t)}; fa[e] = (f32x2){bflo(sa[e]), bfhi(sa[e])};
    }
#pragma unroll
    for (int j = 0; j < 8; ++j) {
      u32x4 h;
#pragma unroll
      for (int e = 0; e < 4; ++e) { const f32x2 t = __builtin_elementwise_fma(xn[j][e], fs[e], fa[e]); h[e] = pk2(t[0], t[1]); }
      sts16(aw[j], h);
    }
    if (w == 1) st_gran(ws, WS_MODL + (u32)(bi >> 2) * 16384 + (u32)((xcd * 4 + (bi & 3)) * 64 + lane) * 8,
                        sum8(lds16f(SCR + 1024 + lane * 32), lds16f(SCR + 1024 + lane * 32 + 16)), tag);
  }
  const rsrc_t rwg = __builtin_amdgcn_make_buffer_rsrc((void*)(wmat ? pwu : pwg), 0, bi < 28 ? 512 * 1792 * 2 : 0, 0x00020000);
#define WG_DMA(i) dma(65536 + w * 2048 + (i) * 1024, pg, rwg, (i) * (8 * 3584))
  u32x4 g0, g1, g2, g3, xres;
  {
    f32x4 acc[2][3] = {};
    bf16x8 fa[2][2][2], fb[2][2][3];
#define Q_RA(S, s, kk, m) fa[S][kk][m] = lds16(ab[(2 * (s) + (kk)) & 3] + ((2 * (s) + (kk)) >> 2) * 256 + (m) * 16384)
#define Q_RB(S, s, kk, sg) fb[S][kk][sg] = ldtr((((s) & 2) ? bh : bl) + ((s) & 1) * 24576 + (sg) * 8192 + (kk) * 4096)
#define Q_M(S, kk, m, sg) do { acc[m][sg] = (sg) < 2 ? MFMA16(fb[S][kk][sg], fa[S][kk][m], acc[m][sg]) : MFMA16(fa[S][kk][m], fb[S][kk][sg], acc[m][sg]); PIN(acc[m][sg]); } while (0)
#define Q_STAGE(C, N, s, RD, D0, D1, D2, D3) do { \
      if (RD) { Q_RA(N, s, 0, 0); Q_RA(N, s, 0, 1); } Q_M(C, 0, 0, 0); SBAR(); \
      if (RD) Q_RB(N, s, 0, 0); Q_M(C, 0, 0, 1); SBAR(); \
      if (RD) Q_RB(N, s, 0, 1); Q_M(C, 0, 0, 2); SBAR(); \
      if (RD) Q_RB(N, s, 0, 2); Q_M(C, 0, 1, 0); SBAR(); \
      if (RD) { Q_RA(N, s, 1, 0); Q_RA(N, s, 1, 1); } Q_M(C, 0, 1, 1); SBAR(); \
      if (RD) Q_RB(N, s, 1, 0); Q_M(C, 0, 1, 2); SBAR(); \
      if (RD) Q_RB(N, s, 1, 1); Q_M(C, 1, 0, 0); SBAR(); \
      if (RD) Q_RB(N, s, 1, 2); Q_M(C, 1, 0, 1); SBAR(); \
      D0; Q_M(C, 1, 0, 2); SBAR(); D1; Q_M(C, 1, 1, 0); SBAR(); D2; Q_M(C, 1, 1, 1); SBAR(); D3; Q_M(C, 1, 1, 2); SBAR(); } while (0)
    WAITBAR(0);
    QKV_DMA(2, 0); QKV_DMA(2, 1); QKV_DMA(2, 2);
#pragma unroll
    for (int kk = 0; kk < 2; ++kk) { Q_RA(0, 0, kk, 0); Q_RA(0, 0, kk, 1); Q_RB(0, 0, kk, 0); Q_RB(0, 0, kk, 1); Q_RB(0, 0, kk, 2); }
    STAMP(20);
    LBAR();
    Q_STAGE(0, 1, 1, true, QKV_DMA(3, 0), QKV_DMA(3, 1), QKV_DMA(3, 2), QKV_DMA(4, 0));
    WAITBAR(4); Q_STAGE(1, 0, 2, true, QKV_DMA(4, 1), QKV_DMA(4, 2), QKV_DMA(5, 0), QKV_DMA(5, 1));
    WAITBAR(5); Q_STAGE(0, 1, 3, true, QKV_DMA(5, 2), QKV_DMA(6, 0), QKV_DMA(6, 1), QKV_DMA(6, 2));
    WAITBAR(6); Q_STAGE(1, 0, 4, true, QKV_DMA(7, 0), QKV_DMA(7, 1), QKV_DMA(7, 2), NOP_);
    WAITBAR(6); Q_STAGE(0, 1, 5, true, WG_DMA(0), WG_DMA(1), ld16(xres, xro, rx, 0), NOP_);
    WAITBAR(6); Q_STAGE(1, 0, 6, true, LD4X(g0, g1, g2, g3, gvo, WS_MODL, 16384), NOP_, NOP_, NOP_);
    WAITBAR(7); Q_STAGE(0, 1, 7, true, NOP_, NOP_, NOP_, NOP_);
    Q_STAGE(1, 0, 0, false, NOP_, NOP_, NOP_, NOP_);
    STAMP(3);
    constexpr float QS = 0.25f * 1.4426950408889634f;
    {
      u32 p0 = pk2(acc[0][0][0] * QS, acc[0][0][1] * QS), p1 = pk2(acc[0][0][2] * QS, acc[0][0][3] * QS);
      u32 r0 = pk2(acc[1][0][0] * QS, acc[1][0][1] * QS), r1 = pk2(acc[1][0][2] * QS, acc[1][0][3] * QS);
      swap16u(p0, r0); swap16u(p1, r1);
      st16_sc1((u32x4){p0, p1, r0, r1}, rws, WS_Q + qdst);
    }
    {
      u32 p0 = pk2(acc[0][1][0], acc[0][1][1]), p1 = pk2(acc[0][1][2], acc[0][1][3]);
      u32 r0 = pk2(acc[1][1][0], acc[1][1][1]), r1 = pk2(acc[1][1][2], acc[1][1][3]);
      swap16u(p0, r0); swap16u(p1, r1);
      st16_sc1((u32x4){p0, p1, r0, r1}, rws, WS_K + qdst);
    }
    {
      u32 p0 = pk2(acc[0][2][0], acc[0][2][1]), p1 = pk2(acc[0][2][2], acc[0][2][3]);
      u32 r0 = pk2(acc[1][2][0], acc[1][2][1]), r1 = pk2(acc[1][2][2], acc[1][2][3]);
      swap32u(p0, r0); swap32u(p1, r1);
      st16_sc1((u32x4){p0, p1, r0, r1}, rws, WS_V + qdst);
    }
  }
  u32x4 gav, gbv, x1v, q;
  {
    pv = (u32)(lane >> 3) * 1024 + (((lane & 7) ^ (((lane >> 4) & 1) << 2)) * 16) + n64 * 128;
#pragma unroll
    for (int e = 0; e < 16; ++e) ms[e] = (e & 3) + 8 * (e >> 2) + 4 * (lane >> 5) > (lane & 31) ? 0xffffffffu : 0u;
#pragma unroll
    for (int e = 0; e < 8; ++e) mk[e] = (ms[2 * e] & 0xffffu) | (ms[2 * e + 1] & 0xffff0000u);
    kb = (u32)(w * 1024 + lane * 16);
    vb0 = 16384 + w * 1040 + ((lane & 31) < 16 ? (lane >> 5) * 256 + (lane & 31) * 16 : 1024);
    vb1 = 16384 + w * 1040 + ((lane & 31) < 16 ? 512 + (lane >> 5) * 256 + (lane & 31) * 16 : 1024);
    const int wcu = wg * 4 + g, wn = wcu / 7, wfr = wcu - wn * 7;
    wdst = WS_WP + (u32)wn * WPN + wfr * 1024 + (u32)(wmat * 16 + fr) * 16;
    ta = 65536 + w * 2048 + (u32)(fr >> 2) * 128 + ((g ^ ((fr >> 2) & 2)) * 32) + (lane & 3) * 8;
    odst = WS_O + (u32)((8 * range + w) * 32 + head2) * 1024 + lv;
    const int hd = 4 * grp + (lane & 3), rg = (m64a >> 2) + ((lane >> 2) & 1);
    postq = lane < 4 || (lane < 8 && rg < 8) ? mbox(hd >> 2, (rg << 2) | (hd & 3), MB_QKV + (m64a & 3) + (lane < 4 ? 4 : 0)) : lane == 8 ? WS_ARR + b * 4 : NOWHERE;
    posto = lane < 32 ? mbox(range, lane, MB_O + head2) : NOWHERE;
    const int wnp = (wg * 4 + (lane < 16 ? 0 : 3)) / 7;
    postw = lane < 32 ? mbox(((fr >> 3) << 2) | (wnp >> 2), ((wnp & 3) << 3) | (lane & 7), wmat * 12 + (wg - ((7 * wnp) >> 2)) * 4 + wq) : NOWHERE;
    pollq = mbox(xcd, bi, MB_QKV + (w >> 1) + ((lane & 1) || range == 0 ? 4 : 0));
    const int l24 = (lane & 31) < 24 ? (lane & 31) : 0, l12 = l24 < 12 ? l24 : l24 - 12;
    const int wslot = l12 < 4 * (((7 * nt + 6) >> 2) - ((7 * nt) >> 2) + 1) ? l24 : 0;
    pollw = mbox(xcd, bi, MB_W + wslot);
    pollx = mbox(xcd, bi, lane < 32 ? MB_X1 + ((w >> 1) & 1) * 8 + (lane & 7) : MB_E + wslot);
    PIN(pv); PIN(kb); PIN(vb0); PIN(vb1); PIN(wdst); PIN(ta); PIN(odst); PIN(postq); PIN(posto); PIN(postw); PIN(pollq); PIN(pollw); PIN(pollx);
#pragma unroll
    for (int j = 0; j < 8; ++j) PIN(mk[j]);
#pragma unroll
    for (int e = 0; e < 16; ++e) PIN(ms[e]);
  }
  {
    asm volatile("s_waitcnt vmcnt(0) lgkmcnt(0)\n\ts_barrier" : "+v"(g0), "+v"(g1), "+v"(g2), "+v"(g3), "+v"(xres)::"memory");
    if (w == 0) post(postq, rws, tag);
    STAMP(4);
    if (tid < 16) { const u32 o1 = tid >= 8 || range > 0 ? 0x3f803f80u : 0u; sts16(16384 + tid * 1040 + 1024, (u32x4){o1, o1, o1, o1}); }
#pragma unroll 1
    for (u32 spins = 0; spins < SPIN_CAP; ++spins) {
      if (__all(tags_ok(g0, g1, tag) && tags_ok(g2, g3, tag))) break;
      __builtin_amdgcn_s_sleep(1);
      LD4X(g0, g1, g2, g3, gvo, WS_MODL, 16384);
      WAIT4X(0, g0, g1, g2, g3);
    }
    const u32 gs = 49152 + w * 128;
    {
      float t0 = gsum(g0, g1, g2, g3, 0), t1 = gsum(g0, g1, g2, g3, 2);
      t0 += dppf<0x128>(t0); t1 += dppf<0x128>(t1);
      u32 t = pk2(t0, t1);
      if ((lane >> 4) == 1) t = pk2(1.f + bflo(t), 1.f + bfhi(t));
      if ((lane & 8) == 0) *(LAS u32*)(uintptr_t)(gs + (lane >> 4) * 32 + (lane & 7) * 4) = t;
      asm volatile("s_waitcnt lgkmcnt(0)" ::: "memory");
    }
    gav = lds16u(gs + 64 + (lane >> 5) * 16); gbv = lds16u(gs + 96 + (lane >> 5) * 16);
    if (bi < 28) {
      float u = 0.f, bs = 0.f;
#pragma unroll
      for (int h = 0; h < 2; ++h) {
        const u32x4 wv = __builtin_bit_cast(u32x4, ldtr(ta + h * 1024)), sb = lds16u(gs + h * 16), cb = lds16u(gs + 32 + h * 16);
        u32x4 e;
#pragma unroll
        for (int j = 0; j < 4; ++j) {
          e[j] = pk2(bflo(wv[j]) * bflo(cb[j]), bfhi(wv[j]) * bfhi(cb[j]));
          u += bflo(e[j]) + bfhi(e[j]);
          bs = __builtin_fmaf(bflo(wv[j]), bflo(sb[j]), __builtin_fmaf(bfhi(wv[j]), bfhi(sb[j]), bs));
        }
        st16_sc1(e, rws, wdst + h * 512 + (u32)(wq * 8 + w) * 7168);
      }
      sts4f(SC2 + lane * 32 + w * 4, u); sts4f(SC2 + 2048 + lane * 32 + w * 4, bs);
    }
    STAMP(5);
    poll(pollq, rws, tag);
    STAMP(6);
    const u32 src = (u32)(head2 * 64 + 8 * range + w) * 1024;
    const rsrc_t rh = __builtin_amdgcn_make_buffer_rsrc(ws, 0, range > 0 ? WS_BYTES : 0, 0x00020000);
    dma_sc1((w + 8) * 1024, lv, rws, WS_K + src);
    ld16_sc1(q, lv, rws, WS_Q + src);
    dma_sc1(w * 1024, lv, rh, WS_K + src - 8192);
    dma_sc1(16384 + (w + 8) * 1040, lv, rws, WS_V + src);
    dma_sc1(16384 + w * 1040, lv, rh, WS_V + src - 8192);
  }
  {
    asm volatile("s_waitcnt vmcnt(0) lgkmcnt(0)\n\ts_barrier" : "+v"(q)::"memory");
    STAMP(7);
    if (bi < 28 && w == 7) {
      post(postw + MB_W * 4, rws, tag);
      const float t = sum8(lds16f(SC2 + lane * 32), lds16f(SC2 + lane * 32 + 16)), o = sum8(lds16f(SC2 + 2048 + lane * 32), lds16f(SC2 + 2048 + lane * 32 + 16));
      const float uh = rbf(t), ul = t - uh, bh = rbf(o), bl = o - bh;
      st16_sc1((u32x4){pk2(uh, ul), pk2(uh, bh), pk2(bl, bh), 0u}, rws, wdst + (wq & 1) * 512 + (u32)(32 + (wq >> 1)) * 7168);
    }
    const u32x4 ov = attn_wave(range > 0, w, lane, BF8(q), mk, ms, kb, vb0, vb1, PRE_ARGS,
                               pv, rwo, (u32)n64 | (u32)(bi & 3) << 9);
    st16_sc1(ov, rws, odst);
    STAMP(8);
    u32 fw;
    ld4_sc1(fw, pollw, rws, 0);
    {
#pragma unroll
      for (int c = 0; c < 2; ++c) tb[c] = (u32)(8 * (lane >> 5) + (fr >> 2)) * 128 + ((((2 * c + (g & 1) + ksl) & 3) ^ ((fr >> 2) & 2)) * 32) + (lane & 3) * 8;
      xdst3 = WS_X1 + (u32)(tile3 * 32 + n64 * 4 + ksl) * 1024 + lv;
      adst = WS_ACT + (u32)((4 * m128 + hn) * 112 + nt * 7 + (w & 4)) * 1024 + lv;
      postx = lane < 16 ? mbox(((m64 >> 4) << 2) | (lane >> 2), ((lane & 3) << 3) | ((m64 >> 1) & 7), MB_X1 + (m64 & 1) * 8 + n64) : NOWHERE;
      posta = lane < 16 ? mbox(m128 >> 1, ((lane >> 1) << 2) | ((2 * m128 + (lane & 1)) & 3), MB_ACT + nt) : NOWHERE;
      pollo = mbox(xcd, bi, MB_O + ksl * 8 + (lane & 7));
      polla = mbox(xcd, bi, MB_ACT + ksl * 4 + (lane & 3));
#pragma unroll
      for (int j = 1; j < 4; ++j) rbw[j - 1] = (mh ? 36864 : 65536) + (u32)(((ksl + j) & 3) * 3 + j - 1) * 2048 + lv;
      rbr = (mh ? 36864 : 65536) + (u32)ksl * 6144 + lv;
      wl = lv + (w & 4) * 1024; xl = lv + 114688 + hn * 4096;
      stv = (u32)(lane >> 5) * 2048 + (lane & 31) * 16;
      PIN(tb[0]); PIN(tb[1]); PIN(xdst3); PIN(adst); PIN(postx); PIN(posta); PIN(pollo); PIN(polla); PIN(rbw[0]); PIN(rbw[1]); PIN(rbw[2]); PIN(rbr); PIN(wl); PIN(xl); PIN(stv);
    }
    asm volatile("s_waitcnt vmcnt(0) lgkmcnt(0)\n\ts_barrier" : "+v"(fw)::"memory");
    if (w == 0) post(posto, rws, tag);
    if (bi < 28 && w == 7) post(postw + MB_E * 4, rws, tag);
    STAMP(21);
    if (!__all(fw == tag)) poll(pollw, rws, tag);
#pragma unroll
    for (int i = 0; i < 4; ++i) if (i < 3 || w < 4) dma_sc1(w * 1024 + i * 8192, lv, rws, WS_WP + (u32)nt * WPN + ((2 * nt + m128) & 7) * 28672 + w * 1024 + i * 8192);
    bf16x8 wf[8][2];
#pragma unroll
    for (int ks = 0; ks < 8; ++ks)
#pragma unroll
      for (int c = 0; c < 2; ++c) wf[ks][c] = ldtr(98304 + ksl * 16384 + tb[c] + ks * 2048);
    if (b == 255 && w == 1) {
#pragma unroll 1
      for (int i = 0; i < 4; ++i) poll(WS_ARR + i * 256 + lane * 4, rws, tag);
      if (lane == 0) __hip_atomic_store((GAS u32*)(ws + WS_SEED), tag, RLX_AGENT);
    }
    poll(pollo, rws, tag);
    STAMP(9);
    u32x4 of[8];
#pragma unroll
    for (int ks = 0; ks < 8; ++ks) ld16_sc1(of[ks], lv, rws, WS_O + (u32)(tile3 * 32 + ksl * 8 + (ks ^ n64)) * 1024);
    f32x16 acc[2] = {};
#pragma unroll
    for (int ks = 0; ks < 8; ++ks) {
      WAITV1(7 - ks, of[ks]);
      acc[0] = MFMA32(wf[ks][0], BF8(of[ks]), acc[0]);
      acc[1] = MFMA32(wf[ks][1], BF8(of[ks]), acc[1]);
      PIN(acc[0]); PIN(acc[1]);
    }
#pragma unroll
    for (int j = 1; j < 4; ++j) {
      const u32 a = rbw[j - 1];
      const int o = (j & 1) * 8;
      sts16f(a, (f32x4){acc[j >> 1][o], acc[j >> 1][o + 1], acc[j >> 1][o + 2], acc[j >> 1][o + 3]});
      sts16f(a + 1024, (f32x4){acc[j >> 1][o + 4], acc[j >> 1][o + 5], acc[j >> 1][o + 6], acc[j >> 1][o + 7]});
    }
    LBAR();
    float y[8];
#pragma unroll
    for (int e = 0; e < 8; ++e) y[e] = acc[0][e];
#pragma unroll
    for (int j = 0; j < 3; ++j) {
      const f32x4 a = lds16f(rbr + j * 2048), d = lds16f(rbr + j * 2048 + 1024);
#pragma unroll
      for (int e = 0; e < 4; ++e) { y[e] += a[e]; y[4 + e] += d[e]; }
    }
    x1v = resid_epi(y, xres, gav);
    st16_sc1(x1v, rws, xdst3);
    STAMP(10);
    {
      float s = 0.f, q2 = 0.f;
#pragma unroll
      for (int e = 0; e < 4; ++e) { const float a = bflo(x1v[e]), c = bfhi(x1v[e]); s += a + c; q2 = __builtin_fmaf(a, a, __builtin_fmaf(c, c, q2)); }
      swap32f(s, q2);
      sts4f(SC3 + (u32)(mh * 1024 + ksl * 256 + (lane & 31) * 8 + (lane >> 5) * 4), s + q2);
    }
    WAITBAR(0);
    if (w == 2) post(postx, rws, tag);
    if (ksl == 0) {
      const u32 a = SC3 + (u32)(mh * 1024 + (lane & 31) * 8 + (lane >> 5) * 4);
      const float t = (lds4f(a) + lds4f(a + 256)) + (lds4f(a + 512) + lds4f(a + 768));
      st_gran(ws, WS_ST + (u32)(tile3 * 8 + n64) * 512 + (lane & 31) * 16 + (lane >> 5) * 8, t, tag);
      asm volatile("s_waitcnt lgkmcnt(0)" ::: "memory");
    }
    STAMP(11);
  }
  const u32 r5 = (u32)(((n64 * 7) >> 1) + 7 * (bi & 3)) % 28;
  u32 wh = wl + 57344;
  PIN(wh);
  if (w < 4) ffn_up<4>(w, lane, tag, ws, rws, rwd, pv, m128, nt, r5, pollx, wl, wh, xl, stv, adst, b DBGPASS);
  else ffn_up<3>(w, lane, tag, ws, rws, rwd, pv, m128, nt, r5, pollx, wl, wh, xl, stv, adst, b DBGPASS);
  STAMP(16);
  {
    u32 t0 = tb[0] + ksl * 40960, t1 = tb[1] + ksl * 40960;
    PIN(t0); PIN(t1);
    WAITBAR(0);
    if (w == 0) post(posta, rws, tag);
    bf16x8 wp[8][2];
#pragma unroll
    for (int s = 0; s < 8; ++s) { wp[s][0] = ldtr(t0 + s * 2048); wp[s][1] = ldtr(t1 + s * 2048); }
    LBAR();
#define DN_RF(i) dma((u32)((w >> 1) * 40 + (w & 1) * 8 + (i)) * 1024, pv, rwd, (u32)((w >> 1) * 448 + ((i) & 1) * 8 + rot28(20 + (w & 1) * 4 + ((i) >> 1), r5) * 16) * 1024)
    DN_RF(0); DN_RF(1); DN_RF(2); DN_RF(3);
    DN_RF(4); DN_RF(5); DN_RF(6); DN_RF(7);
    STAMP(15);
    poll(polla, rws, tag);
    STAMP(17);
    u32x4 af[28];
    const u32 asrc = WS_ACT + (u32)(tile3 * 112 + ksl * 28) * 1024;
#pragma unroll
    for (int ks = 0; ks < DEP; ++ks) ld16_sc1(af[ks], lv, rws, asrc + rot28(ks, r5) * 1024);
    f32x16 acc[2] = {};
    bf16x8 wa0, wa1, wb0, wb1;
#define DN_PG(s) ((s) < 20 ? (s) * 2048 : ((s) - 20) * 2048)
#define DN_RD(A, B, s) do { A = ldtr(t0 + DN_PG(s)); B = ldtr(t1 + DN_PG(s)); } while (0)
#define DN_MM(A, B, ks) do { if ((ks) + DEP < 28) ld16_sc1(af[(ks) + DEP < 28 ? (ks) + DEP : 0], lv, rws, asrc + rot28((ks) + DEP, r5) * 1024); \
      WAITV1((ks) + DEP < 28 ? DEP : 27 - (ks), af[ks]); \
      acc[0] = MFMA32(A, BF8(af[ks]), acc[0]); PIN(acc[0]); acc[1] = MFMA32(B, BF8(af[ks]), acc[1]); PIN(acc[1]); SBAR(); } while (0)
#pragma unroll
    for (int ks = 0; ks < 7; ++ks) DN_MM(wp[ks][0], wp[ks][1], ks);
    DN_RD(wa0, wa1, 8); DN_MM(wp[7][0], wp[7][1], 7);
#pragma unroll
    for (int ks = 8; ks < 28; ks += 2) {
      DN_RD(wb0, wb1, ks + 1); DN_MM(wa0, wa1, ks);
      if (ks == 18) LBAR();
      if (ks + 2 < 28) DN_RD(wa0, wa1, ks + 2);
      DN_MM(wb0, wb1, ks + 1);
    }
    STAMP(18);
    const u32 sb = (u32)ksl * 40960 + 16384 + mh * 6144 + lv;
#pragma unroll
    for (int j = 1; j < 4; ++j) {
      const int o = (j & 1) * 8;
      sts16f(sb + (j - 1) * 2048, (f32x4){acc[j >> 1][o], acc[j >> 1][o + 1], acc[j >> 1][o + 2], acc[j >> 1][o + 3]});
      sts16f(sb + (j - 1) * 2048 + 1024, (f32x4){acc[j >> 1][o + 4], acc[j >> 1][o + 5], acc[j >> 1][o + 6], acc[j >> 1][o + 7]});
    }
    LBAR();
    float y[8];
#pragma unroll
    for (int e = 0; e < 8; ++e) y[e] = acc[0][e];
#pragma unroll
    for (int j = 1; j < 4; ++j) {
      const u32 a = (u32)((ksl - j) & 3) * 40960 + 16384 + mh * 6144 + (j - 1) * 2048 + lv;
      const f32x4 c = lds16f(a), d = lds16f(a + 1024);
#pragma unroll
      for (int e = 0; e < 4; ++e) { y[e] += c[e]; y[4 + e] += d[e]; }
    }
    const u32x4 ov = resid_epi(y, x1v, gbv);
    __builtin_amdgcn_raw_buffer_store_b128(ov, rout, xro, 0, 0);
    STAMP(19);
    STAMP_END();
  }
}

extern "C" void kernel_launch(void* const* d_in, const int* in_sizes, int n_in, void* d_out, int out_size, void* d_ws, size_t ws_size,
                              hipStream_t stream) {
  fused_block<<<256, 576, 0, stream>>>(d_in[0], d_in[1], d_in[2], d_in[4], d_in[5], d_in[6], d_in[7], d_in[8], d_out, (char*)d_ws);
}
```

```cpp
#include <hip/hip_runtime.h>
#include <stdint.h>

typedef unsigned u32;
typedef unsigned long long u64;
typedef __attribute__((ext_vector_type(2))) u32 u32x2;
typedef __attribute__((ext_vector_type(4))) u32 u32x4;
typedef __attribute__((ext_vector_type(2))) float f32x2;
typedef __attribute__((ext_vector_type(4))) float f32x4;
typedef __attribute__((ext_vector_type(16))) float f32x16;
typedef __attribute__((ext_vector_type(8))) short bf16x8;
typedef __attribute__((ext_vector_type(2))) __bf16 bf16x2;
typedef __amdgpu_buffer_rsrc_t rsrc_t;
#define LAS __attribute__((address_space(3)))
#define GAS __attribute__((address_space(1)))
#define DI __device__ __forceinline__
#define RLX_AGENT __ATOMIC_RELAXED, __HIP_MEMORY_SCOPE_AGENT

constexpr u32 WS_SEED = 0;
constexpr u32 WS_ARR = 4096;
constexpr u32 WS_MODL = 65536;
constexpr u32 WS_MODC = 524288;
constexpr u32 WS_MB = 262144;
constexpr u32 MB_QKV = 0, MB_O = 32, MB_X1 = 64, MB_E = 96, MB_W = 128, MB_ACT = 160;
constexpr u32 WS_ST = 786432;
constexpr u32 WS_Q = 1u << 20, WS_K = 3u << 20, WS_V = 5u << 20;
constexpr u32 WS_O = 7u << 20;
constexpr u32 WS_X1 = 9u << 20;
constexpr u32 WS_ACT = 11u << 20;
constexpr u32 WS_WP = 18u << 20;
constexpr u32 WPN = 34 * 7168;
constexpr u32 WS_BYTES = 22u << 20;
constexpr u32 WS_DBG = 24u << 20;
#define DEP 10
#define PRE_ARGS rws, WS_WP + (u32)nt * WPN + (u32)((bi & 7) * 30 + w) * 1024
constexpr u32 SPIN_CAP = 1u << 15;
#define START_DELAY 8

DI float bflo(u32 w) { return __uint_as_float(w << 16); }
DI float bfhi(u32 w) { return __uint_as_float(w & 0xffff0000u); }
DI u32 pk2(float a, float b) { f32x2 v = {a, b}; return __builtin_bit_cast(u32, __builtin_convertvector(v, bf16x2)); }
DI float rbf(float a) { return bflo(pk2(a, a)); }
template <int CTRL> DI float dppf(float x) { return __builtin_bit_cast(float, __builtin_amdgcn_update_dpp(0, __builtin_bit_cast(int, x), CTRL, 0xf, 0xf, true)); }
DI void swap32f(float& a, float& b) { auto r = __builtin_amdgcn_permlane32_swap(__float_as_uint(a), __float_as_uint(b), false, false); a = __uint_as_float(r[0]); b = __uint_as_float(r[1]); }
DI void swap16f(float& a, float& b) { auto r = __builtin_amdgcn_permlane16_swap(__float_as_uint(a), __float_as_uint(b), false, false); a = __uint_as_float(r[0]); b = __uint_as_float(r[1]); }
DI void swap32u(u32& a, u32& b) { auto r = __builtin_amdgcn_permlane32_swap(a, b, false, false); a = r[0]; b = r[1]; }
DI void swap16u(u32& a, u32& b) { auto r = __builtin_amdgcn_permlane16_swap(a, b, false, false); a = r[0]; b = r[1]; }
DI float row16_sum(float x) { x += dppf<0xB1>(x); x += dppf<0x4E>(x); x += dppf<0x141>(x); x += dppf<0x140>(x); return x; }
DI float rdlane(float x, int l) { return __uint_as_float(__builtin_amdgcn_readlane(__float_as_uint(x), l)); }

#define MFMA16(a, b, c) __builtin_amdgcn_mfma_f32_16x16x32_bf16((a), (b), (c), 0, 0, 0)
#define MFMA32(a, b, c) __builtin_amdgcn_mfma_f32_32x32x16_bf16((a), (b), (c), 0, 0, 0)
#define BF8(x) __builtin_bit_cast(bf16x8, (x))
#define WAITV(n) asm volatile("s_waitcnt vmcnt(%0)" ::"i"(n) : "memory")
#define WAITBAR(n) asm volatile("s_waitcnt vmcnt(%0) lgkmcnt(0)\n\ts_barrier" ::"i"(n) : "memory")
#define LBAR() asm volatile("s_waitcnt lgkmcnt(0)\n\ts_barrier" ::: "memory")
#define SBAR() __builtin_amdgcn_sched_barrier(0)
#define NOP_ do {} while (0)

DI void dma(u32 lds, u32 voff, rsrc_t rs, u32 soff) {
  asm volatile("s_mov_b32 m0, %0\n\ts_nop 0\n\tbuffer_load_dwordx4 %1, %2, %3 offen lds" ::"s"(lds), "v"(voff), "s"(rs), "s"(soff) : "memory");
}
DI void dma_sc1(u32 lds, u32 voff, rsrc_t rs, u32 soff) {
  asm volatile("s_mov_b32 m0, %0\n\ts_nop 0\n\tbuffer_load_dwordx4 %1, %2, %3 offen sc1 lds" ::"s"(lds), "v"(voff), "s"(rs), "s"(soff) : "memory");
}
DI void dma4(u32 lds, u32 voff, rsrc_t rs, u32 soff) {
  asm volatile("s_mov_b32 m0, %0\n\ts_nop 0\n\tbuffer_load_dword %1, %2, %3 offen lds" ::"s"(lds), "v"(voff), "s"(rs), "s"(soff) : "memory");
}
DI void ld16(u32x4& r, u32 voff, rsrc_t rs, u32 soff) { asm volatile("buffer_load_dwordx4 %0, %1, %2, %3 offen" : "=v"(r) : "v"(voff), "s"(rs), "s"(soff) : "memory"); }
DI void ld16_sc1(u32x4& r, u32 voff, rsrc_t rs, u32 soff) { asm volatile("buffer_load_dwordx4 %0, %1, %2, %3 offen sc1" : "=v"(r) : "v"(voff), "s"(rs), "s"(soff) : "memory"); }
DI void ld8_sc1(u32x2& r, u32 voff, rsrc_t rs, u32 soff) { asm volatile("buffer_load_dwordx2 %0, %1, %2, %3 offen sc1" : "=v"(r) : "v"(voff), "s"(rs), "s"(soff) : "memory"); }
DI void ld4(u32& r, u32 voff, rsrc_t rs, u32 soff) { asm volatile("buffer_load_dword %0, %1, %2, %3 offen" : "=v"(r) : "v"(voff), "s"(rs), "s"(soff) : "memory"); }
DI void ld2(u32& r, u32 voff, rsrc_t rs, u32 soff) { asm volatile("buffer_load_ushort %0, %1, %2, %3 offen" : "=v"(r) : "v"(voff), "s"(rs), "s"(soff) : "memory"); }
#define WAITV1(n, a) asm volatile("s_waitcnt vmcnt(%1)" : "+v"(a) : "i"(n) : "memory")
#define PIN(a) asm volatile("" : "+v"(a))
DI void st16_sc1(u32x4 v, rsrc_t rs, u32 voff) { __builtin_amdgcn_raw_buffer_store_b128(v, rs, voff, 0, 16); }

DI bf16x8 lds16(u32 a) { return *(const LAS bf16x8*)(uintptr_t)a; }
DI u32x4 lds16u(u32 a) { return *(const LAS u32x4*)(uintptr_t)a; }
DI f32x4 lds16f(u32 a) { return *(const LAS f32x4*)(uintptr_t)a; }
DI f32x2 lds8f(u32 a) { return *(const LAS f32x2*)(uintptr_t)a; }
DI float lds4f(u32 a) { return *(const LAS float*)(uintptr_t)a; }
DI void sts16(u32 a, u32x4 v) { *(LAS u32x4*)(uintptr_t)a = v; }
DI void sts16f(u32 a, f32x4 v) { *(LAS f32x4*)(uintptr_t)a = v; }
DI void sts8f(u32 a, f32x2 v) { *(LAS f32x2*)(uintptr_t)a = v; }
DI void sts4f(u32 a, float v) { *(LAS float*)(uintptr_t)a = v; }
typedef short v4i16_t __attribute__((ext_vector_type(4)));
DI bf16x8 ldtr(u32 a) {
  v4i16_t lo = __builtin_amdgcn_ds_read_tr16_b64_v4i16((LAS v4i16_t*)(uintptr_t)a);
  v4i16_t hi = __builtin_amdgcn_ds_read_tr16_b64_v4i16((LAS v4i16_t*)(uintptr_t)(a + 512));
  return __builtin_shufflevector(lo, hi, 0, 1, 2, 3, 4, 5, 6, 7);
}

DI void poll(u32 voff, rsrc_t rws, u32 tag) {
#pragma unroll 1
  for (u32 spins = 0; spins < SPIN_CAP; ++spins) {
    u32 v;
    asm volatile("buffer_load_dword %0, %1, %2, 0 offen sc1\n\ts_waitcnt vmcnt(0)" : "=v"(v) : "v"(voff), "s"(rws) : "memory");
    if (__all(v == tag)) break;
    __builtin_amdgcn_s_sleep(1);
  }
}
DI void post(u32 voff, rsrc_t rws, u32 tag) { __builtin_amdgcn_raw_buffer_store_b32(tag, rws, voff, 0, 16); }
constexpr u32 NOWHERE = 0x7ffffff0u;
DI u32 mbox(int xcd, int bi, u32 word) { return WS_MB + (u32)(bi * 8 + xcd) * 1024 + word * 4; }
DI bool tags_ok(u32x4 a, u32x4 b, u32 tag) { return a[1] == tag && a[3] == tag && b[1] == tag && b[3] == tag; }
DI float gsum(u32x4 a, u32x4 b, u32x4 c, u32x4 d, int i) { return (__uint_as_float(a[i]) + __uint_as_float(b[i])) + (__uint_as_float(c[i]) + __uint_as_float(d[i])); }
DI void st_gran(char* ws, u32 byte, float v, u32 tag) { __hip_atomic_store((GAS u64*)(ws + byte), ((u64)tag << 32) | __float_as_uint(v), RLX_AGENT); }
DI void ld4_sc1(u32& r, u32 voff, rsrc_t rs, u32 soff) { asm volatile("buffer_load_dword %0, %1, %2, %3 offen sc1" : "=v"(r) : "v"(voff), "s"(rs), "s"(soff) : "memory"); }
#define LD4X(a, b, c, d, voff, base, step) do { ld16_sc1(a, voff, rws, base); ld16_sc1(b, voff, rws, (base) + (step)); ld16_sc1(c, voff, rws, (base) + 2 * (step)); ld16_sc1(d, voff, rws, (base) + 3 * (step)); } while (0)
#define WAIT4X(n, a, b, c, d) asm volatile("s_waitcnt vmcnt(%4)" : "+v"(a), "+v"(b), "+v"(c), "+v"(d) : "i"(n) : "memory")

#define STAMP(i)
#define STAMP_END()
#define DBGARG
#define DBGPASS

DI float silu_bf(float c) { return rbf(c * __builtin_amdgcn_rcpf(1.f + __builtin_amdgcn_exp2f(c * -1.4426950408889634f))); }
DI float sum8(f32x4 a, f32x4 d) { return ((a[0] + a[1]) + (a[2] + a[3])) + ((d[0] + d[1]) + (d[2] + d[3])); }

constexpr u64 upper_mask(int e) {
  u64 m = 0;
  for (int l = 0; l < 64; ++l) if ((e & 3) + 8 * (e >> 2) + 4 * (l >> 5) > (l & 31)) m |= 1ull << l;
  return m;
}
template <int E> DI bool upper() { constexpr u64 m = upper_mask(E); return __builtin_amdgcn_inverse_ballot_w64(m); }
#define MX3(a, b, c) __builtin_fmaxf(__builtin_fmaxf((a), (b)), (c))
#define EX2(x) __builtin_amdgcn_exp2f(x)
template <int I> DI void sel_fill(f32x16& sel, const f32x16& sf, const f32x16& sd) {
  if constexpr (I < 16) { sel[I] = upper<I>() ? sf[I] : sd[I]; sel_fill<I + 1>(sel, sf, sd); }
}
#define WP_PRE(i) dma(40960 + w * 1024, lane * 16, rws, psrc + (i) * 8192)
#define PV(V, P) do { o = MFMA32(V, BF8(P), o); PIN(o); } while (0)
DI float half_max(const f32x16& s) {
  float m0 = __builtin_fmaxf(s[0], s[1]), m1 = __builtin_fmaxf(s[2], s[3]);
#pragma unroll
  for (int e = 4; e < 16; e += 4) { m0 = MX3(m0, s[e], s[e + 1]); m1 = MX3(m1, s[e + 2], s[e + 3]); }
  return __builtin_fmaxf(m0, m1);
}
DI float both_max(float m) { float mo = m; swap32f(m, mo); return __builtin_fmaxf(m, mo); }
DI void boundary_p(const f32x16& sel, float m, const u32 (&mk)[8], u32x4 (&pf)[2], u32x4 (&pd)[2]) {
#pragma unroll
  for (int e = 0; e < 8; ++e) {
    const u32 p = pk2(EX2(sel[2 * e] - m), EX2(sel[2 * e + 1] - m));
    pf[e >> 2][e & 3] = p & mk[e];
    pd[e >> 2][e & 3] = p & ~mk[e];
  }
}
DI f32x16 attn_exact(int ilo, bf16x8 qf, const u32 (&mk)[8], u32 kb, u32 vb0, u32 vb1) {
  const f32x16 zero = {};
  f32x16 sel, sf, sd = MFMA32(lds16(kb + 8192), qf, zero);
  if (ilo == 0) sf = MFMA32(lds16(kb), qf, zero);
  else {
#pragma unroll
    for (int e = 0; e < 16; ++e) sf[e] = -INFINITY;
  }
  sel_fill<0>(sel, sf, sd);
  float m = half_max(sel);
#pragma unroll 1
  for (int i = ilo > 1 ? ilo : 1; i < 8; ++i) m = __builtin_fmaxf(m, half_max(MFMA32(lds16(kb + i * 1024), qf, zero)));
  m = both_max(m);
  f32x16 negm;
#pragma unroll
  for (int e = 0; e < 16; ++e) negm[e] = -m;
  u32x4 pf[2], pd[2];
  boundary_p(sel, m, mk, pf, pd);
  f32x16 o = MFMA32(lds16(vb0 + 8 * 1040), BF8(pd[0]), zero);
  o = MFMA32(lds16(vb1 + 8 * 1040), BF8(pd[1]), o);
  if (ilo == 0) { o = MFMA32(lds16(vb0), BF8(pf[0]), o); o = MFMA32(lds16(vb1), BF8(pf[1]), o); }
#pragma unroll 1
  for (int i = ilo > 1 ? ilo : 1; i < 8; ++i) {
    const f32x16 sc = MFMA32(lds16(kb + i * 1024), qf, negm);
    u32x4 p0, p1;
#pragma unroll
    for (int e = 0; e < 4; ++e) { p0[e] = pk2(EX2(sc[2 * e]), EX2(sc[2 * e + 1])); p1[e] = pk2(EX2(sc[8 + 2 * e]), EX2(sc[8 + 2 * e + 1])); }
    o = MFMA32(lds16(vb0 + i * 1040), BF8(p0), o);
    o = MFMA32(lds16(vb1 + i * 1040), BF8(p1), o);
  }
  return o;
}
DI f32x16 attn_fast(int w, int lane, bf16x8 qf, const u32 (&mk)[8], const u32 (&ms)[16], u32 kb, u32 vb0, u32 vb1, rsrc_t rws, u32 psrc,
                    u32 pv, rsrc_t rwo, u32 r3) {
#define WO_DMA(i) dma(98304 + ((((i) ^ (r3 >> 8)) * 8 + w) * 1024), pv, rwo, (u32)((((i) ^ (r3 >> 8)) * 8 + w) ^ ((r3 & 7) << 1)) * 8192)
#define TWO(a, b) do { a; b; } while (0)
  const f32x16 zero = {};
  bf16x8 k8 = lds16(kb + 8192), k0 = lds16(kb), ka = lds16(kb + 1024), kc = lds16(kb + 2048);
  bf16x8 va = lds16(vb0 + 8 * 1040), vc = lds16(vb1 + 8 * 1040), vd = lds16(vb0), ve = lds16(vb1);
  SBAR();
  f32x16 sa = MFMA32(k8, qf, zero);
  PIN(sa); WO_DMA(0); SBAR();
  const float m = both_max(half_max(sa));
  f32x16 negm;
#pragma unroll
  for (int e = 0; e < 16; ++e) negm[e] = -m;
  PIN(negm); SBAR();
  f32x16 sd = MFMA32(k8, qf, negm), sb = MFMA32(k0, qf, negm);
  sa = MFMA32(ka, qf, negm);
  PIN(sd); PIN(sb); PIN(sa); WO_DMA(1); SBAR();
  f32x16 o = zero;
  u32x4 pd[2], pf[2], p0, p1;
#pragma unroll
  for (int e = 0; e < 8; ++e) {
    const float x0 = __uint_as_float((__float_as_uint(sb[2 * e]) & ms[2 * e]) | (__float_as_uint(sd[2 * e]) & ~ms[2 * e]));
    const float x1 = __uint_as_float((__float_as_uint(sb[2 * e + 1]) & ms[2 * e + 1]) | (__float_as_uint(sd[2 * e + 1]) & ~ms[2 * e + 1]));
    const u32 p = pk2(EX2(x0), EX2(x1));
    pf[e >> 2][e & 3] = p & mk[e];
    pd[e >> 2][e & 3] = p & ~mk[e];
  }
  PIN(pf[0]); PIN(pf[1]); PIN(pd[0]); PIN(pd[1]); WO_DMA(2); SBAR();
#define AT_Q(P, S, o_) do { P[(o_) & 3] = pk2(EX2(S[2 * (o_)]), EX2(S[2 * (o_) + 1])); P[((o_) & 3) + 1] = pk2(EX2(S[2 * (o_) + 2]), EX2(S[2 * (o_) + 3])); PIN(P); SBAR(); } while (0)
#define AT_STEP(S, N, KN, KR, i, M0, M1, M2, M3) do { \
    if ((i) < 7) { N = MFMA32(KN, qf, negm); PIN(N); if ((i) < 6) KR = lds16(kb + ((i) + 2) * 1024); SBAR(); } \
    u32x4 q0_, q1_; \
    AT_Q(q0_, S, 0); M0; SBAR(); AT_Q(q0_, S, 2); M1; SBAR(); AT_Q(q1_, S, 4); M2; SBAR(); AT_Q(q1_, S, 6); M3; \
    va = lds16(vb0 + (i) * 1040); vc = lds16(vb1 + (i) * 1040); p0 = q0_; p1 = q1_; SBAR(); } while (0)
  AT_STEP(sa, sb, kc, ka, 1, PV(va, pd[0]), PV(vc, pd[1]), PV(vd, pf[0]), PV(ve, pf[1]));
  AT_STEP(sb, sa, ka, kc, 2, WP_PRE(0), PV(va, p0), PV(vc, p1), WO_DMA(3));
  AT_STEP(sa, sb, kc, ka, 3, WP_PRE(1), PV(va, p0), PV(vc, p1), WO_DMA(4));
  AT_STEP(sb, sa, ka, kc, 4, WP_PRE(2), PV(va, p0), PV(vc, p1), WO_DMA(5));
  AT_STEP(sa, sb, kc, ka, 5, WP_PRE(3), PV(va, p0), PV(vc, p1), WO_DMA(6));
  AT_STEP(sb, sa, ka, kc, 6, WO_DMA(7), PV(va, p0), PV(vc, p1), NOP_);
  AT_STEP(sa, sb, kc, ka, 7, NOP_, PV(va, p0), PV(vc, p1), NOP_);
  PV(va, p0); PV(vc, p1);
  return o;
}
DI u32x4 attn_wave(bool full, int w, int lane, bf16x8 qf, const u32 (&mk)[8], const u32 (&ms)[16], u32 kb, u32 vb0, u32 vb1, rsrc_t rws, u32 psrc,
                   u32 pv, rsrc_t rwo, u32 r3) {
  f32x16 o = attn_fast(w, lane, qf, mk, ms, kb, vb0, vb1, rws, psrc, pv, rwo, r3);
  if (!__all(o[8] < 1.8446744e19f && o[8] > 5.4210109e-20f))
    o = attn_exact(full ? 0 : 8 - w, qf, mk, kb, vb0, vb1);
  const float rl = __builtin_amdgcn_rcpf(o[8]);
  u32 a0 = pk2(o[0] * rl, o[1] * rl), a1 = pk2(o[2] * rl, o[3] * rl), b0 = pk2(o[4] * rl, o[5] * rl), b1 = pk2(o[6] * rl, o[7] * rl);
  swap32u(a0, b0); swap32u(a1, b1);
  return (u32x4){a0, a1, b0, b1};
}

DI u32x4 resid_epi(float (&y)[8], u32x4 res, u32x4 gate) {
#pragma unroll
  for (int i = 0; i < 4; ++i) swap32f(y[i], y[4 + i]);
  u32x4 o;
#pragma unroll
  for (int i = 0; i < 4; ++i) {
    const u32 yb = pk2(y[2 * i], y[2 * i + 1]);
    const u32 tb = pk2(bflo(yb) * bflo(gate[i]), bfhi(yb) * bfhi(gate[i]));
    o[i] = pk2(bflo(res[i]) + bflo(tb), bfhi(res[i]) + bfhi(tb));
  }
  return o;
}

DI u32 rot28(u32 s, u32 r) { const u32 t = s + r; return t >= 28 ? t - 28 : t; }
#define WD_DMA(base) dma((u32)((base) + w) * 1024, pv, rwd, (u32)(((base) / 40) * 448 + (w & 1) * 8 + rot28((((base) % 40) + w) >> 1, r5) * 16) * 1024)

template <int NF>
DI void ffn_up(int w, int lane, u32 tag, char* ws, rsrc_t rws, rsrc_t rwd, u32 pv, int m128, int nt, u32 r5, u32 pollx, u32 wl, u32 wh, u32 xl, u32 stv, u32 adst, int b DBGARG) {
  constexpr int NW = NF;
  constexpr int F0 = NF == 4 ? 0 : 4;
  const int p = w & 3, tile = 4 * m128 + p;
  const u32 lv = lane * 16;
  const u32 wsrc = WS_WP + (u32)nt * WPN + w * 1024;
  const u32 xsrc = WS_X1 + (u32)(tile * 32 + 2 * (w >> 2)) * 1024;
  const u32 xdst = 114688 + (p * 4 + 2 * (w >> 2)) * 1024;
  const u32 r4 = (2 * nt + m128) & 7;
#define UP_W1(g, i) dma_sc1(((g) & 3) * 28672 + w * 1024 + (i) * 8192, lv, rws, wsrc + ((g) ^ r4) * 28672 + (i) * 8192)
#define UP_X1(g, i) dma_sc1(xdst + ((g) % 3) * 16384 + (i) * 1024, lv, rws, xsrc + ((g) ^ r4) * 4096 + (i) * 1024)
#pragma unroll
  for (int g = 1; g < 4; ++g)
#pragma unroll
    for (int i = 0; i < NW; ++i) UP_W1(g, i);
  STAMP(12);
  poll(pollx, rws, tag);
  STAMP(13);
#pragma unroll
  for (int g = 0; g < 3; ++g) { UP_X1(g, 0); UP_X1(g, 1); }
  f32x16 acc[NF];
#pragma unroll
  for (int f = 0; f < NF; ++f) acc[f] = (f32x16){};
  bf16x8 xa, wa[NF], xb, wb[NF];
  u32x4 s0, s1_, s2_, s3;
#define UP_RX(X, ks) X = lds16(xl + (((ks) >> 2) % 3) * 16384 + ((ks) & 3) * 1024)
#define UP_RW(W, f, ks) W[f] = lds16(((((ks) >> 2) & 2) ? wh : wl) + (((ks) >> 2) & 1) * 28672 + (((ks) & 3) * 7 + (f)) * 1024)
#define UP_STEP(XC, WC, XN, WN, ks, RD, MM, D0, D1) do { \
    if (RD) { UP_RX(XN, ks); UP_RW(WN, 0, ks); } SBAR(); if (MM) { acc[0] = MFMA32(WC[0], XC, acc[0]); PIN(acc[0]); } SBAR(); \
    if (RD) { UP_RW(WN, 1, ks); UP_RW(WN, 2, ks); } SBAR(); if (MM) { acc[1] = MFMA32(WC[1], XC, acc[1]); PIN(acc[1]); } SBAR(); \
    if (RD && NF == 4) UP_RW(WN, NF - 1, ks); D0; SBAR(); if (MM) { acc[2] = MFMA32(WC[2], XC, acc[2]); PIN(acc[2]); } SBAR(); \
    D1; if (MM && NF == 4) { acc[NF - 1] = MFMA32(WC[NF - 1], XC, acc[NF - 1]); PIN(acc[NF - 1]); } SBAR(); } while (0)
#define UP_WL(g) do { if (NF == 4) UP_W1(g, 3); } while (0)
#define UP_GROUP(g, MM0, A0, A1, B0, B1, C0, C1, E0, E1) do { \
    UP_STEP(xb, wb, xa, wa, 4 * (g), true, MM0, A0, A1); UP_STEP(xa, wa, xb, wb, 4 * (g) + 1, true, true, B0, B1); \
    UP_STEP(xb, wb, xa, wa, 4 * (g) + 2, true, true, C0, C1); UP_STEP(xa, wa, xb, wb, 4 * (g) + 3, true, true, E0, E1); } while (0)
#define UP_GREFILL(g, S0, S1) UP_GROUP(g, true, UP_X1((g) + 2, 0), UP_X1((g) + 2, 1), UP_W1((g) + 3, 0), S0, UP_W1((g) + 3, 1), UP_W1((g) + 3, 2), UP_WL((g) + 3), S1)
#define UP_E(i) dma_sc1(((i) * 8 + w) * 1024, lv, rws, WS_WP + (u32)nt * WPN + 32 * 7168 + ((i) * 8 + w) * 1024)
  WAITBAR(4); STAMP(24); UP_GROUP(0, false, NOP_, NOP_, NOP_, NOP_, NOP_, NOP_, NOP_, NOP_);
  WAITBAR(2); STAMP(25); UP_GREFILL(1, NOP_, NOP_);
  WAITBAR(NW + 2); STAMP(26); UP_GREFILL(2, NOP_, NOP_);
  WAITBAR(2 * NW + 2); STAMP(27); UP_GREFILL(3, NOP_, LD4X(s0, s1_, s2_, s3, stv, WS_ST + (u32)tile * 4096, 512));
  WAITBAR(2 * NW + 6); STAMP(28); UP_GREFILL(4, NOP_, NOP_);
  WAITBAR(2 * NW + 6); STAMP(29);
  UP_GROUP(5, true, UP_X1(7, 0), UP_X1(7, 1), UP_E(0), NOP_, UP_E(1), NOP_, WD_DMA(16), NOP_);
  asm volatile("s_waitcnt vmcnt(%4) lgkmcnt(0)\n\ts_barrier" : "+v"(s0), "+v"(s1_), "+v"(s2_), "+v"(s3) : "i"(NW + 5) : "memory");
  STAMP(30);
#pragma unroll 1
  for (u32 spins = 0; spins < SPIN_CAP; ++spins) {
    if (__all(tags_ok(s0, s1_, tag) && tags_ok(s2_, s3, tag))) break;
    LD4X(s0, s1_, s2_, s3, stv, WS_ST + (u32)tile * 4096, 512);
    WAIT4X(0, s0, s1_, s2_, s3);
  }
  float s1 = gsum(s0, s1_, s2_, s3, 0), s2 = gsum(s0, s1_, s2_, s3, 2);
  { float t1 = s1, t2 = s2; swap32f(s1, t1); swap32f(s2, t2); s1 += t1; s2 += t2; }
  const float mu = s1 * (1.f / 512.f);
  const float var = __builtin_fmaxf(s2 * (1.f / 512.f) - mu * mu, 0.f) + 1e-5f;
  float rstd = __builtin_amdgcn_rsqf(var);
  const float sig = var * rstd;
  const float mh = rbf(-mu), ml = -mu - mh, sh = rbf(sig), sl = sig - sh;
  u32x4 ev = {pk2(mh, mh), pk2(ml, sh), pk2(sh, sl), 0u};
  PIN(ev); PIN(rstd); SBAR();
  UP_GROUP(6, true, WD_DMA(24), WD_DMA(32), WD_DMA(40), NOP_, WD_DMA(48), NOP_, WD_DMA(144), NOP_);
  WAITBAR(6); STAMP(31);
  UP_GROUP(7, true, WD_DMA(56), WD_DMA(64), WD_DMA(72), NOP_, WD_DMA(112), NOP_, WD_DMA(120), NOP_);
#pragma unroll
  for (int f = 0; f < NF; ++f) wa[f] = lds16(wl + f * 1024);
  UP_STEP(xb, wb, xa, wa, 0, false, true, NOP_, NOP_);
#pragma unroll
  for (int f = 0; f < NF; ++f) wb[f] = lds16(wl + 7168 + f * 1024);
#pragma unroll
  for (int f = 0; f < NF; ++f) { acc[f] = MFMA32(wa[f], BF8(ev), acc[f]); PIN(acc[f]); }
  LBAR();
  STAMP(14);
#pragma unroll
  for (int f = 0; f < NF; ++f) acc[f] = MFMA32(wb[f], BF8(ev), acc[f]);
  WD_DMA(0); WD_DMA(8); WD_DMA(80); WD_DMA(88);
  SBAR();
  constexpr float C1 = -2.f * 0.7978845608028654f * 1.4426950408889634f, C2 = C1 * 0.044715f;
  const float k1 = C1 * rstd, k3 = C2 * rstd * rstd * rstd;
#pragma unroll
  for (int f = 0; f < NF; ++f) {
    float a[8];
#pragma unroll
    for (int e = 0; e < 8; ++e) {
      const float g = acc[f][e];
      const float z = g * __builtin_fmaf(g * g, k3, k1);
      a[e] = (g * acc[f][8 + e]) * __builtin_amdgcn_rcpf(__builtin_fmaf(__builtin_amdgcn_exp2f(z), var, var));
    }
    u32 a0 = pk2(a[0], a[1]), a1 = pk2(a[2], a[3]), b0 = pk2(a[4], a[5]), b1 = pk2(a[6], a[7]);
    swap32u(a0, b0); swap32u(a1, b1);
    st16_sc1((u32x4){a0, a1, b0, b1}, rws, adst + f * 1024);
    if (f == 0) { WD_DMA(128); WD_DMA(96); }
    if (f == 1) { WD_DMA(104); WD_DMA(136); }
    if (f == 2) WD_DMA(152);
    SBAR();
  }

}

__global__ void __launch_bounds__(512, 2)
fused_block(const void* __restrict__ px, const void* __restrict__ pwqkv, const void* __restrict__ pwo, const void* __restrict__ pwg,
            const void* __restrict__ pwu, const void* __restrict__ pwd, const void* __restrict__ pc, const void* __restrict__ pwm,
            void* pout, char* ws) {
  __shared__ __attribute__((aligned(16))) char smem[163840];
  asm volatile("" ::"v"((u32)(uintptr_t)(LAS char*)smem));
  const int tid = threadIdx.x, lane = tid & 63, w = __builtin_amdgcn_readfirstlane(tid >> 6);
  const int b = blockIdx.x, xcd = b & 7, bi = b >> 3;
  const rsrc_t rx = __builtin_amdgcn_make_buffer_rsrc((void*)px, 0, 2048 * 512 * 2, 0x00020000);
  const rsrc_t rqkv = __builtin_amdgcn_make_buffer_rsrc((void*)pwqkv, 0, 512 * 1536 * 2, 0x00020000);
  const rsrc_t rwo = __builtin_amdgcn_make_buffer_rsrc((void*)pwo, 0, 512 * 512 * 2, 0x00020000);
  const rsrc_t rwd = __builtin_amdgcn_make_buffer_rsrc((void*)pwd, 0, 1792 * 512 * 2, 0x00020000);
  const rsrc_t rout = __builtin_amdgcn_make_buffer_rsrc(pout, 0, 2048 * 512 * 2, 0x00020000);
  const rsrc_t rws = __builtin_amdgcn_make_buffer_rsrc(ws, 0, WS_BYTES, 0x00020000);
  const u32 lv = lane * 16;
  STAMP(0);

  const int m64a = 8 * (xcd & 3) + (bi & 7), grp = 4 * (xcd >> 2) + (bi >> 3);
  constexpr u32 SCR = 155648;
  const rsrc_t rc = __builtin_amdgcn_make_buffer_rsrc((void*)pc, 0, 512 * 2, 0x00020000);
  const rsrc_t rwm = __builtin_amdgcn_make_buffer_rsrc((void*)pwm, 0, 512 * 3072 * 2, 0x00020000);
  if (w == 0) {
    __builtin_amdgcn_s_setprio(2);
    u32 cv[4], seed; u32x4 m[4];
    asm volatile("s_load_dword %0, %1, 0x0" : "=s"(seed) : "s"(ws) : "memory");
    const u32 r0 = (u32)(bi >> 3) * 128 + (lane >> 1);
#pragma unroll
    for (int i = 0; i < 4; ++i) ld16(m[i], r0 * 6144 + (lane & 1) * 16, rwm, (u32)(xcd * 8 + (bi & 7)) * 32 + i * (32 * 6144));
#pragma unroll
    for (int i = 0; i < 4; ++i) ld2(cv[i], r0 * 2, rc, i * 64);
    asm volatile("s_waitcnt vmcnt(0) lgkmcnt(0)" : "+v"(cv[0]), "+v"(cv[1]), "+v"(cv[2]), "+v"(cv[3]), "+v"(m[0]), "+v"(m[1]), "+v"(m[2]), "+v"(m[3]), "+s"(seed)::"memory");
    float v[8] = {};
#pragma unroll
    for (int i = 0; i < 4; ++i) {
      const float sc = silu_bf(bflo(cv[i]));
#pragma unroll
      for (int e = 0; e < 4; ++e) { v[2 * e] = __builtin_fmaf(sc, bflo(m[i][e]), v[2 * e]); v[2 * e + 1] = __builtin_fmaf(sc, bfhi(m[i][e]), v[2 * e + 1]); }
    }
#pragma unroll
    for (int j = 0; j < 4; ++j) { swap32f(v[j], v[j + 4]); v[j] += v[j + 4]; }
#pragma unroll
    for (int j = 0; j < 2; ++j) { swap16f(v[j], v[j + 2]); v[j] += v[j + 2]; }
    const bool o = lane & 8;
    float t = (o ? v[1] : v[0]) + dppf<0x128>(o ? v[0] : v[1]);
    t += dppf<0x4E>(t);
    t += __builtin_bit_cast(float, __builtin_amdgcn_update_dpp(__builtin_amdgcn_update_dpp(0, __builtin_bit_cast(int, t), 0x104, 0xf, 0x5, false), __builtin_bit_cast(int, t), 0x114, 0xf, 0xa, false));
    const u32 a = WS_MODC + (u32)((lane >> 1) & 3) * 32768 + (u32)(bi >> 3) * 8192 + (u32)((xcd * 8 + (bi & 7)) * 16 + (lane & 1) * 8 + (lane >> 3)) * 8;
    st_gran(ws, a, t, seed + 1u); st_gran(ws, a + 131072, t, seed + 1u);
  }
  STAMP(1);
  u32 seed;
  asm volatile("s_load_dword %0, %1, 0x0" : "=s"(seed) : "s"(ws) : "memory");
  u32x4 xr[8];
#pragma unroll
  for (int j = 0; j < 8; ++j) ld16(xr[j], lv, rx, (u32)(m64a * 64 + w * 8 + j) * 1024);
  u32 cv1; u32x4 ml;
  {
    const u32 r1 = (u32)(bi >> 2) * 64 + w * 8 + (lane >> 3);
    ld2(cv1, r1 * 2, rc, 0);
    ld16(ml, r1 * 6144 + (lane & 7) * 16, rwm, 2048 + (u32)(xcd * 4 + (bi & 3)) * 128);
  }
  const u32 r1 = bi & 7;
  const int n64 = bi >> 2, m64 = 4 * xcd + (bi & 3);
  const int mh = w & 1, ksl = w >> 1, tile3 = 2 * m64 + mh;
  const int head2 = 4 * xcd + (bi & 3), range = bi >> 2;
  const int m128 = 8 * (xcd >> 2) + (bi & 7), nt = 4 * (xcd & 3) + (bi >> 3);
  const int wid = xcd * 28 + bi, wq = wid & 3, wmat = wid >= 112, wg = (wid >> 2) - wmat * 28;
  constexpr u32 SC2 = 53248, SC3 = 94208;
  u32 qv, ab[4], bl, bh, aw[8], pv, pg, gvo, xro, tb[2], mk[8], ms[16], kb, vb0, vb1, wdst, ta, qdst, odst, xdst3, adst;
  u32 postq, posto, postw, postx, posta, pollq, pollo, pollw, pollx, polla, rbw[3], rbr, wl, xl, stv;
  const int wm = w >> 2, hn = w & 3, fr = lane & 15, g = lane >> 4;
  {
    qv = (u32)(w * 8 + (lane >> 3)) * 3072 + (((lane & 7) ^ ((((lane >> 4) & 1) | ((w & 1) << 1)) << 1)) * 16) + grp * 128;
#pragma unroll
    for (int t = 0; t < 4; ++t) ab[t] = (u32)(wm * 32 + fr) * 1024 + ((g ^ (fr & 3)) * 16) + ((t ^ (fr >> 2)) * 64);
    bl = 65536 + (u32)(8 * g + (fr >> 2)) * 128 + ((hn ^ (((fr >> 3) & 1) | ((g & 1) << 1))) * 32) + (lane & 3) * 8; bh = bl + 49152;
#pragma unroll
    for (int j = 0; j < 8; ++j) aw[j] = (u32)(w * 8 + j) * 1024 + ((lane ^ (r1 << 3) ^ ((w * 8 + j) & 15)) * 16);
    pg = (u32)(lane >> 3) * 3584 + (((lane & 7) ^ (((lane >> 4) & 1) << 2)) * 16) + (u32)(wq * 128 + w * 16) * 3584 + wg * 128;
    gvo = (u32)((lane >> 3) & 1) * 65536 + (lane & 7) * 16 + 8 * (lane < 32 ? 512 + (lane >> 4) * 512 + wq * 128 + w * 16 : (lane < 48 ? 0 : 1536) + n64 * 64 + ksl * 16);
    xro = (u32)(tile3 * 32 + (lane & 31)) * 1024 + (u32)(n64 * 64 + ksl * 16 + (lane >> 5) * 8) * 2;
    qdst = (u32)((4 * grp + hn) * 64 + 2 * m64a + wm) * 1024 + lv;
    PIN(qv); PIN(bl); PIN(bh); PIN(pg); PIN(gvo); PIN(xro); PIN(qdst);
#pragma unroll
    for (int t = 0; t < 4; ++t) PIN(ab[t]);
#pragma unroll
    for (int j = 0; j < 8; ++j) PIN(aw[j]);
  }
#define QKV_DMA(s, seg) dma(65536 + ((s) & 3) * 24576 + (seg) * 8192 + w * 1024, qv, rqkv, ((s) ^ r1) * (64 * 3072) + (seg) * 1024)
  asm volatile("s_waitcnt vmcnt(2) lgkmcnt(0)" : "+s"(seed)::"memory");
  const u32 tag = seed + 1u;
  u32x4 a0, a1, a2, a3;
  f32x2 xn[8][4];
  {
    asm volatile("" : "+v"(xr[0]), "+v"(xr[1]), "+v"(xr[2]), "+v"(xr[3]), "+v"(xr[4]), "+v"(xr[5]), "+v"(xr[6]), "+v"(xr[7])::"memory");
    float S[8], Q[8];
#pragma unroll
    for (int j = 0; j < 8; ++j) {
#pragma unroll
      for (int e = 0; e < 4; ++e) xn[j][e] = (f32x2){bflo(xr[j][e]), bfhi(xr[j][e])};
      const f32x2 s2 = (xn[j][0] + xn[j][1]) + (xn[j][2] + xn[j][3]);
      const f32x2 q2 = __builtin_elementwise_fma(xn[j][0], xn[j][0], __builtin_elementwise_fma(xn[j][1], xn[j][1], __builtin_elementwise_fma(xn[j][2], xn[j][2], xn[j][3] * xn[j][3])));
      S[j] = s2[0] + s2[1]; Q[j] = q2[0] + q2[1];
    }
#pragma unroll
    for (int j = 0; j < 4; ++j) { swap32f(S[j], S[j + 4]); S[j] += S[j + 4]; swap32f(Q[j], Q[j + 4]); Q[j] += Q[j + 4]; }
#pragma unroll
    for (int j = 0; j < 2; ++j) { swap16f(S[j], S[j + 2]); S[j] += S[j + 2]; swap16f(Q[j], Q[j + 2]); Q[j] += Q[j + 2]; }
    float ra[2], rb[2];
#pragma unroll
    for (int j = 0; j < 2; ++j) {
      const float mu = row16_sum(S[j]) * (1.f / 512.f);
      const float var = __builtin_fmaxf(row16_sum(Q[j]) * (1.f / 512.f) - mu * mu, 0.f) + 1e-5f;
      ra[j] = __builtin_amdgcn_rsqf(var); rb[j] = -mu * ra[j];
    }
    SBAR();
    LD4X(a0, a1, a2, a3, lv, WS_MODC + (u32)xcd * 32768 + w * 1024, 8192);
    SBAR();
#pragma unroll
    for (int j = 0; j < 8; ++j) {
      const float a = rdlane(ra[j & 1], (j >> 1) * 16), c = rdlane(rb[j & 1], (j >> 1) * 16);
#pragma unroll
      for (int e = 0; e < 4; ++e) { xn[j][e] = __builtin_elementwise_fma(xn[j][e], (f32x2){a, a}, (f32x2){c, c}); PIN(xn[j][e]); }
    }
  }
  asm volatile("s_waitcnt vmcnt(4)" : "+v"(cv1), "+v"(ml)::"memory");
  {
    const float sc = silu_bf(bflo(cv1));
    float v[8];
#pragma unroll
    for (int e = 0; e < 4; ++e) { v[2 * e] = sc * bflo(ml[e]); v[2 * e + 1] = sc * bfhi(ml[e]); }
#pragma unroll
    for (int j = 0; j < 4; ++j) { swap32f(v[j], v[j + 4]); v[j] += v[j + 4]; }
#pragma unroll
    for (int j = 0; j < 2; ++j) { swap16f(v[j], v[j + 2]); v[j] += v[j + 2]; }
    const bool o = lane & 8;
    const float a = (o ? v[1] : v[0]) + dppf<0x128>(o ? v[0] : v[1]);
    sts4f(SCR + 1024 + ((lane & 7) * 8 + (lane >> 3)) * 32 + w * 4, a);
  }
  STAMP(22);
  QKV_DMA(0, 0); QKV_DMA(0, 1); QKV_DMA(0, 2); QKV_DMA(1, 0); QKV_DMA(1, 1); QKV_DMA(1, 2);
  {
    WAIT4X(6, a0, a1, a2, a3);
#pragma unroll 1
    for (u32 spins = 0; spins < SPIN_CAP; ++spins) {
      if (__all(tags_ok(a0, a1, tag) && tags_ok(a2, a3, tag))) break;
      LD4X(a0, a1, a2, a3, lv, WS_MODC + (u32)xcd * 32768 + w * 1024, 8192);
      WAIT4X(0, a0, a1, a2, a3);
    }
    if (w == 0) __builtin_amdgcn_s_setprio(0);
    *(LAS u32*)(uintptr_t)(SCR + 4096 + tid * 4) = pk2(gsum(a0, a1, a2, a3, 0), gsum(a0, a1, a2, a3, 2));
  }
  STAMP(23);
  LBAR();
  STAMP(2);
  {
    const u32x4 sa = lds16u(SCR + 4096 + lv), ca = lds16u(SCR + 5120 + lv);
    f32x2 fs[4], fa[4];
#pragma unroll
    for (int e = 0; e < 4; ++e) {
      const u32 t = pk2(1.f + bflo(ca[e]), 1.f + bfhi(ca[e]));
      fs[e] = (f32x2){bflo(t), bfhi(t)}; fa[e] = (f32x2){bflo(sa[e]), bfhi(sa[e])};
    }
#pragma unroll
    for (int j = 0; j < 8; ++j) {
      u32x4 h;
#pragma unroll
      for (int e = 0; e < 4; ++e) { const f32x2 t = __builtin_elementwise_fma(xn[j][e], fs[e], fa[e]); h[e] = pk2(t[0], t[1]); }
      sts16(aw[j], h);
    }
    if (w == 1) st_gran(ws, WS_MODL + (u32)(bi >> 2) * 16384 + (u32)((xcd * 4 + (bi & 3)) * 64 + lane) * 8,
                        sum8(lds16f(SCR + 1024 + lane * 32), lds16f(SCR + 1024 + lane * 32 + 16)), tag);
  }
  const rsrc_t rwg = __builtin_amdgcn_make_buffer_rsrc((void*)(wmat ? pwu : pwg), 0, bi < 28 ? 512 * 1792 * 2 : 0, 0x00020000);
#define WG_DMA(i) dma(65536 + w * 2048 + (i) * 1024, pg, rwg, (i) * (8 * 3584))
  u32x4 g0, g1, g2, g3, xres;
  {
    f32x4 acc[2][3] = {};
    bf16x8 fa[2][2][2], fb[2][2][3];
#define Q_RA(S, s, kk, m) fa[S][kk][m] = lds16(ab[(2 * (s) + (kk)) & 3] + ((2 * (s) + (kk)) >> 2) * 256 + (m) * 16384)
#define Q_RB(S, s, kk, sg) fb[S][kk][sg] = ldtr((((s) & 2) ? bh : bl) + ((s) & 1) * 24576 + (sg) * 8192 + (kk) * 4096)
#define Q_M(S, kk, m, sg) do { acc[m][sg] = (sg) < 2 ? MFMA16(fb[S][kk][sg], fa[S][kk][m], acc[m][sg]) : MFMA16(fa[S][kk][m], fb[S][kk][sg], acc[m][sg]); PIN(acc[m][sg]); } while (0)
#define Q_STAGE(C, N, s, RD, D0, D1, D2, D3) do { \
      if (RD) { Q_RA(N, s, 0, 0); Q_RA(N, s, 0, 1); } Q_M(C, 0, 0, 0); SBAR(); \
      if (RD) Q_RB(N, s, 0, 0); Q_M(C, 0, 0, 1); SBAR(); \
      if (RD) Q_RB(N, s, 0, 1); Q_M(C, 0, 0, 2); SBAR(); \
      if (RD) Q_RB(N, s, 0, 2); Q_M(C, 0, 1, 0); SBAR(); \
      if (RD) { Q_RA(N, s, 1, 0); Q_RA(N, s, 1, 1); } Q_M(C, 0, 1, 1); SBAR(); \
      if (RD) Q_RB(N, s, 1, 0); Q_M(C, 0, 1, 2); SBAR(); \
      if (RD) Q_RB(N, s, 1, 1); Q_M(C, 1, 0, 0); SBAR(); \
      if (RD) Q_RB(N, s, 1, 2); Q_M(C, 1, 0, 1); SBAR(); \
      D0; Q_M(C, 1, 0, 2); SBAR(); D1; Q_M(C, 1, 1, 0); SBAR(); D2; Q_M(C, 1, 1, 1); SBAR(); D3; Q_M(C, 1, 1, 2); SBAR(); } while (0)
    WAITBAR(0);
    QKV_DMA(2, 0); QKV_DMA(2, 1); QKV_DMA(2, 2);
#pragma unroll
    for (int kk = 0; kk < 2; ++kk) { Q_RA(0, 0, kk, 0); Q_RA(0, 0, kk, 1); Q_RB(0, 0, kk, 0); Q_RB(0, 0, kk, 1); Q_RB(0, 0, kk, 2); }
    STAMP(20);
    LBAR();
    Q_STAGE(0, 1, 1, true, QKV_DMA(3, 0), QKV_DMA(3, 1), QKV_DMA(3, 2), QKV_DMA(4, 0));
    WAITBAR(4); Q_STAGE(1, 0, 2, true, QKV_DMA(4, 1), QKV_DMA(4, 2), QKV_DMA(5, 0), QKV_DMA(5, 1));
    WAITBAR(5); Q_STAGE(0, 1, 3, true, QKV_DMA(5, 2), QKV_DMA(6, 0), QKV_DMA(6, 1), QKV_DMA(6, 2));
    WAITBAR(6); Q_STAGE(1, 0, 4, true, QKV_DMA(7, 0), QKV_DMA(7, 1), QKV_DMA(7, 2), NOP_);
    WAITBAR(6); Q_STAGE(0, 1, 5, true, WG_DMA(0), WG_DMA(1), ld16(xres, xro, rx, 0), NOP_);
    WAITBAR(6); Q_STAGE(1, 0, 6, true, LD4X(g0, g1, g2, g3, gvo, WS_MODL, 16384), NOP_, NOP_, NOP_);
    WAITBAR(7); Q_STAGE(0, 1, 7, true, NOP_, NOP_, NOP_, NOP_);
    Q_STAGE(1, 0, 0, false, NOP_, NOP_, NOP_, NOP_);
    STAMP(3);
    constexpr float QS = 0.25f * 1.4426950408889634f;
    {
      u32 p0 = pk2(acc[0][0][0] * QS, acc[0][0][1] * QS), p1 = pk2(acc[0][0][2] * QS, acc[0][0][3] * QS);
      u32 r0 = pk2(acc[1][0][0] * QS, acc[1][0][1] * QS), r1 = pk2(acc[1][0][2] * QS, acc[1][0][3] * QS);
      swap16u(p0, r0); swap16u(p1, r1);
      st16_sc1((u32x4){p0, p1, r0, r1}, rws, WS_Q + qdst);
    }
    {
      u32 p0 = pk2(acc[0][1][0], acc[0][1][1]), p1 = pk2(acc[0][1][2], acc[0][1][3]);
      u32 r0 = pk2(acc[1][1][0], acc[1][1][1]), r1 = pk2(acc[1][1][2], acc[1][1][3]);
      swap16u(p0, r0); swap16u(p1, r1);
      st16_sc1((u32x4){p0, p1, r0, r1}, rws, WS_K + qdst);
    }
    {
      u32 p0 = pk2(acc[0][2][0], acc[0][2][1]), p1 = pk2(acc[0][2][2], acc[0][2][3]);
      u32 r0 = pk2(acc[1][2][0], acc[1][2][1]), r1 = pk2(acc[1][2][2], acc[1][2][3]);
      swap32u(p0, r0); swap32u(p1, r1);
      st16_sc1((u32x4){p0, p1, r0, r1}, rws, WS_V + qdst);
    }
  }
  u32x4 gav, gbv, x1v, q;
  {
    pv = (u32)(lane >> 3) * 1024 + (((lane & 7) ^ (((lane >> 4) & 1) << 2)) * 16) + n64 * 128;
#pragma unroll
    for (int e = 0; e < 16; ++e) ms[e] = (e & 3) + 8 * (e >> 2) + 4 * (lane >> 5) > (lane & 31) ? 0xffffffffu : 0u;
#pragma unroll
    for (int e = 0; e < 8; ++e) mk[e] = (ms[2 * e] & 0xffffu) | (ms[2 * e + 1] & 0xffff0000u);
    kb = (u32)(w * 1024 + lane * 16);
    vb0 = 16384 + w * 1040 + ((lane & 31) < 16 ? (lane >> 5) * 256 + (lane & 31) * 16 : 1024);
    vb1 = 16384 + w * 1040 + ((lane & 31) < 16 ? 512 + (lane >> 5) * 256 + (lane & 31) * 16 : 1024);
    const int wcu = wg * 4 + g, wn = wcu / 7, wfr = wcu - wn * 7;
    wdst = WS_WP + (u32)wn * WPN + wfr * 1024 + (u32)(wmat * 16 + fr) * 16;
    ta = 65536 + w * 2048 + (u32)(fr >> 2) * 128 + ((g ^ ((fr >> 2) & 2)) * 32) + (lane & 3) * 8;
    odst = WS_O + (u32)((8 * range + w) * 32 + head2) * 1024 + lv;
    const int hd = 4 * grp + (lane & 3), rg = (m64a >> 2) + ((lane >> 2) & 1);
    postq = lane < 4 || (lane < 8 && rg < 8) ? mbox(hd >> 2, (rg << 2) | (hd & 3), MB_QKV + (m64a & 3) + (lane < 4 ? 4 : 0)) : lane == 8 ? WS_ARR + b * 4 : NOWHERE;
    posto = lane < 32 ? mbox(range, lane, MB_O + head2) : NOWHERE;
    const int wnp = (wg * 4 + (lane < 16 ? 0 : 3)) / 7;
    postw = lane < 32 ? mbox(((fr >> 3) << 2) | (wnp >> 2), ((wnp & 3) << 3) | (lane & 7), wmat * 12 + (wg - ((7 * wnp) >> 2)) * 4 + wq) : NOWHERE;
    pollq = mbox(xcd, bi, MB_QKV + (w >> 1) + ((lane & 1) || range == 0 ? 4 : 0));
    const int l24 = (lane & 31) < 24 ? (lane & 31) : 0, l12 = l24 < 12 ? l24 : l24 - 12;
    const int wslot = l12 < 4 * (((7 * nt + 6) >> 2) - ((7 * nt) >> 2) + 1) ? l24 : 0;
    pollw = mbox(xcd, bi, MB_W + wslot);
    pollx = mbox(xcd, bi, lane < 32 ? MB_X1 + ((w >> 1) & 1) * 8 + (lane & 7) : MB_E + wslot);
    PIN(pv); PIN(kb); PIN(vb0); PIN(vb1); PIN(wdst); PIN(ta); PIN(odst); PIN(postq); PIN(posto); PIN(postw); PIN(pollq); PIN(pollw); PIN(pollx);
#pragma unroll
    for (int j = 0; j < 8; ++j) PIN(mk[j]);
#pragma unroll
    for (int e = 0; e < 16; ++e) PIN(ms[e]);
  }
  {
    asm volatile("s_waitcnt vmcnt(0) lgkmcnt(0)\n\ts_barrier" : "+v"(g0), "+v"(g1), "+v"(g2), "+v"(g3), "+v"(xres)::"memory");
    if (w == 0) post(postq, rws, tag);
    STAMP(4);
    if (tid < 16) { const u32 o1 = tid >= 8 || range > 0 ? 0x3f803f80u : 0u; sts16(16384 + tid * 1040 + 1024, (u32x4){o1, o1, o1, o1}); }
#pragma unroll 1
    for (u32 spins = 0; spins < SPIN_CAP; ++spins) {
      if (__all(tags_ok(g0, g1, tag) && tags_ok(g2, g3, tag))) break;
      __builtin_amdgcn_s_sleep(1);
      LD4X(g0, g1, g2, g3, gvo, WS_MODL, 16384);
      WAIT4X(0, g0, g1, g2, g3);
    }
    const u32 gs = 49152 + w * 128;
    {
      float t0 = gsum(g0, g1, g2, g3, 0), t1 = gsum(g0, g1, g2, g3, 2);
      t0 += dppf<0x128>(t0); t1 += dppf<0x128>(t1);
      u32 t = pk2(t0, t1);
      if ((lane >> 4) == 1) t = pk2(1.f + bflo(t), 1.f + bfhi(t));
      if ((lane & 8) == 0) *(LAS u32*)(uintptr_t)(gs + (lane >> 4) * 32 + (lane & 7) * 4) = t;
      asm volatile("s_waitcnt lgkmcnt(0)" ::: "memory");
    }
    gav = lds16u(gs + 64 + (lane >> 5) * 16); gbv = lds16u(gs + 96 + (lane >> 5) * 16);
    if (bi < 28) {
      float u = 0.f, bs = 0.f;
#pragma unroll
      for (int h = 0; h < 2; ++h) {
        const u32x4 wv = __builtin_bit_cast(u32x4, ldtr(ta + h * 1024)), sb = lds16u(gs + h * 16), cb = lds16u(gs + 32 + h * 16);
        u32x4 e;
#pragma unroll
        for (int j = 0; j < 4; ++j) {
          e[j] = pk2(bflo(wv[j]) * bflo(cb[j]), bfhi(wv[j]) * bfhi(cb[j]));
          u += bflo(e[j]) + bfhi(e[j]);
          bs = __builtin_fmaf(bflo(wv[j]), bflo(sb[j]), __builtin_fmaf(bfhi(wv[j]), bfhi(sb[j]), bs));
        }
        st16_sc1(e, rws, wdst + h * 512 + (u32)(wq * 8 + w) * 7168);
      }
      sts4f(SC2 + lane * 32 + w * 4, u); sts4f(SC2 + 2048 + lane * 32 + w * 4, bs);
    }
    STAMP(5);
    poll(pollq, rws, tag);
    STAMP(6);
    const u32 src = (u32)(head2 * 64 + 8 * range + w) * 1024;
    const rsrc_t rh = __builtin_amdgcn_make_buffer_rsrc(ws, 0, range > 0 ? WS_BYTES : 0, 0x00020000);
    dma_sc1((w + 8) * 1024, lv, rws, WS_K + src);
    ld16_sc1(q, lv, rws, WS_Q + src);
    dma_sc1(w * 1024, lv, rh, WS_K + src - 8192);
    dma_sc1(16384 + (w + 8) * 1040, lv, rws, WS_V + src);
    dma_sc1(16384 + w * 1040, lv, rh, WS_V + src - 8192);
  }
  {
    asm volatile("s_waitcnt vmcnt(0) lgkmcnt(0)\n\ts_barrier" : "+v"(q)::"memory");
    STAMP(7);
    if (bi < 28 && w == 7) {
      post(postw + MB_W * 4, rws, tag);
      const float t = sum8(lds16f(SC2 + lane * 32), lds16f(SC2 + lane * 32 + 16)), o = sum8(lds16f(SC2 + 2048 + lane * 32), lds16f(SC2 + 2048 + lane * 32 + 16));
      const float uh = rbf(t), ul = t - uh, bh = rbf(o), bl = o - bh;
      st16_sc1((u32x4){pk2(uh, ul), pk2(uh, bh), pk2(bl, bh), 0u}, rws, wdst + (wq & 1) * 512 + (u32)(32 + (wq >> 1)) * 7168);
    }
    const u32x4 ov = attn_wave(range > 0, w, lane, BF8(q), mk, ms, kb, vb0, vb1, PRE_ARGS,
                               pv, rwo, (u32)n64 | (u32)(bi & 3) << 9);
    st16_sc1(ov, rws, odst);
    STAMP(8);
    u32 fw;
    ld4_sc1(fw, pollw, rws, 0);
    {
#pragma unroll
      for (int c = 0; c < 2; ++c) tb[c] = (u32)(8 * (lane >> 5) + (fr >> 2)) * 128 + ((((2 * c + (g & 1) + ksl) & 3) ^ ((fr >> 2) & 2)) * 32) + (lane & 3) * 8;
      xdst3 = WS_X1 + (u32)(tile3 * 32 + n64 * 4 + ksl) * 1024 + lv;
      adst = WS_ACT + (u32)((4 * m128 + hn) * 112 + nt * 7 + (w & 4)) * 1024 + lv;
      postx = lane < 16 ? mbox(((m64 >> 4) << 2) | (lane >> 2), ((lane & 3) << 3) | ((m64 >> 1) & 7), MB_X1 + (m64 & 1) * 8 + n64) : NOWHERE;
      posta = lane < 16 ? mbox(m128 >> 1, ((lane >> 1) << 2) | ((2 * m128 + (lane & 1)) & 3), MB_ACT + nt) : NOWHERE;
      pollo = mbox(xcd, bi, MB_O + ksl * 8 + (lane & 7));
      polla = mbox(xcd, bi, MB_ACT + ksl * 4 + (lane & 3));
#pragma unroll
      for (int j = 1; j < 4; ++j) rbw[j - 1] = (mh ? 36864 : 65536) + (u32)(((ksl + j) & 3) * 3 + j - 1) * 2048 + lv;
      rbr = (mh ? 36864 : 65536) + (u32)ksl * 6144 + lv;
      wl = lv + (w & 4) * 1024; xl = lv + 114688 + hn * 4096;
      stv = (u32)(lane >> 5) * 2048 + (lane & 31) * 16;
      PIN(tb[0]); PIN(tb[1]); PIN(xdst3); PIN(adst); PIN(postx); PIN(posta); PIN(pollo); PIN(polla); PIN(rbw[0]); PIN(rbw[1]); PIN(rbw[2]); PIN(rbr); PIN(wl); PIN(xl); PIN(stv);
    }
    asm volatile("s_waitcnt vmcnt(0) lgkmcnt(0)\n\ts_barrier" : "+v"(fw)::"memory");
    if (w == 0) post(posto, rws, tag);
    if (bi < 28 && w == 7) post(postw + MB_E * 4, rws, tag);
    STAMP(21);
    if (!__all(fw == tag)) poll(pollw, rws, tag);
#pragma unroll
    for (int i = 0; i < 4; ++i) if (i < 3 || w < 4) dma_sc1(w * 1024 + i * 8192, lv, rws, WS_WP + (u32)nt * WPN + ((2 * nt + m128) & 7) * 28672 + w * 1024 + i * 8192);
    bf16x8 wf[8][2];
#pragma unroll
    for (int ks = 0; ks < 8; ++ks)
#pragma unroll
      for (int c = 0; c < 2; ++c) wf[ks][c] = ldtr(98304 + ksl * 16384 + tb[c] + ks * 2048);
    if (b == 255 && w == 1) {
#pragma unroll 1
      for (int i = 0; i < 4; ++i) poll(WS_ARR + i * 256 + lane * 4, rws, tag);
      if (lane == 0) __hip_atomic_store((GAS u32*)(ws + WS_SEED), tag, RLX_AGENT);
    }
    poll(pollo, rws, tag);
    STAMP(9);
    u32x4 of[8];
#pragma unroll
    for (int ks = 0; ks < 8; ++ks) ld16_sc1(of[ks], lv, rws, WS_O + (u32)(tile3 * 32 + ksl * 8 + (ks ^ n64)) * 1024);
    f32x16 acc[2] = {};
#pragma unroll
    for (int ks = 0; ks < 8; ++ks) {
      WAITV1(7 - ks, of[ks]);
      acc[0] = MFMA32(wf[ks][0], BF8(of[ks]), acc[0]);
      acc[1] = MFMA32(wf[ks][1], BF8(of[ks]), acc[1]);
      PIN(acc[0]); PIN(acc[1]);
    }
#pragma unroll
    for (int j = 1; j < 4; ++j) {
      const u32 a = rbw[j - 1];
      const int o = (j & 1) * 8;
      sts16f(a, (f32x4){acc[j >> 1][o], acc[j >> 1][o + 1], acc[j >> 1][o + 2], acc[j >> 1][o + 3]});
      sts16f(a + 1024, (f32x4){acc[j >> 1][o + 4], acc[j >> 1][o + 5], acc[j >> 1][o + 6], acc[j >> 1][o + 7]});
    }
    LBAR();
    float y[8];
#pragma unroll
    for (int e = 0; e < 8; ++e) y[e] = acc[0][e];
#pragma unroll
    for (int j = 0; j < 3; ++j) {
      const f32x4 a = lds16f(rbr + j * 2048), d = lds16f(rbr + j * 2048 + 1024);
#pragma unroll
      for (int e = 0; e < 4; ++e) { y[e] += a[e]; y[4 + e] += d[e]; }
    }
    x1v = resid_epi(y, xres, gav);
    st16_sc1(x1v, rws, xdst3);
    STAMP(10);
    {
      float s = 0.f, q2 = 0.f;
#pragma unroll
      for (int e = 0; e < 4; ++e) { const float a = bflo(x1v[e]), c = bfhi(x1v[e]); s += a + c; q2 = __builtin_fmaf(a, a, __builtin_fmaf(c, c, q2)); }
      swap32f(s, q2);
      sts4f(SC3 + (u32)(mh * 1024 + ksl * 256 + (lane & 31) * 8 + (lane >> 5) * 4), s + q2);
    }
    WAITBAR(0);
    if (w == 2) post(postx, rws, tag);
    if (ksl == 0) {
      const u32 a = SC3 + (u32)(mh * 1024 + (lane & 31) * 8 + (lane >> 5) * 4);
      const float t = (lds4f(a) + lds4f(a + 256)) + (lds4f(a + 512) + lds4f(a + 768));
      st_gran(ws, WS_ST + (u32)(tile3 * 8 + n64) * 512 + (lane & 31) * 16 + (lane >> 5) * 8, t, tag);
      asm volatile("s_waitcnt lgkmcnt(0)" ::: "memory");
    }
    STAMP(11);
  }
  const u32 r5 = (u32)(((n64 * 7) >> 1) + 7 * (bi & 3)) % 28;
  u32 wh = wl + 57344;
  PIN(wh);
  if (w < 4) ffn_up<4>(w, lane, tag, ws, rws, rwd, pv, m128, nt, r5, pollx, wl, wh, xl, stv, adst, b DBGPASS);
  else ffn_up<3>(w, lane, tag, ws, rws, rwd, pv, m128, nt, r5, pollx, wl, wh, xl, stv, adst, b DBGPASS);
  STAMP(16);
  {
    u32 t0 = tb[0] + ksl * 40960, t1 = tb[1] + ksl * 40960;
    PIN(t0); PIN(t1);
    WAITBAR(0);
    if (w == 0) post(posta, rws, tag);
    bf16x8 wp[8][2];
#pragma unroll
    for (int s = 0; s < 8; ++s) { wp[s][0] = ldtr(t0 + s * 2048); wp[s][1] = ldtr(t1 + s * 2048); }
    LBAR();
#define DN_RF(i) dma((u32)((w >> 1) * 40 + (w & 1) * 8 + (i)) * 1024, pv, rwd, (u32)((w >> 1) * 448 + ((i) & 1) * 8 + rot28(20 + (w & 1) * 4 + ((i) >> 1), r5) * 16) * 1024)
    DN_RF(0); DN_RF(1); DN_RF(2); DN_RF(3);
    DN_RF(4); DN_RF(5); DN_RF(6); DN_RF(7);
    STAMP(15);
    poll(polla, rws, tag);
    STAMP(17);
    u32x4 af[28];
    const u32 asrc = WS_ACT + (u32)(tile3 * 112 + ksl * 28) * 1024;
#pragma unroll
    for (int ks = 0; ks < DEP; ++ks) ld16_sc1(af[ks], lv, rws, asrc + rot28(ks, r5) * 1024);
    f32x16 acc[2] = {};
    bf16x8 wa0, wa1, wb0, wb1;
#define DN_PG(s) ((s) < 20 ? (s) * 2048 : ((s) - 20) * 2048)
#define DN_RD(A, B, s) do { A = ldtr(t0 + DN_PG(s)); B = ldtr(t1 + DN_PG(s)); } while (0)
#define DN_MM(A, B, ks) do { if ((ks) + DEP < 28) ld16_sc1(af[(ks) + DEP < 28 ? (ks) + DEP : 0], lv, rws, asrc + rot28((ks) + DEP, r5) * 1024); \
      WAITV1((ks) + DEP < 28 ? DEP : 27 - (ks), af[ks]); \
      acc[0] = MFMA32(A, BF8(af[ks]), acc[0]); PIN(acc[0]); acc[1] = MFMA32(B, BF8(af[ks]), acc[1]); PIN(acc[1]); SBAR(); } while (0)
#pragma unroll
    for (int ks = 0; ks < 7; ++ks) DN_MM(wp[ks][0], wp[ks][1], ks);
    DN_RD(wa0, wa1, 8); DN_MM(wp[7][0], wp[7][1], 7);
#pragma unroll
    for (int ks = 8; ks < 28; ks += 2) {
      DN_RD(wb0, wb1, ks + 1); DN_MM(wa0, wa1, ks);
      if (ks == 18) LBAR();
      if (ks + 2 < 28) DN_RD(wa0, wa1, ks + 2);
      DN_MM(wb0, wb1, ks + 1);
    }
    STAMP(18);
    const u32 sb = (u32)ksl * 40960 + 16384 + mh * 6144 + lv;
#pragma unroll
    for (int j = 1; j < 4; ++j) {
      const int o = (j & 1) * 8;
      sts16f(sb + (j - 1) * 2048, (f32x4){acc[j >> 1][o], acc[j >> 1][o + 1], acc[j >> 1][o + 2], acc[j >> 1][o + 3]});
      sts16f(sb + (j - 1) * 2048 + 1024, (f32x4){acc[j >> 1][o + 4], acc[j >> 1][o + 5], acc[j >> 1][o + 6], acc[j >> 1][o + 7]});
    }
    LBAR();
    float y[8];
#pragma unroll
    for (int e = 0; e < 8; ++e) y[e] = acc[0][e];
#pragma unroll
    for (int j = 1; j < 4; ++j) {
      const u32 a = (u32)((ksl - j) & 3) * 40960 + 16384 + mh * 6144 + (j - 1) * 2048 + lv;
      const f32x4 c = lds16f(a), d = lds16f(a + 1024);
#pragma unroll
      for (int e = 0; e < 4; ++e) { y[e] += c[e]; y[4 + e] += d[e]; }
    }
    const u32x4 ov = resid_epi(y, x1v, gbv);
    __builtin_amdgcn_raw_buffer_store_b128(ov, rout, xro, 0, 0);
    STAMP(19);
    STAMP_END();
  }
}

extern "C" void kernel_launch(void* const* d_in, const int* in_sizes, int n_in, void* d_out, int out_size, void* d_ws, size_t ws_size,
                              hipStream_t stream) {
  fused_block<<<256, 512, 0, stream>>>(d_in[0], d_in[1], d_in[2], d_in[4], d_in[5], d_in[6], d_in[7], d_in[8], d_out, (char*)d_ws + (ws_size / 2 & ~(size_t)0xfffff));
}
```

```cpp
#include <hip/hip_runtime.h>
#include <stdint.h>

typedef unsigned u32;
typedef unsigned long long u64;
typedef __attribute__((ext_vector_type(2))) u32 u32x2;
typedef __attribute__((ext_vector_type(4))) u32 u32x4;
typedef __attribute__((ext_vector_type(2))) float f32x2;
typedef __attribute__((ext_vector_type(4))) float f32x4;
typedef __attribute__((ext_vector_type(16))) float f32x16;
typedef __attribute__((ext_vector_type(8))) short bf16x8;
typedef __attribute__((ext_vector_type(2))) __bf16 bf16x2;
typedef __amdgpu_buffer_rsrc_t rsrc_t;
#define LAS __attribute__((address_space(3)))
#define GAS __attribute__((address_space(1)))
#define DI __device__ __forceinline__
#define RLX_AGENT __ATOMIC_RELAXED, __HIP_MEMORY_SCOPE_AGENT

constexpr u32 WS_SEED = 0;
constexpr u32 WS_ARR = 4096;
constexpr u32 WS_MODL = 65536;
constexpr u32 WS_MODC = 524288;
constexpr u32 WS_MB = 262144;
constexpr u32 MB_QKV = 0, MB_O = 32, MB_X1 = 64, MB_E = 96, MB_W = 128, MB_ACT = 160;
constexpr u32 WS_ST = 786432;
constexpr u32 WS_Q = 1u << 20, WS_K = 3u << 20, WS_V = 5u << 20;
constexpr u32 WS_O = 7u << 20;
constexpr u32 WS_X1 = 9u << 20;
constexpr u32 WS_ACT = 11u << 20;
constexpr u32 WS_WP = 18u << 20;
constexpr u32 WPN = 34 * 7168;
constexpr u32 WS_BYTES = 22u << 20;
constexpr u32 WS_DBG = 24u << 20;
#define DEP 10
#define PRE_ARGS rws, WS_WP + (u32)nt * WPN + (u32)((bi & 7) * 30 + w) * 1024
constexpr u32 SPIN_CAP = 1u << 15;
#define START_DELAY 8

DI float bflo(u32 w) { return __uint_as_float(w << 16); }
DI float bfhi(u32 w) { return __uint_as_float(w & 0xffff0000u); }
DI u32 pk2(float a, float b) { f32x2 v = {a, b}; return __builtin_bit_cast(u32, __builtin_convertvector(v, bf16x2)); }
DI float rbf(float a) { return bflo(pk2(a, a)); }
template <int CTRL> DI float dppf(float x) { return __builtin_bit_cast(float, __builtin_amdgcn_update_dpp(0, __builtin_bit_cast(int, x), CTRL, 0xf, 0xf, true)); }
DI void swap32f(float& a, float& b) { auto r = __builtin_amdgcn_permlane32_swap(__float_as_uint(a), __float_as_uint(b), false, false); a = __uint_as_float(r[0]); b = __uint_as_float(r[1]); }
DI void swap16f(float& a, float& b) { auto r = __builtin_amdgcn_permlane16_swap(__float_as_uint(a), __float_as_uint(b), false, false); a = __uint_as_float(r[0]); b = __uint_as_float(r[1]); }
DI void swap32u(u32& a, u32& b) { auto r = __builtin_amdgcn_permlane32_swap(a, b, false, false); a = r[0]; b = r[1]; }
DI void swap16u(u32& a, u32& b) { auto r = __builtin_amdgcn_permlane16_swap(a, b, false, false); a = r[0]; b = r[1]; }
DI float row16_sum(float x) { x += dppf<0xB1>(x); x += dppf<0x4E>(x); x += dppf<0x141>(x); x += dppf<0x140>(x); return x; }
DI float rdlane(float x, int l) { return __uint_as_float(__builtin_amdgcn_readlane(__float_as_uint(x), l)); }

#define MFMA16(a, b, c) __builtin_amdgcn_mfma_f32_16x16x32_bf16((a), (b), (c), 0, 0, 0)
#define MFMA32(a, b, c) __builtin_amdgcn_mfma_f32_32x32x16_bf16((a), (b), (c), 0, 0, 0)
#define BF8(x) __builtin_bit_cast(bf16x8, (x))
#define WAITV(n) asm volatile("s_waitcnt vmcnt(%0)" ::"i"(n) : "memory")
#define WAITBAR(n) asm volatile("s_waitcnt vmcnt(%0) lgkmcnt(0)\n\ts_barrier" ::"i"(n) : "memory")
#define LBAR() asm volatile("s_waitcnt lgkmcnt(0)\n\ts_barrier" ::: "memory")
#define SBAR() __builtin_amdgcn_sched_barrier(0)
#define NOP_ do {} while (0)

DI void dma(u32 lds, u32 voff, rsrc_t rs, u32 soff) {
  asm volatile("s_mov_b32 m0, %0\n\ts_nop 0\n\tbuffer_load_dwordx4 %1, %2, %3 offen lds" ::"s"(lds), "v"(voff), "s"(rs), "s"(soff) : "memory");
}
DI void dma_sc1(u32 lds, u32 voff, rsrc_t rs, u32 soff) {
  asm volatile("s_mov_b32 m0, %0\n\ts_nop 0\n\tbuffer_load_dwordx4 %1, %2, %3 offen sc1 lds" ::"s"(lds), "v"(voff), "s"(rs), "s"(soff) : "memory");
}
DI void dma4(u32 lds, u32 voff, rsrc_t rs, u32 soff) {
  asm volatile("s_mov_b32 m0, %0\n\ts_nop 0\n\tbuffer_load_dword %1, %2, %3 offen lds" ::"s"(lds), "v"(voff), "s"(rs), "s"(soff) : "memory");
}
DI void ld16(u32x4& r, u32 voff, rsrc_t rs, u32 soff) { asm volatile("buffer_load_dwordx4 %0, %1, %2, %3 offen" : "=v"(r) : "v"(voff), "s"(rs), "s"(soff) : "memory"); }
DI void ld16_sc1(u32x4& r, u32 voff, rsrc_t rs, u32 soff) { asm volatile("buffer_load_dwordx4 %0, %1, %2, %3 offen sc1" : "=v"(r) : "v"(voff), "s"(rs), "s"(soff) : "memory"); }
DI void ld8_sc1(u32x2& r, u32 voff, rsrc_t rs, u32 soff) { asm volatile("buffer_load_dwordx2 %0, %1, %2, %3 offen sc1" : "=v"(r) : "v"(voff), "s"(rs), "s"(soff) : "memory"); }
DI void ld4(u32& r, u32 voff, rsrc_t rs, u32 soff) { asm volatile("buffer_load_dword %0, %1, %2, %3 offen" : "=v"(r) : "v"(voff), "s"(rs), "s"(soff) : "memory"); }
DI void ld2(u32& r, u32 voff, rsrc_t rs, u32 soff) { asm volatile("buffer_load_ushort %0, %1, %2, %3 offen" : "=v"(r) : "v"(voff), "s"(rs), "s"(soff) : "memory"); }
#define WAITV1(n, a) asm volatile("s_waitcnt vmcnt(%1)" : "+v"(a) : "i"(n) : "memory")
#define PIN(a) asm volatile("" : "+v"(a))
DI void st16_sc1(u32x4 v, rsrc_t rs, u32 voff) { __builtin_amdgcn_raw_buffer_store_b128(v, rs, voff, 0, 16); }

DI bf16x8 lds16(u32 a) { return *(const LAS bf16x8*)(uintptr_t)a; }
DI u32x4 lds16u(u32 a) { return *(const LAS u32x4*)(uintptr_t)a; }
DI f32x4 lds16f(u32 a) { return *(const LAS f32x4*)(uintptr_t)a; }
DI f32x2 lds8f(u32 a) { return *(const LAS f32x2*)(uintptr_t)a; }
DI float lds4f(u32 a) { return *(const LAS float*)(uintptr_t)a; }
DI void sts16(u32 a, u32x4 v) { *(LAS u32x4*)(uintptr_t)a = v; }
DI void sts16f(u32 a, f32x4 v) { *(LAS f32x4*)(uintptr_t)a = v; }
DI void sts8f(u32 a, f32x2 v) { *(LAS f32x2*)(uintptr_t)a = v; }
DI void sts4f(u32 a, float v) { *(LAS float*)(uintptr_t)a = v; }
typedef short v4i16_t __attribute__((ext_vector_type(4)));
DI bf16x8 ldtr(u32 a) {
  v4i16_t lo = __builtin_amdgcn_ds_read_tr16_b64_v4i16((LAS v4i16_t*)(uintptr_t)a);
  v4i16_t hi = __builtin_amdgcn_ds_read_tr16_b64_v4i16((LAS v4i16_t*)(uintptr_t)(a + 512));
  return __builtin_shufflevector(lo, hi, 0, 1, 2, 3, 4, 5, 6, 7);
}

DI void poll(u32 voff, rsrc_t rws, u32 tag) {
#pragma unroll 1
  for (u32 spins = 0; spins < SPIN_CAP; ++spins) {
    u32 v;
    asm volatile("buffer_load_dword %0, %1, %2, 0 offen sc1\n\ts_waitcnt vmcnt(0)" : "=v"(v) : "v"(voff), "s"(rws) : "memory");
    if (__all(v == tag)) break;
    __builtin_amdgcn_s_sleep(1);
  }
}
DI void post(u32 voff, rsrc_t rws, u32 tag) { __builtin_amdgcn_raw_buffer_store_b32(tag, rws, voff, 0, 16); }
constexpr u32 NOWHERE = 0x7ffffff0u;
DI u32 mbox(int xcd, int bi, u32 word) { return WS_MB + (u32)(bi * 8 + xcd) * 1024 + word * 4; }
DI bool tags_ok(u32x4 a, u32x4 b, u32 tag) { return a[1] == tag && a[3] == tag && b[1] == tag && b[3] == tag; }
DI float gsum(u32x4 a, u32x4 b, u32x4 c, u32x4 d, int i) { return (__uint_as_float(a[i]) + __uint_as_float(b[i])) + (__uint_as_float(c[i]) + __uint_as_float(d[i])); }
DI void st_gran(char* ws, u32 byte, float v, u32 tag) { __hip_atomic_store((GAS u64*)(ws + byte), ((u64)tag << 32) | __float_as_uint(v), RLX_AGENT); }
DI void ld4_sc1(u32& r, u32 voff, rsrc_t rs, u32 soff) { asm volatile("buffer_load_dword %0, %1, %2, %3 offen sc1" : "=v"(r) : "v"(voff), "s"(rs), "s"(soff) : "memory"); }
#define LD4X(a, b, c, d, voff, base, step) do { ld16_sc1(a, voff, rws, base); ld16_sc1(b, voff, rws, (base) + (step)); ld16_sc1(c, voff, rws, (base) + 2 * (step)); ld16_sc1(d, voff, rws, (base) + 3 * (step)); } while (0)
#define WAIT4X(n, a, b, c, d) asm volatile("s_waitcnt vmcnt(%4)" : "+v"(a), "+v"(b), "+v"(c), "+v"(d) : "i"(n) : "memory")

#define NOW(t_) asm volatile("s_memrealtime %0\n\ts_waitcnt lgkmcnt(0)" : "=s"(t_)::"memory")
#define STAMP(i) do { if ((i) == 0) NOW(dbg[0]); if ((i) == KEXT) NOW(dbg[1]); } while (0)
#define STAMP_END() do { u64 te_, tn_; NOW(te_); for (int i_ = 0; i_ < 100000; ++i_) { NOW(tn_); if (tn_ - te_ >= dbg[1] - dbg[0]) break; __builtin_amdgcn_s_sleep(1); } } while (0)
#define DBGARG , u64 (&dbg)[2]
#define DBGPASS , dbg
#define KEXT 2

DI float silu_bf(float c) { return rbf(c * __builtin_amdgcn_rcpf(1.f + __builtin_amdgcn_exp2f(c * -1.4426950408889634f))); }
DI float sum8(f32x4 a, f32x4 d) { return ((a[0] + a[1]) + (a[2] + a[3])) + ((d[0] + d[1]) + (d[2] + d[3])); }

constexpr u64 upper_mask(int e) {
  u64 m = 0;
  for (int l = 0; l < 64; ++l) if ((e & 3) + 8 * (e >> 2) + 4 * (l >> 5) > (l & 31)) m |= 1ull << l;
  return m;
}
template <int E> DI bool upper() { constexpr u64 m = upper_mask(E); return __builtin_amdgcn_inverse_ballot_w64(m); }
#define MX3(a, b, c) __builtin_fmaxf(__builtin_fmaxf((a), (b)), (c))
#define EX2(x) __builtin_amdgcn_exp2f(x)
template <int I> DI void sel_fill(f32x16& sel, const f32x16& sf, const f32x16& sd) {
  if constexpr (I < 16) { sel[I] = upper<I>() ? sf[I] : sd[I]; sel_fill<I + 1>(sel, sf, sd); }
}
#define WP_PRE(i) dma(40960 + w * 1024, lane * 16, rws, psrc + (i) * 8192)
#define PV(V, P) do { o = MFMA32(V, BF8(P), o); PIN(o); } while (0)
DI float half_max(const f32x16& s) {
  float m0 = __builtin_fmaxf(s[0], s[1]), m1 = __builtin_fmaxf(s[2], s[3]);
#pragma unroll
  for (int e = 4; e < 16; e += 4) { m0 = MX3(m0, s[e], s[e + 1]); m1 = MX3(m1, s[e + 2], s[e + 3]); }
  return __builtin_fmaxf(m0, m1);
}
DI float both_max(float m) { float mo = m; swap32f(m, mo); return __builtin_fmaxf(m, mo); }
DI void boundary_p(const f32x16& sel, float m, const u32 (&mk)[8], u32x4 (&pf)[2], u32x4 (&pd)[2]) {
#pragma unroll
  for (int e = 0; e < 8; ++e) {
    const u32 p = pk2(EX2(sel[2 * e] - m), EX2(sel[2 * e + 1] - m));
    pf[e >> 2][e & 3] = p & mk[e];
    pd[e >> 2][e & 3] = p & ~mk[e];
  }
}
DI f32x16 attn_exact(int ilo, bf16x8 qf, const u32 (&mk)[8], u32 kb, u32 vb0, u32 vb1) {
  const f32x16 zero = {};
  f32x16 sel, sf, sd = MFMA32(lds16(kb + 8192), qf, zero);
  if (ilo == 0) sf = MFMA32(lds16(kb), qf, zero);
  else {
#pragma unroll
    for (int e = 0; e < 16; ++e) sf[e] = -INFINITY;
  }
  sel_fill<0>(sel, sf, sd);
  float m = half_max(sel);
#pragma unroll 1
  for (int i = ilo > 1 ? ilo : 1; i < 8; ++i) m = __builtin_fmaxf(m, half_max(MFMA32(lds16(kb + i * 1024), qf, zero)));
  m = both_max(m);
  f32x16 negm;
#pragma unroll
  for (int e = 0; e < 16; ++e) negm[e] = -m;
  u32x4 pf[2], pd[2];
  boundary_p(sel, m, mk, pf, pd);
  f32x16 o = MFMA32(lds16(vb0 + 8 * 1040), BF8(pd[0]), zero);
  o = MFMA32(lds16(vb1 + 8 * 1040), BF8(pd[1]), o);
  if (ilo == 0) { o = MFMA32(lds16(vb0), BF8(pf[0]), o); o = MFMA32(lds16(vb1), BF8(pf[1]), o); }
#pragma unroll 1
  for (int i = ilo > 1 ? ilo : 1; i < 8; ++i) {
    const f32x16 sc = MFMA32(lds16(kb + i * 1024), qf, negm);
    u32x4 p0, p1;
#pragma unroll
    for (int e = 0; e < 4; ++e) { p0[e] = pk2(EX2(sc[2 * e]), EX2(sc[2 * e + 1])); p1[e] = pk2(EX2(sc[8 + 2 * e]), EX2(sc[8 + 2 * e + 1])); }
    o = MFMA32(lds16(vb0 + i * 1040), BF8(p0), o);
    o = MFMA32(lds16(vb1 + i * 1040), BF8(p1), o);
  }
  return o;
}
DI f32x16 attn_fast(int w, int lane, bf16x8 qf, const u32 (&mk)[8], const u32 (&ms)[16], u32 kb, u32 vb0, u32 vb1, rsrc_t rws, u32 psrc,
                    u32 pv, rsrc_t rwo, u32 r3) {
#define WO_DMA(i) dma(98304 + ((((i) ^ (r3 >> 8)) * 8 + w) * 1024), pv, rwo, (u32)((((i) ^ (r3 >> 8)) * 8 + w) ^ ((r3 & 7) << 1)) * 8192)
#define TWO(a, b) do { a; b; } while (0)
  const f32x16 zero = {};
  bf16x8 k8 = lds16(kb + 8192), k0 = lds16(kb), ka = lds16(kb + 1024), kc = lds16(kb + 2048);
  bf16x8 va = lds16(vb0 + 8 * 1040), vc = lds16(vb1 + 8 * 1040), vd = lds16(vb0), ve = lds16(vb1);
  SBAR();
  f32x16 sa = MFMA32(k8, qf, zero);
  PIN(sa); WO_DMA(0); SBAR();
  const float m = both_max(half_max(sa));
  f32x16 negm;
#pragma unroll
  for (int e = 0; e < 16; ++e) negm[e] = -m;
  PIN(negm); SBAR();
  f32x16 sd = MFMA32(k8, qf, negm), sb = MFMA32(k0, qf, negm);
  sa = MFMA32(ka, qf, negm);
  PIN(sd); PIN(sb); PIN(sa); WO_DMA(1); SBAR();
  f32x16 o = zero;
  u32x4 pd[2], pf[2], p0, p1;
#pragma unroll
  for (int e = 0; e < 8; ++e) {
    const float x0 = __uint_as_float((__float_as_uint(sb[2 * e]) & ms[2 * e]) | (__float_as_uint(sd[2 * e]) & ~ms[2 * e]));
    const float x1 = __uint_as_float((__float_as_uint(sb[2 * e + 1]) & ms[2 * e + 1]) | (__float_as_uint(sd[2 * e + 1]) & ~ms[2 * e + 1]));
    const u32 p = pk2(EX2(x0), EX2(x1));
    pf[e >> 2][e & 3] = p & mk[e];
    pd[e >> 2][e & 3] = p & ~mk[e];
  }
  PIN(pf[0]); PIN(pf[1]); PIN(pd[0]); PIN(pd[1]); WO_DMA(2); SBAR();
#define AT_Q(P, S, o_) do { P[(o_) & 3] = pk2(EX2(S[2 * (o_)]), EX2(S[2 * (o_) + 1])); P[((o_) & 3) + 1] = pk2(EX2(S[2 * (o_) + 2]), EX2(S[2 * (o_) + 3])); PIN(P); SBAR(); } while (0)
#define AT_STEP(S, N, KN, KR, i, M0, M1, M2, M3) do { \
    if ((i) < 7) { N = MFMA32(KN, qf, negm); PIN(N); if ((i) < 6) KR = lds16(kb + ((i) + 2) * 1024); SBAR(); } \
    u32x4 q0_, q1_; \
    AT_Q(q0_, S, 0); M0; SBAR(); AT_Q(q0_, S, 2); M1; SBAR(); AT_Q(q1_, S, 4); M2; SBAR(); AT_Q(q1_, S, 6); M3; \
    va = lds16(vb0 + (i) * 1040); vc = lds16(vb1 + (i) * 1040); p0 = q0_; p1 = q1_; SBAR(); } while (0)
  AT_STEP(sa, sb, kc, ka, 1, PV(va, pd[0]), PV(vc, pd[1]), PV(vd, pf[0]), PV(ve, pf[1]));
  AT_STEP(sb, sa, ka, kc, 2, WP_PRE(0), PV(va, p0), PV(vc, p1), WO_DMA(3));
  AT_STEP(sa, sb, kc, ka, 3, WP_PRE(1), PV(va, p0), PV(vc, p1), WO_DMA(4));
  AT_STEP(sb, sa, ka, kc, 4, WP_PRE(2), PV(va, p0), PV(vc, p1), WO_DMA(5));
  AT_STEP(sa, sb, kc, ka, 5, WP_PRE(3), PV(va, p0), PV(vc, p1), WO_DMA(6));
  AT_STEP(sb, sa, ka, kc, 6, WO_DMA(7), PV(va, p0), PV(vc, p1), NOP_);
  AT_STEP(sa, sb, kc, ka, 7, NOP_, PV(va, p0), PV(vc, p1), NOP_);
  PV(va, p0); PV(vc, p1);
  return o;
}
DI u32x4 attn_wave(bool full, int w, int lane, bf16x8 qf, const u32 (&mk)[8], const u32 (&ms)[16], u32 kb, u32 vb0, u32 vb1, rsrc_t rws, u32 psrc,
                   u32 pv, rsrc_t rwo, u32 r3) {
  f32x16 o = attn_fast(w, lane, qf, mk, ms, kb, vb0, vb1, rws, psrc, pv, rwo, r3);
  if (!__all(o[8] < 1.8446744e19f && o[8] > 5.4210109e-20f))
    o = attn_exact(full ? 0 : 8 - w, qf, mk, kb, vb0, vb1);
  const float rl = __builtin_amdgcn_rcpf(o[8]);
  u32 a0 = pk2(o[0] * rl, o[1] * rl), a1 = pk2(o[2] * rl, o[3] * rl), b0 = pk2(o[4] * rl, o[5] * rl), b1 = pk2(o[6] * rl, o[7] * rl);
  swap32u(a0, b0); swap32u(a1, b1);
  return (u32x4){a0, a1, b0, b1};
}

DI u32x4 resid_epi(float (&y)[8], u32x4 res, u32x4 gate) {
#pragma unroll
  for (int i = 0; i < 4; ++i) swap32f(y[i], y[4 + i]);
  u32x4 o;
#pragma unroll
  for (int i = 0; i < 4; ++i) {
    const u32 yb = pk2(y[2 * i], y[2 * i + 1]);
    const u32 tb = pk2(bflo(yb) * bflo(gate[i]), bfhi(yb) * bfhi(gate[i]));
    o[i] = pk2(bflo(res[i]) + bflo(tb), bfhi(res[i]) + bfhi(tb));
  }
  return o;
}

DI u32 rot28(u32 s, u32 r) { const u32 t = s + r; return t >= 28 ? t - 28 : t; }
#define WD_DMA(base) dma((u32)((base) + w) * 1024, pv, rwd, (u32)(((base) / 40) * 448 + (w & 1) * 8 + rot28((((base) % 40) + w) >> 1, r5) * 16) * 1024)

template <int NF>
DI void ffn_up(int w, int lane, u32 tag, char* ws, rsrc_t rws, rsrc_t rwd, u32 pv, int m128, int nt, u32 r5, u32 pollx, u32 wl, u32 wh, u32 xl, u32 stv, u32 adst, int b DBGARG) {
  constexpr int NW = NF;
  constexpr int F0 = NF == 4 ? 0 : 4;
  const int p = w & 3, tile = 4 * m128 + p;
  const u32 lv = lane * 16;
  const u32 wsrc = WS_WP + (u32)nt * WPN + w * 1024;
  const u32 xsrc = WS_X1 + (u32)(tile * 32 + 2 * (w >> 2)) * 1024;
  const u32 xdst = 114688 + (p * 4 + 2 * (w >> 2)) * 1024;
  const u32 r4 = (2 * nt + m128) & 7;
#define UP_W1(g, i) dma_sc1(((g) & 3) * 28672 + w * 1024 + (i) * 8192, lv, rws, wsrc + ((g) ^ r4) * 28672 + (i) * 8192)
#define UP_X1(g, i) dma_sc1(xdst + ((g) % 3) * 16384 + (i) * 1024, lv, rws, xsrc + ((g) ^ r4) * 4096 + (i) * 1024)
#pragma unroll
  for (int g = 1; g < 4; ++g)
#pragma unroll
    for (int i = 0; i < NW; ++i) UP_W1(g, i);
  STAMP(12);
  poll(pollx, rws, tag);
  STAMP(13);
#pragma unroll
  for (int g = 0; g < 3; ++g) { UP_X1(g, 0); UP_X1(g, 1); }
  f32x16 acc[NF];
#pragma unroll
  for (int f = 0; f < NF; ++f) acc[f] = (f32x16){};
  bf16x8 xa, wa[NF], xb, wb[NF];
  u32x4 s0, s1_, s2_, s3;
#define UP_RX(X, ks) X = lds16(xl + (((ks) >> 2) % 3) * 16384 + ((ks) & 3) * 1024)
#define UP_RW(W, f, ks) W[f] = lds16(((((ks) >> 2) & 2) ? wh : wl) + (((ks) >> 2) & 1) * 28672 + (((ks) & 3) * 7 + (f)) * 1024)
#define UP_STEP(XC, WC, XN, WN, ks, RD, MM, D0, D1) do { \
    if (RD) { UP_RX(XN, ks); UP_RW(WN, 0, ks); } SBAR(); if (MM) { acc[0] = MFMA32(WC[0], XC, acc[0]); PIN(acc[0]); } SBAR(); \
    if (RD) { UP_RW(WN, 1, ks); UP_RW(WN, 2, ks); } SBAR(); if (MM) { acc[1] = MFMA32(WC[1], XC, acc[1]); PIN(acc[1]); } SBAR(); \
    if (RD && NF == 4) UP_RW(WN, NF - 1, ks); D0; SBAR(); if (MM) { acc[2] = MFMA32(WC[2], XC, acc[2]); PIN(acc[2]); } SBAR(); \
    D1; if (MM && NF == 4) { acc[NF - 1] = MFMA32(WC[NF - 1], XC, acc[NF - 1]); PIN(acc[NF - 1]); } SBAR(); } while (0)
#define UP_WL(g) do { if (NF == 4) UP_W1(g, 3); } while (0)
#define UP_GROUP(g, MM0, A0, A1, B0, B1, C0, C1, E0, E1) do { \
    UP_STEP(xb, wb, xa, wa, 4 * (g), true, MM0, A0, A1); UP_STEP(xa, wa, xb, wb, 4 * (g) + 1, true, true, B0, B1); \
    UP_STEP(xb, wb, xa, wa, 4 * (g) + 2, true, true, C0, C1); UP_STEP(xa, wa, xb, wb, 4 * (g) + 3, true, true, E0, E1); } while (0)
#define UP_GREFILL(g, S0, S1) UP_GROUP(g, true, UP_X1((g) + 2, 0), UP_X1((g) + 2, 1), UP_W1((g) + 3, 0), S0, UP_W1((g) + 3, 1), UP_W1((g) + 3, 2), UP_WL((g) + 3), S1)
#define UP_E(i) dma_sc1(((i) * 8 + w) * 1024, lv, rws, WS_WP + (u32)nt * WPN + 32 * 7168 + ((i) * 8 + w) * 1024)
  WAITBAR(4); STAMP(24); UP_GROUP(0, false, NOP_, NOP_, NOP_, NOP_, NOP_, NOP_, NOP_, NOP_);
  WAITBAR(2); STAMP(25); UP_GREFILL(1, NOP_, NOP_);
  WAITBAR(NW + 2); STAMP(26); UP_GREFILL(2, NOP_, NOP_);
  WAITBAR(2 * NW + 2); STAMP(27); UP_GREFILL(3, NOP_, LD4X(s0, s1_, s2_, s3, stv, WS_ST + (u32)tile * 4096, 512));
  WAITBAR(2 * NW + 6); STAMP(28); UP_GREFILL(4, NOP_, NOP_);
  WAITBAR(2 * NW + 6); STAMP(29);
  UP_GROUP(5, true, UP_X1(7, 0), UP_X1(7, 1), UP_E(0), NOP_, UP_E(1), NOP_, WD_DMA(16), NOP_);
  asm volatile("s_waitcnt vmcnt(%4) lgkmcnt(0)\n\ts_barrier" : "+v"(s0), "+v"(s1_), "+v"(s2_), "+v"(s3) : "i"(NW + 5) : "memory");
  STAMP(30);
#pragma unroll 1
  for (u32 spins = 0; spins < SPIN_CAP; ++spins) {
    if (__all(tags_ok(s0, s1_, tag) && tags_ok(s2_, s3, tag))) break;
    LD4X(s0, s1_, s2_, s3, stv, WS_ST + (u32)tile * 4096, 512);
    WAIT4X(0, s0, s1_, s2_, s3);
  }
  float s1 = gsum(s0, s1_, s2_, s3, 0), s2 = gsum(s0, s1_, s2_, s3, 2);
  { float t1 = s1, t2 = s2; swap32f(s1, t1); swap32f(s2, t2); s1 += t1; s2 += t2; }
  const float mu = s1 * (1.f / 512.f);
  const float var = __builtin_fmaxf(s2 * (1.f / 512.f) - mu * mu, 0.f) + 1e-5f;
  float rstd = __builtin_amdgcn_rsqf(var);
  const float sig = var * rstd;
  const float mh = rbf(-mu), ml = -mu - mh, sh = rbf(sig), sl = sig - sh;
  u32x4 ev = {pk2(mh, mh), pk2(ml, sh), pk2(sh, sl), 0u};
  PIN(ev); PIN(rstd); SBAR();
  UP_GROUP(6, true, WD_DMA(24), WD_DMA(32), WD_DMA(40), NOP_, WD_DMA(48), NOP_, WD_DMA(144), NOP_);
  WAITBAR(6); STAMP(31);
  UP_GROUP(7, true, WD_DMA(56), WD_DMA(64), WD_DMA(72), NOP_, WD_DMA(112), NOP_, WD_DMA(120), NOP_);
#pragma unroll
  for (int f = 0; f < NF; ++f) wa[f] = lds16(wl + f * 1024);
  UP_STEP(xb, wb, xa, wa, 0, false, true, NOP_, NOP_);
#pragma unroll
  for (int f = 0; f < NF; ++f) wb[f] = lds16(wl + 7168 + f * 1024);
#pragma unroll
  for (int f = 0; f < NF; ++f) { acc[f] = MFMA32(wa[f], BF8(ev), acc[f]); PIN(acc[f]); }
  LBAR();
  STAMP(14);
#pragma unroll
  for (int f = 0; f < NF; ++f) acc[f] = MFMA32(wb[f], BF8(ev), acc[f]);
  WD_DMA(0); WD_DMA(8); WD_DMA(80); WD_DMA(88);
  SBAR();
  constexpr float C1 = -2.f * 0.7978845608028654f * 1.4426950408889634f, C2 = C1 * 0.044715f;
  const float k1 = C1 * rstd, k3 = C2 * rstd * rstd * rstd;
#pragma unroll
  for (int f = 0; f < NF; ++f) {
    float a[8];
#pragma unroll
    for (int e = 0; e < 8; ++e) {
      const float g = acc[f][e];
      const float z = g * __builtin_fmaf(g * g, k3, k1);
      a[e] = (g * acc[f][8 + e]) * __builtin_amdgcn_rcpf(__builtin_fmaf(__builtin_amdgcn_exp2f(z), var, var));
    }
    u32 a0 = pk2(a[0], a[1]), a1 = pk2(a[2], a[3]), b0 = pk2(a[4], a[5]), b1 = pk2(a[6], a[7]);
    swap32u(a0, b0); swap32u(a1, b1);
    st16_sc1((u32x4){a0, a1, b0, b1}, rws, adst + f * 1024);
    if (f == 0) { WD_DMA(128); WD_DMA(96); }
    if (f == 1) { WD_DMA(104); WD_DMA(136); }
    if (f == 2) WD_DMA(152);
    SBAR();
  }

}

__global__ void __launch_bounds__(512, 2)
fused_block(const void* __restrict__ px, const void* __restrict__ pwqkv, const void* __restrict__ pwo, const void* __restrict__ pwg,
            const void* __restrict__ pwu, const void* __restrict__ pwd, const void* __restrict__ pc, const void* __restrict__ pwm,
            void* pout, char* ws) {
  __shared__ __attribute__((aligned(16))) char smem[163840];
  asm volatile("" ::"v"((u32)(uintptr_t)(LAS char*)smem));
  const int tid = threadIdx.x, lane = tid & 63, w = __builtin_amdgcn_readfirstlane(tid >> 6);
  const int b = blockIdx.x, xcd = b & 7, bi = b >> 3;
  const rsrc_t rx = __builtin_amdgcn_make_buffer_rsrc((void*)px, 0, 2048 * 512 * 2, 0x00020000);
  const rsrc_t rqkv = __builtin_amdgcn_make_buffer_rsrc((void*)pwqkv, 0, 512 * 1536 * 2, 0x00020000);
  const rsrc_t rwo = __builtin_amdgcn_make_buffer_rsrc((void*)pwo, 0, 512 * 512 * 2, 0x00020000);
  const rsrc_t rwd = __builtin_amdgcn_make_buffer_rsrc((void*)pwd, 0, 1792 * 512 * 2, 0x00020000);
  const rsrc_t rout = __builtin_amdgcn_make_buffer_rsrc(pout, 0, 2048 * 512 * 2, 0x00020000);
  const rsrc_t rws = __builtin_amdgcn_make_buffer_rsrc(ws, 0, WS_BYTES, 0x00020000);
  const u32 lv = lane * 16;
  u64 dbg[2];
  STAMP(0);

  const int m64a = 8 * (xcd & 3) + (bi & 7), grp = 4 * (xcd >> 2) + (bi >> 3);
  constexpr u32 SCR = 155648;
  const rsrc_t rc = __builtin_amdgcn_make_buffer_rsrc((void*)pc, 0, 512 * 2, 0x00020000);
  const rsrc_t rwm = __builtin_amdgcn_make_buffer_rsrc((void*)pwm, 0, 512 * 3072 * 2, 0x00020000);
  if (w == 0) {
    __builtin_amdgcn_s_setprio(2);
    u32 cv[4], seed; u32x4 m[4];
    asm volatile("s_load_dword %0, %1, 0x0" : "=s"(seed) : "s"(ws) : "memory");
    const u32 r0 = (u32)(bi >> 3) * 128 + (lane >> 1);
#pragma unroll
    for (int i = 0; i < 4; ++i) ld16(m[i], r0 * 6144 + (lane & 1) * 16, rwm, (u32)(xcd * 8 + (bi & 7)) * 32 + i * (32 * 6144));
#pragma unroll
    for (int i = 0; i < 4; ++i) ld2(cv[i], r0 * 2, rc, i * 64);
    asm volatile("s_waitcnt vmcnt(0) lgkmcnt(0)" : "+v"(cv[0]), "+v"(cv[1]), "+v"(cv[2]), "+v"(cv[3]), "+v"(m[0]), "+v"(m[1]), "+v"(m[2]), "+v"(m[3]), "+s"(seed)::"memory");
    float v[8] = {};
#pragma unroll
    for (int i = 0; i < 4; ++i) {
      const float sc = silu_bf(bflo(cv[i]));
#pragma unroll
      for (int e = 0; e < 4; ++e) { v[2 * e] = __builtin_fmaf(sc, bflo(m[i][e]), v[2 * e]); v[2 * e + 1] = __builtin_fmaf(sc, bfhi(m[i][e]), v[2 * e + 1]); }
    }
#pragma unroll
    for (int j = 0; j < 4; ++j) { swap32f(v[j], v[j + 4]); v[j] += v[j + 4]; }
#pragma unroll
    for (int j = 0; j < 2; ++j) { swap16f(v[j], v[j + 2]); v[j] += v[j + 2]; }
    const bool o = lane & 8;
    float t = (o ? v[1] : v[0]) + dppf<0x128>(o ? v[0] : v[1]);
    t += dppf<0x4E>(t);
    t += __builtin_bit_cast(float, __builtin_amdgcn_update_dpp(__builtin_amdgcn_update_dpp(0, __builtin_bit_cast(int, t), 0x104, 0xf, 0x5, false), __builtin_bit_cast(int, t), 0x114, 0xf, 0xa, false));
    const u32 a = WS_MODC + (u32)((lane >> 1) & 3) * 32768 + (u32)(bi >> 3) * 8192 + (u32)((xcd * 8 + (bi & 7)) * 16 + (lane & 1) * 8 + (lane >> 3)) * 8;
    st_gran(ws, a, t, seed + 1u); st_gran(ws, a + 131072, t, seed + 1u);
  }
  STAMP(1);
  u32 seed;
  asm volatile("s_load_dword %0, %1, 0x0" : "=s"(seed) : "s"(ws) : "memory");
  u32x4 xr[8];
#pragma unroll
  for (int j = 0; j < 8; ++j) ld16(xr[j], lv, rx, (u32)(m64a * 64 + w * 8 + j) * 1024);
  u32 cv1; u32x4 ml;
  {
    const u32 r1 = (u32)(bi >> 2) * 64 + w * 8 + (lane >> 3);
    ld2(cv1, r1 * 2, rc, 0);
    ld16(ml, r1 * 6144 + (lane & 7) * 16, rwm, 2048 + (u32)(xcd * 4 + (bi & 3)) * 128);
  }
  const u32 r1 = bi & 7;
  const int n64 = bi >> 2, m64 = 4 * xcd + (bi & 3);
  const int mh = w & 1, ksl = w >> 1, tile3 = 2 * m64 + mh;
  const int head2 = 4 * xcd + (bi & 3), range = bi >> 2;
  const int m128 = 8 * (xcd >> 2) + (bi & 7), nt = 4 * (xcd & 3) + (bi >> 3);
  const int wid = xcd * 28 + bi, wq = wid & 3, wmat = wid >= 112, wg = (wid >> 2) - wmat * 28;
  constexpr u32 SC2 = 53248, SC3 = 94208;
  u32 qv, ab[4], bl, bh, aw[8], pv, pg, gvo, xro, tb[2], mk[8], ms[16], kb, vb0, vb1, wdst, ta, qdst, odst, xdst3, adst;
  u32 postq, posto, postw, postx, posta, pollq, pollo, pollw, pollx, polla, rbw[3], rbr, wl, xl, stv;
  const int wm = w >> 2, hn = w & 3, fr = lane & 15, g = lane >> 4;
  {
    qv = (u32)(w * 8 + (lane >> 3)) * 3072 + (((lane & 7) ^ ((((lane >> 4) & 1) | ((w & 1) << 1)) << 1)) * 16) + grp * 128;
#pragma unroll
    for (int t = 0; t < 4; ++t) ab[t] = (u32)(wm * 32 + fr) * 1024 + ((g ^ (fr & 3)) * 16) + ((t ^ (fr >> 2)) * 64);
    bl = 65536 + (u32)(8 * g + (fr >> 2)) * 128 + ((hn ^ (((fr >> 3) & 1) | ((g & 1) << 1))) * 32) + (lane & 3) * 8; bh = bl + 49152;
#pragma unroll
    for (int j = 0; j < 8; ++j) aw[j] = (u32)(w * 8 + j) * 1024 + ((lane ^ (r1 << 3) ^ ((w * 8 + j) & 15)) * 16);
    pg = (u32)(lane >> 3) * 3584 + (((lane & 7) ^ (((lane >> 4) & 1) << 2)) * 16) + (u32)(wq * 128 + w * 16) * 3584 + wg * 128;
    gvo = (u32)((lane >> 3) & 1) * 65536 + (lane & 7) * 16 + 8 * (lane < 32 ? 512 + (lane >> 4) * 512 + wq * 128 + w * 16 : (lane < 48 ? 0 : 1536) + n64 * 64 + ksl * 16);
    xro = (u32)(tile3 * 32 + (lane & 31)) * 1024 + (u32)(n64 * 64 + ksl * 16 + (lane >> 5) * 8) * 2;
    qdst = (u32)((4 * grp + hn) * 64 + 2 * m64a + wm) * 1024 + lv;
    PIN(qv); PIN(bl); PIN(bh); PIN(pg); PIN(gvo); PIN(xro); PIN(qdst);
#pragma unroll
    for (int t = 0; t < 4; ++t) PIN(ab[t]);
#pragma unroll
    for (int j = 0; j < 8; ++j) PIN(aw[j]);
  }
#define QKV_DMA(s, seg) dma(65536 + ((s) & 3) * 24576 + (seg) * 8192 + w * 1024, qv, rqkv, ((s) ^ r1) * (64 * 3072) + (seg) * 1024)
  asm volatile("s_waitcnt vmcnt(2) lgkmcnt(0)" : "+s"(seed)::"memory");
  const u32 tag = seed + 1u;
  u32x4 a0, a1, a2, a3;
  f32x2 xn[8][4];
  {
    asm volatile("" : "+v"(xr[0]), "+v"(xr[1]), "+v"(xr[2]), "+v"(xr[3]), "+v"(xr[4]), "+v"(xr[5]), "+v"(xr[6]), "+v"(xr[7])::"memory");
    float S[8], Q[8];
#pragma unroll
    for (int j = 0; j < 8; ++j) {
#pragma unroll
      for (int e = 0; e < 4; ++e) xn[j][e] = (f32x2){bflo(xr[j][e]), bfhi(xr[j][e])};
      const f32x2 s2 = (xn[j][0] + xn[j][1]) + (xn[j][2] + xn[j][3]);
      const f32x2 q2 = __builtin_elementwise_fma(xn[j][0], xn[j][0], __builtin_elementwise_fma(xn[j][1], xn[j][1], __builtin_elementwise_fma(xn[j][2], xn[j][2], xn[j][3] * xn[j][3])));
      S[j] = s2[0] + s2[1]; Q[j] = q2[0] + q2[1];
    }
#pragma unroll
    for (int j = 0; j < 4; ++j) { swap32f(S[j], S[j + 4]); S[j] += S[j + 4]; swap32f(Q[j], Q[j + 4]); Q[j] += Q[j + 4]; }
#pragma unroll
    for (int j = 0; j < 2; ++j) { swap16f(S[j], S[j + 2]); S[j] += S[j + 2]; swap16f(Q[j], Q[j + 2]); Q[j] += Q[j + 2]; }
    float ra[2], rb[2];
#pragma unroll
    for (int j = 0; j < 2; ++j) {
      const float mu = row16_sum(S[j]) * (1.f / 512.f);
      const float var = __builtin_fmaxf(row16_sum(Q[j]) * (1.f / 512.f) - mu * mu, 0.f) + 1e-5f;
      ra[j] = __builtin_amdgcn_rsqf(var); rb[j] = -mu * ra[j];
    }
    SBAR();
    LD4X(a0, a1, a2, a3, lv, WS_MODC + (u32)xcd * 32768 + w * 1024, 8192);
    SBAR();
#pragma unroll
    for (int j = 0; j < 8; ++j) {
      const float a = rdlane(ra[j & 1], (j >> 1) * 16), c = rdlane(rb[j & 1], (j >> 1) * 16);
#pragma unroll
      for (int e = 0; e < 4; ++e) { xn[j][e] = __builtin_elementwise_fma(xn[j][e], (f32x2){a, a}, (f32x2){c, c}); PIN(xn[j][e]); }
    }
  }
  asm volatile("s_waitcnt vmcnt(4)" : "+v"(cv1), "+v"(ml)::"memory");
  {
    const float sc = silu_bf(bflo(cv1));
    float v[8];
#pragma unroll
    for (int e = 0; e < 4; ++e) { v[2 * e] = sc * bflo(ml[e]); v[2 * e + 1] = sc * bfhi(ml[e]); }
#pragma unroll
    for (int j = 0; j < 4; ++j) { swap32f(v[j], v[j + 4]); v[j] += v[j + 4]; }
#pragma unroll
    for (int j = 0; j < 2; ++j) { swap16f(v[j], v[j + 2]); v[j] += v[j + 2]; }
    const bool o = lane & 8;
    const float a = (o ? v[1] : v[0]) + dppf<0x128>(o ? v[0] : v[1]);
    sts4f(SCR + 1024 + ((lane & 7) * 8 + (lane >> 3)) * 32 + w * 4, a);
  }
  STAMP(22);
  QKV_DMA(0, 0); QKV_DMA(0, 1); QKV_DMA(0, 2); QKV_DMA(1, 0); QKV_DMA(1, 1); QKV_DMA(1, 2);
  {
    WAIT4X(6, a0, a1, a2, a3);
#pragma unroll 1
    for (u32 spins = 0; spins < SPIN_CAP; ++spins) {
      if (__all(tags_ok(a0, a1, tag) && tags_ok(a2, a3, tag))) break;
      LD4X(a0, a1, a2, a3, lv, WS_MODC + (u32)xcd * 32768 + w * 1024, 8192);
      WAIT4X(0, a0, a1, a2, a3);
    }
    if (w == 0) __builtin_amdgcn_s_setprio(0);
    *(LAS u32*)(uintptr_t)(SCR + 4096 + tid * 4) = pk2(gsum(a0, a1, a2, a3, 0), gsum(a0, a1, a2, a3, 2));
  }
  STAMP(23);
  LBAR();
  STAMP(2);
  {
    const u32x4 sa = lds16u(SCR + 4096 + lv), ca = lds16u(SCR + 5120 + lv);
    f32x2 fs[4], fa[4];
#pragma unroll
    for (int e = 0; e < 4; ++e) {
      const u32 t = pk2(1.f + bflo(ca[e]), 1.f + bfhi(ca[e]));
      fs[e] = (f32x2){bflo(t), bfhi(t)}; fa[e] = (f32x2){bflo(sa[e]), bfhi(sa[e])};
    }
#pragma unroll
    for (int j = 0; j < 8; ++j) {
      u32x4 h;
#pragma unroll
      for (int e = 0; e < 4; ++e) { const f32x2 t = __builtin_elementwise_fma(xn[j][e], fs[e], fa[e]); h[e] = pk2(t[0], t[1]); }
      sts16(aw[j], h);
    }
    if (w == 1) st_gran(ws, WS_MODL + (u32)(bi >> 2) * 16384 + (u32)((xcd * 4 + (bi & 3)) * 64 + lane) * 8,
                        sum8(lds16f(SCR + 1024 + lane * 32), lds16f(SCR + 1024 + lane * 32 + 16)), tag);
  }
  const rsrc_t rwg = __builtin_amdgcn_make_buffer_rsrc((void*)(wmat ? pwu : pwg), 0, bi < 28 ? 512 * 1792 * 2 : 0, 0x00020000);
#define WG_DMA(i) dma(65536 + w * 2048 + (i) * 1024, pg, rwg, (i) * (8 * 3584))
  u32x4 g0, g1, g2, g3, xres;
  {
    f32x4 acc[2][3] = {};
    bf16x8 fa[2][2][2], fb[2][2][3];
#define Q_RA(S, s, kk, m) fa[S][kk][m] = lds16(ab[(2 * (s) + (kk)) & 3] + ((2 * (s) + (kk)) >> 2) * 256 + (m) * 16384)
#define Q_RB(S, s, kk, sg) fb[S][kk][sg] = ldtr((((s) & 2) ? bh : bl) + ((s) & 1) * 24576 + (sg) * 8192 + (kk) * 4096)
#define Q_M(S, kk, m, sg) do { acc[m][sg] = (sg) < 2 ? MFMA16(fb[S][kk][sg], fa[S][kk][m], acc[m][sg]) : MFMA16(fa[S][kk][m], fb[S][kk][sg], acc[m][sg]); PIN(acc[m][sg]); } while (0)
#define Q_STAGE(C, N, s, RD, D0, D1, D2, D3) do { \
      if (RD) { Q_RA(N, s, 0, 0); Q_RA(N, s, 0, 1); } Q_M(C, 0, 0, 0); SBAR(); \
      if (RD) Q_RB(N, s, 0, 0); Q_M(C, 0, 0, 1); SBAR(); \
      if (RD) Q_RB(N, s, 0, 1); Q_M(C, 0, 0, 2); SBAR(); \
      if (RD) Q_RB(N, s, 0, 2); Q_M(C, 0, 1, 0); SBAR(); \
      if (RD) { Q_RA(N, s, 1, 0); Q_RA(N, s, 1, 1); } Q_M(C, 0, 1, 1); SBAR(); \
      if (RD) Q_RB(N, s, 1, 0); Q_M(C, 0, 1, 2); SBAR(); \
      if (RD) Q_RB(N, s, 1, 1); Q_M(C, 1, 0, 0); SBAR(); \
      if (RD) Q_RB(N, s, 1, 2); Q_M(C, 1, 0, 1); SBAR(); \
      D0; Q_M(C, 1, 0, 2); SBAR(); D1; Q_M(C, 1, 1, 0); SBAR(); D2; Q_M(C, 1, 1, 1); SBAR(); D3; Q_M(C, 1, 1, 2); SBAR(); } while (0)
    WAITBAR(0);
    QKV_DMA(2, 0); QKV_DMA(2, 1); QKV_DMA(2, 2);
#pragma unroll
    for (int kk = 0; kk < 2; ++kk) { Q_RA(0, 0, kk, 0); Q_RA(0, 0, kk, 1); Q_RB(0, 0, kk, 0); Q_RB(0, 0, kk, 1); Q_RB(0, 0, kk, 2); }
    STAMP(20);
    LBAR();
    Q_STAGE(0, 1, 1, true, QKV_DMA(3, 0), QKV_DMA(3, 1), QKV_DMA(3, 2), QKV_DMA(4, 0));
    WAITBAR(4); Q_STAGE(1, 0, 2, true, QKV_DMA(4, 1), QKV_DMA(4, 2), QKV_DMA(5, 0), QKV_DMA(5, 1));
    WAITBAR(5); Q_STAGE(0, 1, 3, true, QKV_DMA(5, 2), QKV_DMA(6, 0), QKV_DMA(6, 1), QKV_DMA(6, 2));
    WAITBAR(6); Q_STAGE(1, 0, 4, true, QKV_DMA(7, 0), QKV_DMA(7, 1), QKV_DMA(7, 2), NOP_);
    WAITBAR(6); Q_STAGE(0, 1, 5, true, WG_DMA(0), WG_DMA(1), ld16(xres, xro, rx, 0), NOP_);
    WAITBAR(6); Q_STAGE(1, 0, 6, true, LD4X(g0, g1, g2, g3, gvo, WS_MODL, 16384), NOP_, NOP_, NOP_);
    WAITBAR(7); Q_STAGE(0, 1, 7, true, NOP_, NOP_, NOP_, NOP_);
    Q_STAGE(1, 0, 0, false, NOP_, NOP_, NOP_, NOP_);
    STAMP(3);
    constexpr float QS = 0.25f * 1.4426950408889634f;
    {
      u32 p0 = pk2(acc[0][0][0] * QS, acc[0][0][1] * QS), p1 = pk2(acc[0][0][2] * QS, acc[0][0][3] * QS);
      u32 r0 = pk2(acc[1][0][0] * QS, acc[1][0][1] * QS), r1 = pk2(acc[1][0][2] * QS, acc[1][0][3] * QS);
      swap16u(p0, r0); swap16u(p1, r1);
      st16_sc1((u32x4){p0, p1, r0, r1}, rws, WS_Q + qdst);
    }
    {
      u32 p0 = pk2(acc[0][1][0], acc[0][1][1]), p1 = pk2(acc[0][1][2], acc[0][1][3]);
      u32 r0 = pk2(acc[1][1][0], acc[1][1][1]), r1 = pk2(acc[1][1][2], acc[1][1][3]);
      swap16u(p0, r0); swap16u(p1, r1);
      st16_sc1((u32x4){p0, p1, r0, r1}, rws, WS_K + qdst);
    }
    {
      u32 p0 = pk2(acc[0][2][0], acc[0][2][1]), p1 = pk2(acc[0][2][2], acc[0][2][3]);
      u32 r0 = pk2(acc[1][2][0], acc[1][2][1]), r1 = pk2(acc[1][2][2], acc[1][2][3]);
      swap32u(p0, r0); swap32u(p1, r1);
      st16_sc1((u32x4){p0, p1, r0, r1}, rws, WS_V + qdst);
    }
  }
  u32x4 gav, gbv, x1v, q;
  {
    pv = (u32)(lane >> 3) * 1024 + (((lane & 7) ^ (((lane >> 4) & 1) << 2)) * 16) + n64 * 128;
#pragma unroll
    for (int e = 0; e < 16; ++e) ms[e] = (e & 3) + 8 * (e >> 2) + 4 * (lane >> 5) > (lane & 31) ? 0xffffffffu : 0u;
#pragma unroll
    for (int e = 0; e < 8; ++e) mk[e] = (ms[2 * e] & 0xffffu) | (ms[2 * e + 1] & 0xffff0000u);
    kb = (u32)(w * 1024 + lane * 16);
    vb0 = 16384 + w * 1040 + ((lane & 31) < 16 ? (lane >> 5) * 256 + (lane & 31) * 16 : 1024);
    vb1 = 16384 + w * 1040 + ((lane & 31) < 16 ? 512 + (lane >> 5) * 256 + (lane & 31) * 16 : 1024);
    const int wcu = wg * 4 + g, wn = wcu / 7, wfr = wcu - wn * 7;
    wdst = WS_WP + (u32)wn * WPN + wfr * 1024 + (u32)(wmat * 16 + fr) * 16;
    ta = 65536 + w * 2048 + (u32)(fr >> 2) * 128 + ((g ^ ((fr >> 2) & 2)) * 32) + (lane & 3) * 8;
    odst = WS_O + (u32)((8 * range + w) * 32 + head2) * 1024 + lv;
    const int hd = 4 * grp + (lane & 3), rg = (m64a >> 2) + ((lane >> 2) & 1);
    postq = lane < 4 || (lane < 8 && rg < 8) ? mbox(hd >> 2, (rg << 2) | (hd & 3), MB_QKV + (m64a & 3) + (lane < 4 ? 4 : 0)) : lane == 8 ? WS_ARR + b * 4 : NOWHERE;
    posto = lane < 32 ? mbox(range, lane, MB_O + head2) : NOWHERE;
    const int wnp = (wg * 4 + (lane < 16 ? 0 : 3)) / 7;
    postw = lane < 32 ? mbox(((fr >> 3) << 2) | (wnp >> 2), ((wnp & 3) << 3) | (lane & 7), wmat * 12 + (wg - ((7 * wnp) >> 2)) * 4 + wq) : NOWHERE;
    pollq = mbox(xcd, bi, MB_QKV + (w >> 1) + ((lane & 1) || range == 0 ? 4 : 0));
    const int l24 = (lane & 31) < 24 ? (lane & 31) : 0, l12 = l24 < 12 ? l24 : l24 - 12;
    const int wslot = l12 < 4 * (((7 * nt + 6) >> 2) - ((7 * nt) >> 2) + 1) ? l24 : 0;
    pollw = mbox(xcd, bi, MB_W + wslot);
    pollx = mbox(xcd, bi, lane < 32 ? MB_X1 + ((w >> 1) & 1) * 8 + (lane & 7) : MB_E + wslot);
    PIN(pv); PIN(kb); PIN(vb0); PIN(vb1); PIN(wdst); PIN(ta); PIN(odst); PIN(postq); PIN(posto); PIN(postw); PIN(pollq); PIN(pollw); PIN(pollx);
#pragma unroll
    for (int j = 0; j < 8; ++j) PIN(mk[j]);
#pragma unroll
    for (int e = 0; e < 16; ++e) PIN(ms[e]);
  }
  {
    asm volatile("s_waitcnt vmcnt(0) lgkmcnt(0)\n\ts_barrier" : "+v"(g0), "+v"(g1), "+v"(g2), "+v"(g3), "+v"(xres)::"memory");
    if (w == 0) post(postq, rws, tag);
    STAMP(4);
    if (tid < 16) { const u32 o1 = tid >= 8 || range > 0 ? 0x3f803f80u : 0u; sts16(16384 + tid * 1040 + 1024, (u32x4){o1, o1, o1, o1}); }
#pragma unroll 1
    for (u32 spins = 0; spins < SPIN_CAP; ++spins) {
      if (__all(tags_ok(g0, g1, tag) && tags_ok(g2, g3, tag))) break;
      __builtin_amdgcn_s_sleep(1);
      LD4X(g0, g1, g2, g3, gvo, WS_MODL, 16384);
      WAIT4X(0, g0, g1, g2, g3);
    }
    const u32 gs = 49152 + w * 128;
    {
      float t0 = gsum(g0, g1, g2, g3, 0), t1 = gsum(g0, g1, g2, g3, 2);
      t0 += dppf<0x128>(t0); t1 += dppf<0x128>(t1);
      u32 t = pk2(t0, t1);
      if ((lane >> 4) == 1) t = pk2(1.f + bflo(t), 1.f + bfhi(t));
      if ((lane & 8) == 0) *(LAS u32*)(uintptr_t)(gs + (lane >> 4) * 32 + (lane & 7) * 4) = t;
      asm volatile("s_waitcnt lgkmcnt(0)" ::: "memory");
    }
    gav = lds16u(gs + 64 + (lane >> 5) * 16); gbv = lds16u(gs + 96 + (lane >> 5) * 16);
    if (bi < 28) {
      float u = 0.f, bs = 0.f;
#pragma unroll
      for (int h = 0; h < 2; ++h) {
        const u32x4 wv = __builtin_bit_cast(u32x4, ldtr(ta + h * 1024)), sb = lds16u(gs + h * 16), cb = lds16u(gs + 32 + h * 16);
        u32x4 e;
#pragma unroll
        for (int j = 0; j < 4; ++j) {
          e[j] = pk2(bflo(wv[j]) * bflo(cb[j]), bfhi(wv[j]) * bfhi(cb[j]));
          u += bflo(e[j]) + bfhi(e[j]);
          bs = __builtin_fmaf(bflo(wv[j]), bflo(sb[j]), __builtin_fmaf(bfhi(wv[j]), bfhi(sb[j]), bs));
        }
        st16_sc1(e, rws, wdst + h * 512 + (u32)(wq * 8 + w) * 7168);
      }
      sts4f(SC2 + lane * 32 + w * 4, u); sts4f(SC2 + 2048 + lane * 32 + w * 4, bs);
    }
    STAMP(5);
    poll(pollq, rws, tag);
    STAMP(6);
    const u32 src = (u32)(head2 * 64 + 8 * range + w) * 1024;
    const rsrc_t rh = __builtin_amdgcn_make_buffer_rsrc(ws, 0, range > 0 ? WS_BYTES : 0, 0x00020000);
    dma_sc1((w + 8) * 1024, lv, rws, WS_K + src);
    ld16_sc1(q, lv, rws, WS_Q + src);
    dma_sc1(w * 1024, lv, rh, WS_K + src - 8192);
    dma_sc1(16384 + (w + 8) * 1040, lv, rws, WS_V + src);
    dma_sc1(16384 + w * 1040, lv, rh, WS_V + src - 8192);
  }
  {
    asm volatile("s_waitcnt vmcnt(0) lgkmcnt(0)\n\ts_barrier" : "+v"(q)::"memory");
    STAMP(7);
    if (bi < 28 && w == 7) {
      post(postw + MB_W * 4, rws, tag);
      const float t = sum8(lds16f(SC2 + lane * 32), lds16f(SC2 + lane * 32 + 16)), o = sum8(lds16f(SC2 + 2048 + lane * 32), lds16f(SC2 + 2048 + lane * 32 + 16));
      const float uh = rbf(t), ul = t - uh, bh = rbf(o), bl = o - bh;
      st16_sc1((u32x4){pk2(uh, ul), pk2(uh, bh), pk2(bl, bh), 0u}, rws, wdst + (wq & 1) * 512 + (u32)(32 + (wq >> 1)) * 7168);
    }
    const u32x4 ov = attn_wave(range > 0, w, lane, BF8(q), mk, ms, kb, vb0, vb1, PRE_ARGS,
                               pv, rwo, (u32)n64 | (u32)(bi & 3) << 9);
    st16_sc1(ov, rws, odst);
    STAMP(8);
    u32 fw;
    ld4_sc1(fw, pollw, rws, 0);
    {
#pragma unroll
      for (int c = 0; c < 2; ++c) tb[c] = (u32)(8 * (lane >> 5) + (fr >> 2)) * 128 + ((((2 * c + (g & 1) + ksl) & 3) ^ ((fr >> 2) & 2)) * 32) + (lane & 3) * 8;
      xdst3 = WS_X1 + (u32)(tile3 * 32 + n64 * 4 + ksl) * 1024 + lv;
      adst = WS_ACT + (u32)((4 * m128 + hn) * 112 + nt * 7 + (w & 4)) * 1024 + lv;
      postx = lane < 16 ? mbox(((m64 >> 4) << 2) | (lane >> 2), ((lane & 3) << 3) | ((m64 >> 1) & 7), MB_X1 + (m64 & 1) * 8 + n64) : NOWHERE;
      posta = lane < 16 ? mbox(m128 >> 1, ((lane >> 1) << 2) | ((2 * m128 + (lane & 1)) & 3), MB_ACT + nt) : NOWHERE;
      pollo = mbox(xcd, bi, MB_O + ksl * 8 + (lane & 7));
      polla = mbox(xcd, bi, MB_ACT + ksl * 4 + (lane & 3));
#pragma unroll
      for (int j = 1; j < 4; ++j) rbw[j - 1] = (mh ? 36864 : 65536) + (u32)(((ksl + j) & 3) * 3 + j - 1) * 2048 + lv;
      rbr = (mh ? 36864 : 65536) + (u32)ksl * 6144 + lv;
      wl = lv + (w & 4) * 1024; xl = lv + 114688 + hn * 4096;
      stv = (u32)(lane >> 5) * 2048 + (lane & 31) * 16;
      PIN(tb[0]); PIN(tb[1]); PIN(xdst3); PIN(adst); PIN(postx); PIN(posta); PIN(pollo); PIN(polla); PIN(rbw[0]); PIN(rbw[1]); PIN(rbw[2]); PIN(rbr); PIN(wl); PIN(xl); PIN(stv);
    }
    asm volatile("s_waitcnt vmcnt(0) lgkmcnt(0)\n\ts_barrier" : "+v"(fw)::"memory");
    if (w == 0) post(posto, rws, tag);
    if (bi < 28 && w == 7) post(postw + MB_E * 4, rws, tag);
    STAMP(21);
    if (!__all(fw == tag)) poll(pollw, rws, tag);
#pragma unroll
    for (int i = 0; i < 4; ++i) if (i < 3 || w < 4) dma_sc1(w * 1024 + i * 8192, lv, rws, WS_WP + (u32)nt * WPN + ((2 * nt + m128) & 7) * 28672 + w * 1024 + i * 8192);
    bf16x8 wf[8][2];
#pragma unroll
    for (int ks = 0; ks < 8; ++ks)
#pragma unroll
      for (int c = 0; c < 2; ++c) wf[ks][c] = ldtr(98304 + ksl * 16384 + tb[c] + ks * 2048);
    if (b == 255 && w == 1) {
#pragma unroll 1
      for (int i = 0; i < 4; ++i) poll(WS_ARR + i * 256 + lane * 4, rws, tag);
      if (lane == 0) __hip_atomic_store((GAS u32*)(ws + WS_SEED), tag, RLX_AGENT);
    }
    poll(pollo, rws, tag);
    STAMP(9);
    u32x4 of[8];
#pragma unroll
    for (int ks = 0; ks < 8; ++ks) ld16_sc1(of[ks], lv, rws, WS_O + (u32)(tile3 * 32 + ksl * 8 + (ks ^ n64)) * 1024);
    f32x16 acc[2] = {};
#pragma unroll
    for (int ks = 0; ks < 8; ++ks) {
      WAITV1(7 - ks, of[ks]);
      acc[0] = MFMA32(wf[ks][0], BF8(of[ks]), acc[0]);
      acc[1] = MFMA32(wf[ks][1], BF8(of[ks]), acc[1]);
      PIN(acc[0]); PIN(acc[1]);
    }
#pragma unroll
    for (int j = 1; j < 4; ++j) {
      const u32 a = rbw[j - 1];
      const int o = (j & 1) * 8;
      sts16f(a, (f32x4){acc[j >> 1][o], acc[j >> 1][o + 1], acc[j >> 1][o + 2], acc[j >> 1][o + 3]});
      sts16f(a + 1024, (f32x4){acc[j >> 1][o + 4], acc[j >> 1][o + 5], acc[j >> 1][o + 6], acc[j >> 1][o + 7]});
    }
    LBAR();
    float y[8];
#pragma unroll
    for (int e = 0; e < 8; ++e) y[e] = acc[0][e];
#pragma unroll
    for (int j = 0; j < 3; ++j) {
      const f32x4 a = lds16f(rbr + j * 2048), d = lds16f(rbr + j * 2048 + 1024);
#pragma unroll
      for (int e = 0; e < 4; ++e) { y[e] += a[e]; y[4 + e] += d[e]; }
    }
    x1v = resid_epi(y, xres, gav);
    st16_sc1(x1v, rws, xdst3);
    STAMP(10);
    {
      float s = 0.f, q2 = 0.f;
#pragma unroll
      for (int e = 0; e < 4; ++e) { const float a = bflo(x1v[e]), c = bfhi(x1v[e]); s += a + c; q2 = __builtin_fmaf(a, a, __builtin_fmaf(c, c, q2)); }
      swap32f(s, q2);
      sts4f(SC3 + (u32)(mh * 1024 + ksl * 256 + (lane & 31) * 8 + (lane >> 5) * 4), s + q2);
    }
    WAITBAR(0);
    if (w == 2) post(postx, rws, tag);
    if (ksl == 0) {
      const u32 a = SC3 + (u32)(mh * 1024 + (lane & 31) * 8 + (lane >> 5) * 4);
      const float t = (lds4f(a) + lds4f(a + 256)) + (lds4f(a + 512) + lds4f(a + 768));
      st_gran(ws, WS_ST + (u32)(tile3 * 8 + n64) * 512 + (lane & 31) * 16 + (lane >> 5) * 8, t, tag);
      asm volatile("s_waitcnt lgkmcnt(0)" ::: "memory");
    }
    STAMP(11);
  }
  const u32 r5 = (u32)(((n64 * 7) >> 1) + 7 * (bi & 3)) % 28;
  u32 wh = wl + 57344;
  PIN(wh);
  if (w < 4) ffn_up<4>(w, lane, tag, ws, rws, rwd, pv, m128, nt, r5, pollx, wl, wh, xl, stv, adst, b DBGPASS);
  else ffn_up<3>(w, lane, tag, ws, rws, rwd, pv, m128, nt, r5, pollx, wl, wh, xl, stv, adst, b DBGPASS);
  STAMP(16);
  {
    u32 t0 = tb[0] + ksl * 40960, t1 = tb[1] + ksl * 40960;
    PIN(t0); PIN(t1);
    WAITBAR(0);
    if (w == 0) post(posta, rws, tag);
    bf16x8 wp[8][2];
#pragma unroll
    for (int s = 0; s < 8; ++s) { wp[s][0] = ldtr(t0 + s * 2048); wp[s][1] = ldtr(t1 + s * 2048); }
    LBAR();
#define DN_RF(i) dma((u32)((w >> 1) * 40 + (w & 1) * 8 + (i)) * 1024, pv, rwd, (u32)((w >> 1) * 448 + ((i) & 1) * 8 + rot28(20 + (w & 1) * 4 + ((i) >> 1), r5) * 16) * 1024)
    DN_RF(0); DN_RF(1); DN_RF(2); DN_RF(3);
    DN_RF(4); DN_RF(5); DN_RF(6); DN_RF(7);
    STAMP(15);
    poll(polla, rws, tag);
    STAMP(17);
    u32x4 af[28];
    const u32 asrc = WS_ACT + (u32)(tile3 * 112 + ksl * 28) * 1024;
#pragma unroll
    for (int ks = 0; ks < DEP; ++ks) ld16_sc1(af[ks], lv, rws, asrc + rot28(ks, r5) * 1024);
    f32x16 acc[2] = {};
    bf16x8 wa0, wa1, wb0, wb1;
#define DN_PG(s) ((s) < 20 ? (s) * 2048 : ((s) - 20) * 2048)
#define DN_RD(A, B, s) do { A = ldtr(t0 + DN_PG(s)); B = ldtr(t1 + DN_PG(s)); } while (0)
#define DN_MM(A, B, ks) do { if ((ks) + DEP < 28) ld16_sc1(af[(ks) + DEP < 28 ? (ks) + DEP : 0], lv, rws, asrc + rot28((ks) + DEP, r5) * 1024); \
      WAITV1((ks) + DEP < 28 ? DEP : 27 - (ks), af[ks]); \
      acc[0] = MFMA32(A, BF8(af[ks]), acc[0]); PIN(acc[0]); acc[1] = MFMA32(B, BF8(af[ks]), acc[1]); PIN(acc[1]); SBAR(); } while (0)
#pragma unroll
    for (int ks = 0; ks < 7; ++ks) DN_MM(wp[ks][0], wp[ks][1], ks);
    DN_RD(wa0, wa1, 8); DN_MM(wp[7][0], wp[7][1], 7);
#pragma unroll
    for (int ks = 8; ks < 28; ks += 2) {
      DN_RD(wb0, wb1, ks + 1); DN_MM(wa0, wa1, ks);
      if (ks == 18) LBAR();
      if (ks + 2 < 28) DN_RD(wa0, wa1, ks + 2);
      DN_MM(wb0, wb1, ks + 1);
    }
    STAMP(18);
    const u32 sb = (u32)ksl * 40960 + 16384 + mh * 6144 + lv;
#pragma unroll
    for (int j = 1; j < 4; ++j) {
      const int o = (j & 1) * 8;
      sts16f(sb + (j - 1) * 2048, (f32x4){acc[j >> 1][o], acc[j >> 1][o + 1], acc[j >> 1][o + 2], acc[j >> 1][o + 3]});
      sts16f(sb + (j - 1) * 2048 + 1024, (f32x4){acc[j >> 1][o + 4], acc[j >> 1][o + 5], acc[j >> 1][o + 6], acc[j >> 1][o + 7]});
    }
    LBAR();
    float y[8];
#pragma unroll
    for (int e = 0; e < 8; ++e) y[e] = acc[0][e];
#pragma unroll
    for (int j = 1; j < 4; ++j) {
      const u32 a = (u32)((ksl - j) & 3) * 40960 + 16384 + mh * 6144 + (j - 1) * 2048 + lv;
      const f32x4 c = lds16f(a), d = lds16f(a + 1024);
#pragma unroll
      for (int e = 0; e < 4; ++e) { y[e] += c[e]; y[4 + e] += d[e]; }
    }
    const u32x4 ov = resid_epi(y, x1v, gbv);
    __builtin_amdgcn_raw_buffer_store_b128(ov, rout, xro, 0, 0);
    STAMP(19);
    STAMP_END();
  }
}

extern "C" void kernel_launch(void* const* d_in, const int* in_sizes, int n_in, void* d_out, int out_size, void* d_ws, size_t ws_size,
                              hipStream_t stream) {
  fused_block<<<256, 512, 0, stream>>>(d_in[0], d_in[1], d_in[2], d_in[4], d_in[5], d_in[6], d_in[7], d_in[8], d_out, (char*)d_ws);
}
```

```cpp
#include <hip/hip_runtime.h>
#include <stdint.h>

typedef unsigned u32;
typedef unsigned long long u64;
typedef __attribute__((ext_vector_type(2))) u32 u32x2;
typedef __attribute__((ext_vector_type(4))) u32 u32x4;
typedef __attribute__((ext_vector_type(2))) float f32x2;
typedef __attribute__((ext_vector_type(4))) float f32x4;
typedef __attribute__((ext_vector_type(16))) float f32x16;
typedef __attribute__((ext_vector_type(8))) short bf16x8;
typedef __attribute__((ext_vector_type(2))) __bf16 bf16x2;
typedef __amdgpu_buffer_rsrc_t rsrc_t;
#define LAS __attribute__((address_space(3)))
#define GAS __attribute__((address_space(1)))
#define DI __device__ __forceinline__
#define RLX_AGENT __ATOMIC_RELAXED, __HIP_MEMORY_SCOPE_AGENT

constexpr u32 WS_SEED = 0;
constexpr u32 WS_ARR = 4096;
constexpr u32 WS_MODL = 65536;
constexpr u32 WS_MODC = 524288;
constexpr u32 WS_MB = 262144;
constexpr u32 MB_QKV = 0, MB_O = 32, MB_X1 = 64, MB_E = 96, MB_W = 128, MB_ACT = 160;
constexpr u32 WS_ST = 786432;
constexpr u32 WS_Q = 1u << 20, WS_K = 3u << 20, WS_V = 5u << 20;
constexpr u32 WS_O = 7u << 20;
constexpr u32 WS_X1 = 9u << 20;
constexpr u32 WS_ACT = 11u << 20;
constexpr u32 WS_WP = 18u << 20;
constexpr u32 WPN = 34 * 7168;
constexpr u32 WS_BYTES = 22u << 20;
constexpr int DEP = 10;
constexpr u32 SPIN_CAP = 1u << 17;

DI float bflo(u32 w) { return __uint_as_float(w << 16); }
DI float bfhi(u32 w) { return __uint_as_float(w & 0xffff0000u); }
DI u32 pk2(float a, float b) { f32x2 v = {a, b}; return __builtin_bit_cast(u32, __builtin_convertvector(v, bf16x2)); }
DI float rbf(float a) { return bflo(pk2(a, a)); }
template <int CTRL> DI float dppf(float x) { return __builtin_bit_cast(float, __builtin_amdgcn_update_dpp(0, __builtin_bit_cast(int, x), CTRL, 0xf, 0xf, true)); }
DI void swap32f(float& a, float& b) { auto r = __builtin_amdgcn_permlane32_swap(__float_as_uint(a), __float_as_uint(b), false, false); a = __uint_as_float(r[0]); b = __uint_as_float(r[1]); }
DI void swap16f(float& a, float& b) { auto r = __builtin_amdgcn_permlane16_swap(__float_as_uint(a), __float_as_uint(b), false, false); a = __uint_as_float(r[0]); b = __uint_as_float(r[1]); }
DI void swap32u(u32& a, u32& b) { auto r = __builtin_amdgcn_permlane32_swap(a, b, false, false); a = r[0]; b = r[1]; }
DI void swap16u(u32& a, u32& b) { auto r = __builtin_amdgcn_permlane16_swap(a, b, false, false); a = r[0]; b = r[1]; }
DI float row16_sum(float x) { x += dppf<0xB1>(x); x += dppf<0x4E>(x); x += dppf<0x141>(x); x += dppf<0x140>(x); return x; }
DI float rdlane(float x, int l) { return __uint_as_float(__builtin_amdgcn_readlane(__float_as_uint(x), l)); }

#define MFMA16(a, b, c) __builtin_amdgcn_mfma_f32_16x16x32_bf16((a), (b), (c), 0, 0, 0)
#define MFMA32(a, b, c) __builtin_amdgcn_mfma_f32_32x32x16_bf16((a), (b), (c), 0, 0, 0)
#define BF8(x) __builtin_bit_cast(bf16x8, (x))
#define WAITV(n) asm volatile("s_waitcnt vmcnt(%0)" ::"i"(n) : "memory")
#define WAITBAR(n) asm volatile("s_waitcnt vmcnt(%0) lgkmcnt(0)\n\ts_barrier" ::"i"(n) : "memory")
#define LBAR() asm volatile("s_waitcnt lgkmcnt(0)\n\ts_barrier" ::: "memory")
#define SBAR() __builtin_amdgcn_sched_barrier(0)
#define NOP_ do {} while (0)
#define TWO(a, b) do { a; b; } while (0)

DI void dma(u32 lds, u32 voff, rsrc_t rs, u32 soff) {
  asm volatile("s_mov_b32 m0, %0\n\ts_nop 0\n\tbuffer_load_dwordx4 %1, %2, %3 offen lds" ::"s"(lds), "v"(voff), "s"(rs), "s"(soff) : "memory");
}
DI void dma_sc1(u32 lds, u32 voff, rsrc_t rs, u32 soff) {
  asm volatile("s_mov_b32 m0, %0\n\ts_nop 0\n\tbuffer_load_dwordx4 %1, %2, %3 offen sc1 lds" ::"s"(lds), "v"(voff), "s"(rs), "s"(soff) : "memory");
}
DI void ld16(u32x4& r, u32 voff, rsrc_t rs, u32 soff) { asm volatile("buffer_load_dwordx4 %0, %1, %2, %3 offen" : "=v"(r) : "v"(voff), "s"(rs), "s"(soff) : "memory"); }
DI void ld16_sc1(u32x4& r, u32 voff, rsrc_t rs, u32 soff) { asm volatile("buffer_load_dwordx4 %0, %1, %2, %3 offen sc1" : "=v"(r) : "v"(voff), "s"(rs), "s"(soff) : "memory"); }
DI void ld2(u32& r, u32 voff, rsrc_t rs, u32 soff) { asm volatile("buffer_load_ushort %0, %1, %2, %3 offen" : "=v"(r) : "v"(voff), "s"(rs), "s"(soff) : "memory"); }
#define WAITV1(n, a) asm volatile("s_waitcnt vmcnt(%1)" : "+v"(a) : "i"(n) : "memory")
#define PIN(a) asm volatile("" : "+v"(a))
DI void st16_sc1(u32x4 v, rsrc_t rs, u32 voff) { __builtin_amdgcn_raw_buffer_store_b128(v, rs, voff, 0, 16); }

DI bf16x8 lds16(u32 a) { return *(const LAS bf16x8*)(uintptr_t)a; }
DI u32x4 lds16u(u32 a) { return *(const LAS u32x4*)(uintptr_t)a; }
DI f32x4 lds16f(u32 a) { return *(const LAS f32x4*)(uintptr_t)a; }
DI float lds4f(u32 a) { return *(const LAS float*)(uintptr_t)a; }
DI void sts16(u32 a, u32x4 v) { *(LAS u32x4*)(uintptr_t)a = v; }
DI void sts16f(u32 a, f32x4 v) { *(LAS f32x4*)(uintptr_t)a = v; }
DI void sts4f(u32 a, float v) { *(LAS float*)(uintptr_t)a = v; }
typedef short v4i16_t __attribute__((ext_vector_type(4)));
DI bf16x8 ldtr(u32 a) {
  v4i16_t lo = __builtin_amdgcn_ds_read_tr16_b64_v4i16((LAS v4i16_t*)(uintptr_t)a);
  v4i16_t hi = __builtin_amdgcn_ds_read_tr16_b64_v4i16((LAS v4i16_t*)(uintptr_t)(a + 512));
  return __builtin_shufflevector(lo, hi, 0, 1, 2, 3, 4, 5, 6, 7);
}

DI void poll(u32 voff, rsrc_t rws, u32 tag) {
#pragma unroll 1
  for (u32 spins = 0; spins < SPIN_CAP; ++spins) {
    u32 v;
    asm volatile("buffer_load_dword %0, %1, %2, 0 offen sc1\n\ts_waitcnt vmcnt(0)" : "=v"(v) : "v"(voff), "s"(rws) : "memory");
    if (__all(v == tag)) break;
    __builtin_amdgcn_s_sleep(1);
  }
}
DI void post(u32 voff, rsrc_t rws, u32 tag) { __builtin_amdgcn_raw_buffer_store_b32(tag, rws, voff, 0, 16); }
constexpr u32 NOWHERE = 0x7ffffff0u;
DI u32 mbox(int xcd, int bi, u32 word) { return WS_MB + (u32)(bi * 8 + xcd) * 1024 + word * 4; }
DI bool tags_ok(u32x4 a, u32x4 b, u32 tag) { return a[1] == tag && a[3] == tag && b[1] == tag && b[3] == tag; }
DI float gsum(u32x4 a, u32x4 b, u32x4 c, u32x4 d, int i) { return (__uint_as_float(a[i]) + __uint_as_float(b[i])) + (__uint_as_float(c[i]) + __uint_as_float(d[i])); }
DI void st_gran(char* ws, u32 byte, float v, u32 tag) { __hip_atomic_store((GAS u64*)(ws + byte), ((u64)tag << 32) | __float_as_uint(v), RLX_AGENT); }
DI void ld4_sc1(u32& r, u32 voff, rsrc_t rs, u32 soff) { asm volatile("buffer_load_dword %0, %1, %2, %3 offen sc1" : "=v"(r) : "v"(voff), "s"(rs), "s"(soff) : "memory"); }
#define LD4X(a, b, c, d, voff, base, step) do { ld16_sc1(a, voff, rws, base); ld16_sc1(b, voff, rws, (base) + (step)); ld16_sc1(c, voff, rws, (base) + 2 * (step)); ld16_sc1(d, voff, rws, (base) + 3 * (step)); } while (0)
#define WAIT4X(n, a, b, c, d) asm volatile("s_waitcnt vmcnt(%4)" : "+v"(a), "+v"(b), "+v"(c), "+v"(d) : "i"(n) : "memory")

DI float silu_bf(float c) { return rbf(c * __builtin_amdgcn_rcpf(1.f + __builtin_amdgcn_exp2f(c * -1.4426950408889634f))); }
DI float sum8(f32x4 a, f32x4 d) { return ((a[0] + a[1]) + (a[2] + a[3])) + ((d[0] + d[1]) + (d[2] + d[3])); }

constexpr u64 upper_mask(int e) {
  u64 m = 0;
  for (int l = 0; l < 64; ++l) if ((e & 3) + 8 * (e >> 2) + 4 * (l >> 5) > (l & 31)) m |= 1ull << l;
  return m;
}
template <int E> DI bool upper() { constexpr u64 m = upper_mask(E); return __builtin_amdgcn_inverse_ballot_w64(m); }
#define MX3(a, b, c) __builtin_fmaxf(__builtin_fmaxf((a), (b)), (c))
#define EX2(x) __builtin_amdgcn_exp2f(x)
template <int I> DI void sel_fill(f32x16& sel, const f32x16& sf, const f32x16& sd) {
  if constexpr (I < 16) { sel[I] = upper<I>() ? sf[I] : sd[I]; sel_fill<I + 1>(sel, sf, sd); }
}
#define PV(V, P) do { o = MFMA32(V, BF8(P), o); PIN(o); } while (0)
DI float half_max(const f32x16& s) {
  float m0 = __builtin_fmaxf(s[0], s[1]), m1 = __builtin_fmaxf(s[2], s[3]);
#pragma unroll
  for (int e = 4; e < 16; e += 4) { m0 = MX3(m0, s[e], s[e + 1]); m1 = MX3(m1, s[e + 2], s[e + 3]); }
  return __builtin_fmaxf(m0, m1);
}
DI float both_max(float m) { float mo = m; swap32f(m, mo); return __builtin_fmaxf(m, mo); }
DI void boundary_p(const f32x16& sel, float m, const u32 (&mk)[8], u32x4 (&pf)[2], u32x4 (&pd)[2]) {
#pragma unroll
  for (int e = 0; e < 8; ++e) {
    const u32 p = pk2(EX2(sel[2 * e] - m), EX2(sel[2 * e + 1] - m));
    pf[e >> 2][e & 3] = p & mk[e];
    pd[e >> 2][e & 3] = p & ~mk[e];
  }
}
DI f32x16 attn_exact(int ilo, bf16x8 qf, const u32 (&mk)[8], u32 kb, u32 vb0, u32 vb1) {
  const f32x16 zero = {};
  f32x16 sel, sf, sd = MFMA32(lds16(kb + 8192), qf, zero);
  if (ilo == 0) sf = MFMA32(lds16(kb), qf, zero);
  else {
#pragma unroll
    for (int e = 0; e < 16; ++e) sf[e] = -INFINITY;
  }
  sel_fill<0>(sel, sf, sd);
  float m = half_max(sel);
#pragma unroll 1
  for (int i = ilo > 1 ? ilo : 1; i < 8; ++i) m = __builtin_fmaxf(m, half_max(MFMA32(lds16(kb + i * 1024), qf, zero)));
  m = both_max(m);
  f32x16 negm;
#pragma unroll
  for (int e = 0; e < 16; ++e) negm[e] = -m;
  u32x4 pf[2], pd[2];
  boundary_p(sel, m, mk, pf, pd);
  f32x16 o = MFMA32(lds16(vb0 + 8 * 1040), BF8(pd[0]), zero);
  o = MFMA32(lds16(vb1 + 8 * 1040), BF8(pd[1]), o);
  if (ilo == 0) { o = MFMA32(lds16(vb0), BF8(pf[0]), o); o = MFMA32(lds16(vb1), BF8(pf[1]), o); }
#pragma unroll 1
  for (int i = ilo > 1 ? ilo : 1; i < 8; ++i) {
    const f32x16 sc = MFMA32(lds16(kb + i * 1024), qf, negm);
    u32x4 p0, p1;
#pragma unroll
    for (int e = 0; e < 4; ++e) { p0[e] = pk2(EX2(sc[2 * e]), EX2(sc[2 * e + 1])); p1[e] = pk2(EX2(sc[8 + 2 * e]), EX2(sc[8 + 2 * e + 1])); }
    o = MFMA32(lds16(vb0 + i * 1040), BF8(p0), o);
    o = MFMA32(lds16(vb1 + i * 1040), BF8(p1), o);
  }
  return o;
}
DI f32x16 attn_fast(int w, int lane, bf16x8 qf, const u32 (&mk)[8], const u32 (&ms)[16], u32 kb, u32 vb0, u32 vb1,
                    u32 pv, rsrc_t rwo, u32 r3) {
#define WO_DMA(i) dma(98304 + ((((i) ^ (r3 >> 8)) * 8 + w) * 1024), pv, rwo, (u32)((((i) ^ (r3 >> 8)) * 8 + w) ^ ((r3 & 7) << 1)) * 8192)
  const f32x16 zero = {};
  bf16x8 k8 = lds16(kb + 8192), k0 = lds16(kb), ka = lds16(kb + 1024), kc = lds16(kb + 2048);
  bf16x8 va = lds16(vb0 + 8 * 1040), vc = lds16(vb1 + 8 * 1040), vd = lds16(vb0), ve = lds16(vb1);
  SBAR();
  f32x16 sa = MFMA32(k8, qf, zero);
  PIN(sa); WO_DMA(0); SBAR();
  const float m = both_max(half_max(sa));
  f32x16 negm;
#pragma unroll
  for (int e = 0; e < 16; ++e) negm[e] = -m;
  PIN(negm); SBAR();
  f32x16 sd = MFMA32(k8, qf, negm), sb = MFMA32(k0, qf, negm);
  sa = MFMA32(ka, qf, negm);
  PIN(sd); PIN(sb); PIN(sa); WO_DMA(1); SBAR();
  f32x16 o = zero;
  u32x4 pd[2], pf[2], p0, p1;
#pragma unroll
  for (int e = 0; e < 8; ++e) {
    const float x0 = __uint_as_float((__float_as_uint(sb[2 * e]) & ms[2 * e]) | (__float_as_uint(sd[2 * e]) & ~ms[2 * e]));
    const float x1 = __uint_as_float((__float_as_uint(sb[2 * e + 1]) & ms[2 * e + 1]) | (__float_as_uint(sd[2 * e + 1]) & ~ms[2 * e + 1]));
    const u32 p = pk2(EX2(x0), EX2(x1));
    pf[e >> 2][e & 3] = p & mk[e];
    pd[e >> 2][e & 3] = p & ~mk[e];
  }
  PIN(pf[0]); PIN(pf[1]); PIN(pd[0]); PIN(pd[1]); WO_DMA(2); SBAR();
#define AT_Q(P, S, o_) do { P[(o_) & 3] = pk2(EX2(S[2 * (o_)]), EX2(S[2 * (o_) + 1])); P[((o_) & 3) + 1] = pk2(EX2(S[2 * (o_) + 2]), EX2(S[2 * (o_) + 3])); PIN(P); SBAR(); } while (0)
#define AT_STEP(S, N, KN, KR, i, M0, M1, M2, M3) do { \
    if ((i) < 7) { N = MFMA32(KN, qf, negm); PIN(N); if ((i) < 6) KR = lds16(kb + ((i) + 2) * 1024); SBAR(); } \
    u32x4 q0_, q1_; \
    AT_Q(q0_, S, 0); M0; SBAR(); AT_Q(q0_, S, 2); M1; SBAR(); AT_Q(q1_, S, 4); M2; SBAR(); AT_Q(q1_, S, 6); M3; \
    va = lds16(vb0 + (i) * 1040); vc = lds16(vb1 + (i) * 1040); p0 = q0_; p1 = q1_; SBAR(); } while (0)
  AT_STEP(sa, sb, kc, ka, 1, PV(va, pd[0]), PV(vc, pd[1]), PV(vd, pf[0]), PV(ve, pf[1]));
  AT_STEP(sb, sa, ka, kc, 2, NOP_, PV(va, p0), PV(vc, p1), WO_DMA(3));
  AT_STEP(sa, sb, kc, ka, 3, NOP_, PV(va, p0), PV(vc, p1), WO_DMA(4));
  AT_STEP(sb, sa, ka, kc, 4, NOP_, PV(va, p0), PV(vc, p1), WO_DMA(5));
  AT_STEP(sa, sb, kc, ka, 5, NOP_, PV(va, p0), PV(vc, p1), WO_DMA(6));
  AT_STEP(sb, sa, ka, kc, 6, WO_DMA(7), PV(va, p0), PV(vc, p1), NOP_);
  AT_STEP(sa, sb, kc, ka, 7, NOP_, PV(va, p0), PV(vc, p1), NOP_);
  PV(va, p0); PV(vc, p1);
  return o;
}
DI u32x4 attn_wave(bool full, int w, int lane, bf16x8 qf, const u32 (&mk)[8], const u32 (&ms)[16], u32 kb, u32 vb0, u32 vb1,
                   u32 pv, rsrc_t rwo, u32 r3) {
  f32x16 o = attn_fast(w, lane, qf, mk, ms, kb, vb0, vb1, pv, rwo, r3);
  if (!__all(o[8] < 1.8446744e19f && o[8] > 5.4210109e-20f))
    o = attn_exact(full ? 0 : 8 - w, qf, mk, kb, vb0, vb1);
  const float rl = __builtin_amdgcn_rcpf(o[8]);
  u32 a0 = pk2(o[0] * rl, o[1] * rl), a1 = pk2(o[2] * rl, o[3] * rl), b0 = pk2(o[4] * rl, o[5] * rl), b1 = pk2(o[6] * rl, o[7] * rl);
  swap32u(a0, b0); swap32u(a1, b1);
  return (u32x4){a0, a1, b0, b1};
}

DI u32x4 resid_epi(float (&y)[8], u32x4 res, u32x4 gate) {
#pragma unroll
  for (int i = 0; i < 4; ++i) swap32f(y[i], y[4 + i]);
  u32x4 o;
#pragma unroll
  for (int i = 0; i < 4; ++i) {
    o[i] = pk2(__builtin_fmaf(y[2 * i], bflo(gate[i]), bflo(res[i])), __builtin_fmaf(y[2 * i + 1], bfhi(gate[i]), bfhi(res[i])));
  }
  return o;
}

DI u32 rot28(u32 s, u32 r) { const u32 t = s + r; return t >= 28 ? t - 28 : t; }
#define WD_DMA(base) dma((u32)((base) + w) * 1024, pv, rwd, (u32)(((base) / 40) * 448 + (w & 1) * 8 + rot28((((base) % 40) + w) >> 1, r5) * 16) * 1024)

template <int NF>
DI void ffn_up(int w, int lane, u32 tag, char* ws, rsrc_t rws, rsrc_t rwd, u32 pv, int m128, int nt, u32 r5, u32 pollx, u32 wl, u32 wh, u32 xl, u32 stv, u32 adst, int b) {
  constexpr int NW = NF;
  constexpr int F0 = NF == 4 ? 0 : 4;
  const int p = w & 3, tile = 4 * m128 + p;
  const u32 lv = lane * 16;
  const u32 wsrc = WS_WP + (u32)nt * WPN + w * 1024;
  const u32 xsrc = WS_X1 + (u32)(tile * 32 + 2 * (w >> 2)) * 1024;
  const u32 xdst = 114688 + (p * 4 + 2 * (w >> 2)) * 1024;
  const u32 r4 = (2 * nt + m128) & 7;
#define UP_W1(g, i) dma_sc1(((g) & 3) * 28672 + w * 1024 + (i) * 8192, lv, rws, wsrc + ((g) ^ r4) * 28672 + (i) * 8192)
#define UP_X1(g, i) dma_sc1(xdst + ((g) % 3) * 16384 + (i) * 1024, lv, rws, xsrc + ((g) ^ r4) * 4096 + (i) * 1024)
#pragma unroll
  for (int g = 1; g < 4; ++g)
#pragma unroll
    for (int i = 0; i < NW; ++i) UP_W1(g, i);
  poll(pollx, rws, tag);
#pragma unroll
  for (int g = 0; g < 3; ++g) { UP_X1(g, 0); UP_X1(g, 1); }
  f32x16 acc[NF];
#pragma unroll
  for (int f = 0; f < NF; ++f) acc[f] = (f32x16){};
  bf16x8 xa, wa[NF], xb, wb[NF];
  u32x4 s0, s1_, s2_, s3;
#define UP_RX(X, ks) X = lds16(xl + (((ks) >> 2) % 3) * 16384 + ((ks) & 3) * 1024)
#define UP_RW(W, f, ks) W[f] = lds16(((((ks) >> 2) & 2) ? wh : wl) + (((ks) >> 2) & 1) * 28672 + (((ks) & 3) * 7 + (f)) * 1024)
#define UP_STEP(XC, WC, XN, WN, ks, RD, MM, D0, D1) do { \
    if (RD) { UP_RX(XN, ks); UP_RW(WN, 0, ks); } SBAR(); if (MM) { acc[0] = MFMA32(WC[0], XC, acc[0]); PIN(acc[0]); } SBAR(); \
    if (RD) { UP_RW(WN, 1, ks); UP_RW(WN, 2, ks); } SBAR(); if (MM) { acc[1] = MFMA32(WC[1], XC, acc[1]); PIN(acc[1]); } SBAR(); \
    if (RD && NF == 4) UP_RW(WN, NF - 1, ks); D0; SBAR(); if (MM) { acc[2] = MFMA32(WC[2], XC, acc[2]); PIN(acc[2]); } SBAR(); \
    D1; if (MM && NF == 4) { acc[NF - 1] = MFMA32(WC[NF - 1], XC, acc[NF - 1]); PIN(acc[NF - 1]); } SBAR(); } while (0)
#define UP_WL(g) do { if (NF == 4) UP_W1(g, 3); } while (0)
#define UP_GROUP(g, MM0, A0, A1, B0, B1, C0, C1, E0, E1) do { \
    UP_STEP(xb, wb, xa, wa, 4 * (g), true, MM0, A0, A1); UP_STEP(xa, wa, xb, wb, 4 * (g) + 1, true, true, B0, B1); \
    UP_STEP(xb, wb, xa, wa, 4 * (g) + 2, true, true, C0, C1); UP_STEP(xa, wa, xb, wb, 4 * (g) + 3, true, true, E0, E1); } while (0)
#define UP_GREFILL(g, S0, S1) UP_GROUP(g, true, UP_X1((g) + 2, 0), UP_X1((g) + 2, 1), UP_W1((g) + 3, 0), S0, UP_W1((g) + 3, 1), UP_W1((g) + 3, 2), UP_WL((g) + 3), S1)
#define UP_E(i) dma_sc1(((i) * 8 + w) * 1024, lv, rws, (i) * 8 + w < 14 ? WS_WP + (u32)nt * WPN + 32 * 7168 + ((i) * 8 + w) * 1024 : NOWHERE)
  WAITBAR(4); UP_GROUP(0, false, NOP_, NOP_, NOP_, NOP_, NOP_, NOP_, NOP_, NOP_);
  WAITBAR(2); UP_GREFILL(1, NOP_, NOP_);
#define UP_ST(r, i) ld16_sc1(r, stv, rws, WS_ST + (u32)tile * 4096 + (i) * 512)
  WAITBAR(NW + 2); UP_GREFILL(2, UP_ST(s0, 0), NOP_);
  WAITBAR(2 * NW + 3); UP_GREFILL(3, UP_ST(s1_, 1), UP_ST(s2_, 2));
  WAITBAR(2 * NW + 5); UP_GREFILL(4, UP_ST(s3, 3), NOP_);
  WAITBAR(2 * NW + 5);
  UP_GROUP(5, true, UP_X1(7, 0), UP_X1(7, 1), UP_E(0), NOP_, UP_E(1), NOP_, WD_DMA(16), NOP_);
  asm volatile("s_waitcnt vmcnt(%4) lgkmcnt(0)\n\ts_barrier" : "+v"(s0), "+v"(s1_), "+v"(s2_), "+v"(s3) : "i"(NW + 4) : "memory");
  float s1, s2, mu, var, rstd, k1, k3;
  u32x4 ev;
  auto st_a = [&]() __attribute__((always_inline)) {
#pragma unroll 1
    for (u32 spins = 0; spins < SPIN_CAP; ++spins) {
      if (__all(tags_ok(s0, s1_, tag) && tags_ok(s2_, s3, tag))) break;
      LD4X(s0, s1_, s2_, s3, stv, WS_ST + (u32)tile * 4096, 512);
      WAIT4X(0, s0, s1_, s2_, s3);
    }
    s1 = gsum(s0, s1_, s2_, s3, 0); s2 = gsum(s0, s1_, s2_, s3, 2);
    swap32f(s1, s2); s1 += s2; s2 = s1; swap32f(s1, s2);
    PIN(s1); PIN(s2);
  };
  auto st_b = [&]() __attribute__((always_inline)) {
    mu = s1 * (1.f / 512.f); var = __builtin_fmaxf(s2 * (1.f / 512.f) - mu * mu, 0.f) + 1e-5f; rstd = __builtin_amdgcn_rsqf(var);
    k1 = (-2.f * 0.7978845608028654f * 1.4426950408889634f) * rstd; k3 = k1 * 0.044715f * rstd * rstd;
    PIN(mu); PIN(var); PIN(rstd); PIN(k1); PIN(k3);
  };
  auto st_c = [&]() __attribute__((always_inline)) {
    const float sig = var * rstd, mh = rbf(-mu), ml = -mu - mh, sh = rbf(sig), sl = sig - sh;
    ev = (u32x4){pk2(mh, mh), pk2(ml, sh), pk2(sh, sl), 0u};
    PIN(ev);
  };
  UP_GROUP(6, true, WD_DMA(24), TWO(WD_DMA(32), st_a()), WD_DMA(40), st_b(), WD_DMA(48), st_c(), WD_DMA(144), NOP_);
  WAITBAR(6);
  UP_GROUP(7, true, WD_DMA(56), WD_DMA(64), WD_DMA(72), NOP_, WD_DMA(112), NOP_, WD_DMA(120), NOP_);
#pragma unroll
  for (int f = 0; f < NF; ++f) wa[f] = lds16(wl + f * 1024);
  UP_STEP(xb, wb, xa, wa, 0, false, true, NOP_, NOP_);
#pragma unroll
  for (int f = 0; f < NF; ++f) wb[f] = lds16(wl + 7168 + f * 1024);
#pragma unroll
  for (int f = 0; f < NF; ++f) { acc[f] = MFMA32(wa[f], BF8(ev), acc[f]); PIN(acc[f]); }
  LBAR();
#pragma unroll
  for (int f = 0; f < NF; ++f) acc[f] = MFMA32(wb[f], BF8(ev), acc[f]);
  WD_DMA(0); WD_DMA(8); WD_DMA(80); WD_DMA(88);
  SBAR();
#pragma unroll
  for (int f = 0; f < NF; ++f) {
    float a[8];
#pragma unroll
    for (int e = 0; e < 8; ++e) {
      const float g = acc[f][e];
      const float z = g * __builtin_fmaf(g * g, k3, k1);
      a[e] = (g * acc[f][8 + e]) * __builtin_amdgcn_rcpf(__builtin_fmaf(__builtin_amdgcn_exp2f(z), var, var));
    }
    u32 a0 = pk2(a[0], a[1]), a1 = pk2(a[2], a[3]), b0 = pk2(a[4], a[5]), b1 = pk2(a[6], a[7]);
    swap32u(a0, b0); swap32u(a1, b1);
    st16_sc1((u32x4){a0, a1, b0, b1}, rws, adst + f * 1024);
    if (f == 0) { WD_DMA(128); WD_DMA(96); }
    if (f == 1) { WD_DMA(104); WD_DMA(136); }
    if (f == 2) WD_DMA(152);
    SBAR();
  }
}

__global__ void __launch_bounds__(512, 2)
fused_block(const void* __restrict__ px, const void* __restrict__ pwqkv, const void* __restrict__ pwo, const void* __restrict__ pwg,
            const void* __restrict__ pwu, const void* __restrict__ pwd, const void* __restrict__ pc, const void* __restrict__ pwm,
            void* pout, char* ws) {
  __shared__ __attribute__((aligned(16))) char smem[163840];
  asm volatile("" ::"v"((u32)(uintptr_t)(LAS char*)smem));
  const int tid = threadIdx.x, lane = tid & 63, w = __builtin_amdgcn_readfirstlane(tid >> 6);
  const int b = blockIdx.x, xcd = b & 7, bi = b >> 3;
  const rsrc_t rx = __builtin_amdgcn_make_buffer_rsrc((void*)px, 0, 2048 * 512 * 2, 0x00020000);
  const rsrc_t rqkv = __builtin_amdgcn_make_buffer_rsrc((void*)pwqkv, 0, 512 * 1536 * 2, 0x00020000);
  const rsrc_t rwo = __builtin_amdgcn_make_buffer_rsrc((void*)pwo, 0, 512 * 512 * 2, 0x00020000);
  const rsrc_t rwd = __builtin_amdgcn_make_buffer_rsrc((void*)pwd, 0, 1792 * 512 * 2, 0x00020000);
  const rsrc_t rout = __builtin_amdgcn_make_buffer_rsrc(pout, 0, 2048 * 512 * 2, 0x00020000);
  const rsrc_t rws = __builtin_amdgcn_make_buffer_rsrc(ws, 0, WS_BYTES, 0x00020000);
  const u32 lv = lane * 16;

  const int m64a = 8 * (xcd & 3) + (bi & 7), grp = 4 * (xcd >> 2) + (bi >> 3);
  constexpr u32 SCR = 155648;
  const rsrc_t rc = __builtin_amdgcn_make_buffer_rsrc((void*)pc, 0, 512 * 2, 0x00020000);
  const rsrc_t rwm = __builtin_amdgcn_make_buffer_rsrc((void*)pwm, 0, 512 * 3072 * 2, 0x00020000);
  if (w == 0) {
    __builtin_amdgcn_s_setprio(2);
    u32 cv[4], seed; u32x4 m[4];
    ld4_sc1(seed, 0, rws, WS_SEED);
    const u32 r0 = (u32)(bi >> 3) * 128 + (lane >> 1);
#pragma unroll
    for (int i = 0; i < 4; ++i) ld16(m[i], r0 * 6144 + (lane & 1) * 16, rwm, (u32)(xcd * 8 + (bi & 7)) * 32 + i * (32 * 6144));
#pragma unroll
    for (int i = 0; i < 4; ++i) ld2(cv[i], r0 * 2, rc, i * 64);
    asm volatile("s_waitcnt vmcnt(0) lgkmcnt(0)" : "+v"(cv[0]), "+v"(cv[1]), "+v"(cv[2]), "+v"(cv[3]), "+v"(m[0]), "+v"(m[1]), "+v"(m[2]), "+v"(m[3]), "+v"(seed)::"memory");
    float v[8] = {};
#pragma unroll
    for (int i = 0; i < 4; ++i) {
      const float sc = silu_bf(bflo(cv[i]));
#pragma unroll
      for (int e = 0; e < 4; ++e) { v[2 * e] = __builtin_fmaf(sc, bflo(m[i][e]), v[2 * e]); v[2 * e + 1] = __builtin_fmaf(sc, bfhi(m[i][e]), v[2 * e + 1]); }
    }
#pragma unroll
    for (int j = 0; j < 4; ++j) { swap32f(v[j], v[j + 4]); v[j] += v[j + 4]; }
#pragma unroll
    for (int j = 0; j < 2; ++j) { swap16f(v[j], v[j + 2]); v[j] += v[j + 2]; }
    const bool o = lane & 8;
    float t = (o ? v[1] : v[0]) + dppf<0x128>(o ? v[0] : v[1]);
    t += dppf<0x4E>(t);
    t += __builtin_bit_cast(float, __builtin_amdgcn_update_dpp(__builtin_amdgcn_update_dpp(0, __builtin_bit_cast(int, t), 0x104, 0xf, 0x5, false), __builtin_bit_cast(int, t), 0x114, 0xf, 0xa, false));
    const u32 a = WS_MODC + (u32)((lane >> 1) & 3) * 32768 + (u32)(bi >> 3) * 8192 + (u32)((xcd * 8 + (bi & 7)) * 16 + (lane & 1) * 8 + (lane >> 3)) * 8;
    st_gran(ws, a, t, seed + 1u); st_gran(ws, a + 131072, t, seed + 1u);
  }
  if (w == 6) {
    u64 here; asm volatile("s_getpc_b64 %0" : "=s"(here));
    const rsrc_t rcode = __builtin_amdgcn_make_buffer_rsrc((void*)(here & ~127ull), 0, 34816  , 0x00020000);
    asm volatile("s_mov_b32 m0, %0\n\ts_nop 0\n\tbuffer_load_dword %1, %2, %3 offen lds" ::"s"(SCR + 7168), "v"((lane < 9 ? lane : 8) * 128), "s"(rcode), "s"((u32)bi * 1152) : "memory");
  }
  u32 seed;
  ld4_sc1(seed, 0, rws, WS_SEED);
  u32x4 xr[8];
#pragma unroll
  for (int j = 0; j < 8; ++j) ld16(xr[j], lv, rx, (u32)(m64a * 64 + w * 8 + j) * 1024);
  if (w == 7) dma_sc1(SCR + 6144, lv, rws, WS_MB + (u32)b * 1024);
  u32 cv1; u32x4 ml;
  {
    const u32 r1 = (u32)(bi >> 2) * 64 + w * 8 + (lane >> 3);
    ld2(cv1, r1 * 2, rc, 0);
    ld16(ml, r1 * 6144 + (lane & 7) * 16, rwm, 2048 + (u32)(xcd * 4 + (bi & 3)) * 128);
  }
  const u32 r1 = bi & 7;
  const int n64 = bi >> 2, m64 = 4 * xcd + (bi & 3);
  const int mh = w & 1, ksl = w >> 1, tile3 = 2 * m64 + mh;
  const int head2 = 4 * xcd + (bi & 3), range = bi >> 2;
  const int m128 = 8 * (xcd >> 2) + (bi & 7), nt = 4 * (xcd & 3) + (bi >> 3);
  const int wid = xcd * 28 + bi, wq = wid & 3, wmat = wid >= 112, wg = (wid >> 2) - wmat * 28;
  constexpr u32 SC2 = 53248, SC3 = 94208;
  u32 qv, ab[4], bl, bh, aw[8], pv, pg, gvo, xro, tb[2], mk[8], ms[16], kb, vb0, vb1, wdst, ta, qdst, odst, xdst3, adst;
  u32 postq, posto, postw, postx, posta, pollq, pollo, pollw, pollx, polla, rbw[3], rbr, wl, xl, stv;
  const int wm = w >> 2, hn = w & 3, fr = lane & 15, g = lane >> 4;
  {
    qv = (u32)(w * 8 + (lane >> 3)) * 3072 + (((lane & 7) ^ ((((lane >> 4) & 1) | ((w & 1) << 1)) << 1)) * 16) + grp * 128;
#pragma unroll
    for (int t = 0; t < 4; ++t) ab[t] = (u32)(wm * 32 + fr) * 1024 + ((g ^ (fr & 3)) * 16) + ((t ^ (fr >> 2)) * 64);
    bl = 65536 + (u32)(8 * g + (fr >> 2)) * 128 + ((hn ^ (((fr >> 3) & 1) | ((g & 1) << 1))) * 32) + (lane & 3) * 8; bh = bl + 49152;
#pragma unroll
    for (int j = 0; j < 8; ++j) aw[j] = (u32)(w * 8 + j) * 1024 + ((lane ^ (r1 << 3) ^ ((w * 8 + j) & 15)) * 16);
    pg = (u32)(lane >> 3) * 3584 + (((lane & 7) ^ (((lane >> 4) & 1) << 2)) * 16) + (u32)(wq * 128 + w * 16) * 3584 + wg * 128;
    gvo = (u32)((lane >> 3) & 1) * 65536 + (lane & 7) * 16 + 8 * (lane < 32 ? 512 + (lane >> 4) * 512 + wq * 128 + w * 16 : (lane < 48 ? 0 : 1536) + n64 * 64 + ksl * 16);
    xro = (u32)(tile3 * 32 + (lane & 31)) * 1024 + (u32)(n64 * 64 + ksl * 16 + (lane >> 5) * 8) * 2;
    qdst = (u32)((4 * grp + hn) * 64 + 2 * m64a + wm) * 1024 + lv;
    PIN(qv); PIN(bl); PIN(bh); PIN(pg); PIN(gvo); PIN(xro); PIN(qdst);
#pragma unroll
    for (int t = 0; t < 4; ++t) PIN(ab[t]);
#pragma unroll
    for (int j = 0; j < 8; ++j) PIN(aw[j]);
  }
#define QKV_DMA(s, seg) dma(65536 + ((s) & 3) * 24576 + (seg) * 8192 + w * 1024, qv, rqkv, ((s) ^ r1) * (64 * 3072) + (seg) * 1024)
  WAITV1(2, seed);
  const u32 tag = __builtin_amdgcn_readfirstlane(seed) + 1u;
  u32x4 a0, a1, a2, a3;
  f32x2 xn[8][4];
  {
    asm volatile("" : "+v"(xr[0]), "+v"(xr[1]), "+v"(xr[2]), "+v"(xr[3]), "+v"(xr[4]), "+v"(xr[5]), "+v"(xr[6]), "+v"(xr[7])::"memory");
    float S[8], Q[8];
#pragma unroll
    for (int j = 0; j < 8; ++j) {
#pragma unroll
      for (int e = 0; e < 4; ++e) xn[j][e] = (f32x2){bflo(xr[j][e]), bfhi(xr[j][e])};
      const f32x2 s2 = (xn[j][0] + xn[j][1]) + (xn[j][2] + xn[j][3]);
      const f32x2 q2 = __builtin_elementwise_fma(xn[j][0], xn[j][0], __builtin_elementwise_fma(xn[j][1], xn[j][1], __builtin_elementwise_fma(xn[j][2], xn[j][2], xn[j][3] * xn[j][3])));
      S[j] = s2[0] + s2[1]; Q[j] = q2[0] + q2[1];
    }
#pragma unroll
    for (int j = 0; j < 4; ++j) { swap32f(S[j], S[j + 4]); S[j] += S[j + 4]; swap32f(Q[j], Q[j + 4]); Q[j] += Q[j + 4]; }
#pragma unroll
    for (int j = 0; j < 2; ++j) { swap16f(S[j], S[j + 2]); S[j] += S[j + 2]; swap16f(Q[j], Q[j + 2]); Q[j] += Q[j + 2]; }
    float ra[2], rb[2];
#pragma unroll
    for (int j = 0; j < 2; ++j) {
      const float mu = row16_sum(S[j]) * (1.f / 512.f);
      const float var = __builtin_fmaxf(row16_sum(Q[j]) * (1.f / 512.f) - mu * mu, 0.f) + 1e-5f;
      ra[j] = __builtin_amdgcn_rsqf(var); rb[j] = -mu * ra[j];
    }
    SBAR();
    LD4X(a0, a1, a2, a3, lv, WS_MODC + (u32)xcd * 32768 + w * 1024, 8192);
    SBAR();
#pragma unroll
    for (int j = 0; j < 8; ++j) {
      const float a = rdlane(ra[j & 1], (j >> 1) * 16), c = rdlane(rb[j & 1], (j >> 1) * 16);
#pragma unroll
      for (int e = 0; e < 4; ++e) { xn[j][e] = __builtin_elementwise_fma(xn[j][e], (f32x2){a, a}, (f32x2){c, c}); PIN(xn[j][e]); }
    }
  }
  asm volatile("s_waitcnt vmcnt(4)" : "+v"(cv1), "+v"(ml)::"memory");
  {
    const float sc = silu_bf(bflo(cv1));
    float v[8];
#pragma unroll
    for (int e = 0; e < 4; ++e) { v[2 * e] = sc * bflo(ml[e]); v[2 * e + 1] = sc * bfhi(ml[e]); }
#pragma unroll
    for (int j = 0; j < 4; ++j) { swap32f(v[j], v[j + 4]); v[j] += v[j + 4]; }
#pragma unroll
    for (int j = 0; j < 2; ++j) { swap16f(v[j], v[j + 2]); v[j] += v[j + 2]; }
    const bool o = lane & 8;
    const float a = (o ? v[1] : v[0]) + dppf<0x128>(o ? v[0] : v[1]);
    sts4f(SCR + 1024 + ((lane & 7) * 8 + (lane >> 3)) * 32 + w * 4, a);
  }
  QKV_DMA(0, 0); QKV_DMA(0, 1); QKV_DMA(0, 2); QKV_DMA(1, 0); QKV_DMA(1, 1); QKV_DMA(1, 2);
  {
    WAIT4X(6, a0, a1, a2, a3);
#pragma unroll 1
    for (u32 spins = 0; spins < SPIN_CAP; ++spins) {
      if (__all(tags_ok(a0, a1, tag) && tags_ok(a2, a3, tag))) break;
      LD4X(a0, a1, a2, a3, lv, WS_MODC + (u32)xcd * 32768 + w * 1024, 8192);
      WAIT4X(0, a0, a1, a2, a3);
    }
    if (w == 0) __builtin_amdgcn_s_setprio(0);
    *(LAS u32*)(uintptr_t)(SCR + 4096 + tid * 4) = pk2(gsum(a0, a1, a2, a3, 0), gsum(a0, a1, a2, a3, 2));
  }
  LBAR();
  {
    const u32x4 sa = lds16u(SCR + 4096 + lv), ca = lds16u(SCR + 5120 + lv);
    f32x2 fs[4], fa[4];
#pragma unroll
    for (int e = 0; e < 4; ++e) {
      const u32 t = pk2(1.f + bflo(ca[e]), 1.f + bfhi(ca[e]));
      fs[e] = (f32x2){bflo(t), bfhi(t)}; fa[e] = (f32x2){bflo(sa[e]), bfhi(sa[e])};
    }
#pragma unroll
    for (int j = 0; j < 8; ++j) {
      u32x4 h;
#pragma unroll
      for (int e = 0; e < 4; ++e) { const f32x2 t = __builtin_elementwise_fma(xn[j][e], fs[e], fa[e]); h[e] = pk2(t[0], t[1]); }
      sts16(aw[j], h);
    }
    if (w == 1) st_gran(ws, WS_MODL + (u32)(bi >> 2) * 16384 + (u32)((xcd * 4 + (bi & 3)) * 64 + lane) * 8,
                        sum8(lds16f(SCR + 1024 + lane * 32), lds16f(SCR + 1024 + lane * 32 + 16)), tag);
  }
  const rsrc_t rwg = __builtin_amdgcn_make_buffer_rsrc((void*)(wmat ? pwu : pwg), 0, bi < 28 ? 512 * 1792 * 2 : 0, 0x00020000);
#define WG_DMA(i) dma(65536 + w * 2048 + (i) * 1024, pg, rwg, (i) * (8 * 3584))
  u32x4 g0, g1, g2, g3, xres;
  {
    f32x4 acc[2][3] = {};
    bf16x8 fa[2][2][2], fb[2][2][3];
#define Q_RA(S, s, kk, m) fa[S][kk][m] = lds16(ab[(2 * (s) + (kk)) & 3] + ((2 * (s) + (kk)) >> 2) * 256 + (m) * 16384)
#define Q_RB(S, s, kk, sg) fb[S][kk][sg] = ldtr((((s) & 2) ? bh : bl) + ((s) & 1) * 24576 + (sg) * 8192 + (kk) * 4096)
#define Q_M(S, kk, m, sg) do { acc[m][sg] = (sg) < 2 ? MFMA16(fb[S][kk][sg], fa[S][kk][m], acc[m][sg]) : MFMA16(fa[S][kk][m], fb[S][kk][sg], acc[m][sg]); PIN(acc[m][sg]); } while (0)
#define Q_STAGE(C, N, s, RD, D0, D1, D2, D3) do { \
      if (RD) { Q_RA(N, s, 0, 0); Q_RA(N, s, 0, 1); } Q_M(C, 0, 0, 0); SBAR(); \
      if (RD) Q_RB(N, s, 0, 0); Q_M(C, 0, 0, 1); SBAR(); \
      if (RD) Q_RB(N, s, 0, 1); Q_M(C, 0, 0, 2); SBAR(); \
      if (RD) Q_RB(N, s, 0, 2); Q_M(C, 0, 1, 0); SBAR(); \
      if (RD) { Q_RA(N, s, 1, 0); Q_RA(N, s, 1, 1); } Q_M(C, 0, 1, 1); SBAR(); \
      if (RD) Q_RB(N, s, 1, 0); Q_M(C, 0, 1, 2); SBAR(); \
      if (RD) Q_RB(N, s, 1, 1); Q_M(C, 1, 0, 0); SBAR(); \
      if (RD) Q_RB(N, s, 1, 2); Q_M(C, 1, 0, 1); SBAR(); \
      D0; Q_M(C, 1, 0, 2); SBAR(); D1; Q_M(C, 1, 1, 0); SBAR(); D2; Q_M(C, 1, 1, 1); SBAR(); D3; Q_M(C, 1, 1, 2); SBAR(); } while (0)
    WAITBAR(0);
    QKV_DMA(2, 0); QKV_DMA(2, 1); QKV_DMA(2, 2);
#pragma unroll
    for (int kk = 0; kk < 2; ++kk) { Q_RA(0, 0, kk, 0); Q_RA(0, 0, kk, 1); Q_RB(0, 0, kk, 0); Q_RB(0, 0, kk, 1); Q_RB(0, 0, kk, 2); }
    LBAR();
    Q_STAGE(0, 1, 1, true, QKV_DMA(3, 0), QKV_DMA(3, 1), QKV_DMA(3, 2), QKV_DMA(4, 0));
    WAITBAR(4); Q_STAGE(1, 0, 2, true, QKV_DMA(4, 1), QKV_DMA(4, 2), QKV_DMA(5, 0), QKV_DMA(5, 1));
    WAITBAR(5); Q_STAGE(0, 1, 3, true, QKV_DMA(5, 2), QKV_DMA(6, 0), QKV_DMA(6, 1), QKV_DMA(6, 2));
    WAITBAR(6); Q_STAGE(1, 0, 4, true, QKV_DMA(7, 0), QKV_DMA(7, 1), QKV_DMA(7, 2), NOP_);
    WAITBAR(6); Q_STAGE(0, 1, 5, true, WG_DMA(0), WG_DMA(1), ld16(xres, xro, rx, 0), TWO(dma(81920 + w * 1024, lv, rwd, (u32)(b * 8 + w) * 1024), dma(81920 + w * 1024, lv, rwo, (u32)(b * 8 + w) * 1024)));
    WAITBAR(8); Q_STAGE(1, 0, 6, true, LD4X(g0, g1, g2, g3, gvo, WS_MODL, 16384), NOP_, NOP_, NOP_);
    WAITBAR(9); Q_STAGE(0, 1, 7, true, NOP_, NOP_, NOP_, NOP_);
    Q_STAGE(1, 0, 0, false, NOP_, NOP_, NOP_, NOP_);
    constexpr float QS = 0.25f * 1.4426950408889634f;
    {
      u32 p0 = pk2(acc[0][0][0] * QS, acc[0][0][1] * QS), p1 = pk2(acc[0][0][2] * QS, acc[0][0][3] * QS);
      u32 r0 = pk2(acc[1][0][0] * QS, acc[1][0][1] * QS), r1 = pk2(acc[1][0][2] * QS, acc[1][0][3] * QS);
      swap16u(p0, r0); swap16u(p1, r1);
      st16_sc1((u32x4){p0, p1, r0, r1}, rws, WS_Q + qdst);
    }
    {
      u32 p0 = pk2(acc[0][1][0], acc[0][1][1]), p1 = pk2(acc[0][1][2], acc[0][1][3]);
      u32 r0 = pk2(acc[1][1][0], acc[1][1][1]), r1 = pk2(acc[1][1][2], acc[1][1][3]);
      swap16u(p0, r0); swap16u(p1, r1);
      st16_sc1((u32x4){p0, p1, r0, r1}, rws, WS_K + qdst);
    }
    {
      u32 p0 = pk2(acc[0][2][0], acc[0][2][1]), p1 = pk2(acc[0][2][2], acc[0][2][3]);
      u32 r0 = pk2(acc[1][2][0], acc[1][2][1]), r1 = pk2(acc[1][2][2], acc[1][2][3]);
      swap32u(p0, r0); swap32u(p1, r1);
      st16_sc1((u32x4){p0, p1, r0, r1}, rws, WS_V + qdst);
    }
  }
  u32x4 gav, gbv, x1v, q;
  {
    pv = (u32)(lane >> 3) * 1024 + (((lane & 7) ^ (((lane >> 4) & 1) << 2)) * 16) + n64 * 128;
#pragma unroll
    for (int e = 0; e < 16; ++e) ms[e] = (e & 3) + 8 * (e >> 2) + 4 * (lane >> 5) > (lane & 31) ? 0xffffffffu : 0u;
#pragma unroll
    for (int e = 0; e < 8; ++e) mk[e] = (ms[2 * e] & 0xffffu) | (ms[2 * e + 1] & 0xffff0000u);
    kb = (u32)(w * 1024 + lane * 16);
    vb0 = 16384 + w * 1040 + ((lane & 31) < 16 ? (lane >> 5) * 256 + (lane & 31) * 16 : 1024);
    vb1 = 16384 + w * 1040 + ((lane & 31) < 16 ? 512 + (lane >> 5) * 256 + (lane & 31) * 16 : 1024);
    const int wcu = wg * 4 + g, wn = wcu / 7, wfr = wcu - wn * 7;
    wdst = WS_WP + (u32)wn * WPN + wfr * 1024 + (u32)(wmat * 16 + fr) * 16;
    ta = 65536 + w * 2048 + (u32)(fr >> 2) * 128 + ((g ^ ((fr >> 2) & 2)) * 32) + (lane & 3) * 8;
    odst = WS_O + (u32)((8 * range + w) * 32 + head2) * 1024 + lv;
    const int hd = 4 * grp + (lane & 3), rg = (m64a >> 2) + ((lane >> 2) & 1);
    postq = lane < 4 || (lane < 8 && rg < 8) ? mbox(hd >> 2, (rg << 2) | (hd & 3), MB_QKV + (m64a & 3) + (lane < 4 ? 4 : 0)) : lane == 8 ? WS_ARR + b * 4 : NOWHERE;
    posto = lane < 32 ? mbox(range, lane, MB_O + head2) : NOWHERE;
    const int wnp = (wg * 4 + (lane < 16 ? 0 : 3)) / 7;
    postw = lane < 32 ? mbox(((fr >> 3) << 2) | (wnp >> 2), ((wnp & 3) << 3) | (lane & 7), wmat * 12 + (wg - ((7 * wnp) >> 2)) * 4 + wq) : NOWHERE;
    pollq = mbox(xcd, bi, MB_QKV + (w >> 1) + ((lane & 1) || range == 0 ? 4 : 0));
    const int l24 = (lane & 31) < 24 ? (lane & 31) : 0, l12 = l24 < 12 ? l24 : l24 - 12;
    const int wslot = l12 < 4 * (((7 * nt + 6) >> 2) - ((7 * nt) >> 2) + 1) ? l24 : 0;
    pollw = mbox(xcd, bi, MB_W + wslot);
    pollx = mbox(xcd, bi, lane < 32 ? MB_X1 + ((w >> 1) & 1) * 8 + (lane & 7) : MB_E + wslot);
    PIN(pv); PIN(kb); PIN(vb0); PIN(vb1); PIN(wdst); PIN(ta); PIN(odst); PIN(postq); PIN(posto); PIN(postw); PIN(pollq); PIN(pollw); PIN(pollx);
#pragma unroll
    for (int j = 0; j < 8; ++j) PIN(mk[j]);
#pragma unroll
    for (int e = 0; e < 16; ++e) PIN(ms[e]);
  }
  {
    asm volatile("s_waitcnt vmcnt(0) lgkmcnt(0)\n\ts_barrier" : "+v"(g0), "+v"(g1), "+v"(g2), "+v"(g3), "+v"(xres)::"memory");
    if (w == 0) post(postq, rws, tag);
    if (tid < 16) { const u32 o1 = tid >= 8 || range > 0 ? 0x3f803f80u : 0u; sts16(16384 + tid * 1040 + 1024, (u32x4){o1, o1, o1, o1}); }
#pragma unroll 1
    for (u32 spins = 0; spins < SPIN_CAP; ++spins) {
      if (__all(tags_ok(g0, g1, tag) && tags_ok(g2, g3, tag))) break;
      __builtin_amdgcn_s_sleep(1);
      LD4X(g0, g1, g2, g3, gvo, WS_MODL, 16384);
      WAIT4X(0, g0, g1, g2, g3);
    }
    const u32 gs = 49152 + w * 128;
    {
      float t0 = gsum(g0, g1, g2, g3, 0), t1 = gsum(g0, g1, g2, g3, 2);
      t0 += dppf<0x128>(t0); t1 += dppf<0x128>(t1);
      u32 t = pk2(t0, t1);
      if ((lane >> 4) == 1) t = pk2(1.f + bflo(t), 1.f + bfhi(t));
      if ((lane & 8) == 0) *(LAS u32*)(uintptr_t)(gs + (lane >> 4) * 32 + (lane & 7) * 4) = t;
      asm volatile("s_waitcnt lgkmcnt(0)" ::: "memory");
    }
    gav = lds16u(gs + 64 + (lane >> 5) * 16); gbv = lds16u(gs + 96 + (lane >> 5) * 16);
    if (bi < 28) {
      float u = 0.f, bs = 0.f;
#pragma unroll
      for (int h = 0; h < 2; ++h) {
        const u32x4 wv = __builtin_bit_cast(u32x4, ldtr(ta + h * 1024)), sb = lds16u(gs + h * 16), cb = lds16u(gs + 32 + h * 16);
        u32x4 e;
#pragma unroll
        for (int j = 0; j < 4; ++j) {
          e[j] = pk2(bflo(wv[j]) * bflo(cb[j]), bfhi(wv[j]) * bfhi(cb[j]));
          u += bflo(e[j]) + bfhi(e[j]);
          bs = __builtin_fmaf(bflo(wv[j]), bflo(sb[j]), __builtin_fmaf(bfhi(wv[j]), bfhi(sb[j]), bs));
        }
        st16_sc1(e, rws, wdst + h * 512 + (u32)(wq * 8 + w) * 7168);
      }
      sts4f(SC2 + lane * 32 + w * 4, u); sts4f(SC2 + 2048 + lane * 32 + w * 4, bs);
    }
    poll(pollq, rws, tag);
    const u32 src = (u32)(head2 * 64 + 8 * range + w) * 1024;
    const rsrc_t rh = __builtin_amdgcn_make_buffer_rsrc(ws, 0, range > 0 ? WS_BYTES : 0, 0x00020000);
    dma_sc1((w + 8) * 1024, lv, rws, WS_K + src);
    ld16_sc1(q, lv, rws, WS_Q + src);
    dma_sc1(w * 1024, lv, rh, WS_K + src - 8192);
    dma_sc1(16384 + (w + 8) * 1040, lv, rws, WS_V + src);
    dma_sc1(16384 + w * 1040, lv, rh, WS_V + src - 8192);
  }
  {
    asm volatile("s_waitcnt vmcnt(0) lgkmcnt(0)\n\ts_barrier" : "+v"(q)::"memory");
    if (bi < 28 && w == 7) {
      post(postw + MB_W * 4, rws, tag);
      const float t = sum8(lds16f(SC2 + lane * 32), lds16f(SC2 + lane * 32 + 16)), o = sum8(lds16f(SC2 + 2048 + lane * 32), lds16f(SC2 + 2048 + lane * 32 + 16));
      const float uh = rbf(t), ul = t - uh, bh = rbf(o), bl = o - bh;
      st16_sc1((u32x4){pk2(uh, ul), pk2(uh, bh), pk2(bl, bh), 0u}, rws, wdst + (wq & 1) * 512 + (u32)(32 + (wq >> 1)) * 7168);
    }
    const u32x4 ov = attn_wave(range > 0, w, lane, BF8(q), mk, ms, kb, vb0, vb1, pv, rwo, (u32)n64 | (u32)(bi & 3) << 9);
    st16_sc1(ov, rws, odst);
    u32 fw;
    ld4_sc1(fw, pollw, rws, 0);
    {
#pragma unroll
      for (int c = 0; c < 2; ++c) tb[c] = (u32)(8 * (lane >> 5) + (fr >> 2)) * 128 + ((((2 * c + (g & 1) + ksl) & 3) ^ ((fr >> 2) & 2)) * 32) + (lane & 3) * 8;
      xdst3 = WS_X1 + (u32)(tile3 * 32 + n64 * 4 + ksl) * 1024 + lv;
      adst = WS_ACT + (u32)((4 * m128 + hn) * 112 + nt * 7 + (w & 4)) * 1024 + lv;
      postx = lane < 16 ? mbox(((m64 >> 4) << 2) | (lane >> 2), ((lane & 3) << 3) | ((m64 >> 1) & 7), MB_X1 + (m64 & 1) * 8 + n64) : NOWHERE;
      posta = lane < 16 ? mbox(m128 >> 1, ((lane >> 1) << 2) | ((2 * m128 + (lane & 1)) & 3), MB_ACT + nt) : NOWHERE;
      pollo = mbox(xcd, bi, MB_O + ksl * 8 + (lane & 7));
      polla = mbox(xcd, bi, MB_ACT + ksl * 4 + (lane & 3));
#pragma unroll
      for (int j = 1; j < 4; ++j) rbw[j - 1] = (mh ? 36864 : 65536) + (u32)(((ksl + j) & 3) * 3 + j - 1) * 2048 + lv;
      rbr = (mh ? 36864 : 65536) + (u32)ksl * 6144 + lv;
      wl = lv + (w & 4) * 1024; xl = lv + 114688 + hn * 4096;
      stv = (u32)(lane >> 5) * 2048 + (lane & 31) * 16;
      PIN(tb[0]); PIN(tb[1]); PIN(xdst3); PIN(adst); PIN(postx); PIN(posta); PIN(pollo); PIN(polla); PIN(rbw[0]); PIN(rbw[1]); PIN(rbw[2]); PIN(rbr); PIN(wl); PIN(xl); PIN(stv);
    }
    asm volatile("s_waitcnt vmcnt(0) lgkmcnt(0)\n\ts_barrier" : "+v"(fw)::"memory");
    if (w == 0) post(posto, rws, tag);
    if (bi < 28 && w == 7) post(postw + MB_E * 4, rws, tag);
    if (!__all(fw == tag)) poll(pollw, rws, tag);
#pragma unroll
    for (int i = 0; i < 4; ++i) if (i < 3 || w < 4) dma_sc1(w * 1024 + i * 8192, lv, rws, WS_WP + (u32)nt * WPN + ((2 * nt + m128) & 7) * 28672 + w * 1024 + i * 8192);
    bf16x8 wf[8][2];
#pragma unroll
    for (int ks = 0; ks < 8; ++ks)
#pragma unroll
      for (int c = 0; c < 2; ++c) wf[ks][c] = ldtr(98304 + ksl * 16384 + tb[c] + ks * 2048);
    if (b == 255 && w == 1) {
#pragma unroll 1
      for (int i = 0; i < 4; ++i) poll(WS_ARR + i * 256 + lane * 4, rws, tag);
      if (lane == 0) __hip_atomic_store((GAS u32*)(ws + WS_SEED), tag, RLX_AGENT);
    }
    poll(pollo, rws, tag);
    u32x4 of[8];
#pragma unroll
    for (int ks = 0; ks < 8; ++ks) ld16_sc1(of[ks], lv, rws, WS_O + (u32)(tile3 * 32 + ksl * 8 + (ks ^ n64)) * 1024);
    f32x16 acc[2] = {};
#pragma unroll
    for (int ks = 0; ks < 8; ++ks) {
      WAITV1(7 - ks, of[ks]);
      acc[0] = MFMA32(wf[ks][0], BF8(of[ks]), acc[0]);
      acc[1] = MFMA32(wf[ks][1], BF8(of[ks]), acc[1]);
      PIN(acc[0]); PIN(acc[1]);
    }
#pragma unroll
    for (int j = 1; j < 4; ++j) {
      const u32 a = rbw[j - 1];
      const int o = (j & 1) * 8;
      sts16f(a, (f32x4){acc[j >> 1][o], acc[j >> 1][o + 1], acc[j >> 1][o + 2], acc[j >> 1][o + 3]});
      sts16f(a + 1024, (f32x4){acc[j >> 1][o + 4], acc[j >> 1][o + 5], acc[j >> 1][o + 6], acc[j >> 1][o + 7]});
    }
    LBAR();
    float y[8];
#pragma unroll
    for (int e = 0; e < 8; ++e) y[e] = acc[0][e];
#pragma unroll
    for (int j = 0; j < 3; ++j) {
      const f32x4 a = lds16f(rbr + j * 2048), d = lds16f(rbr + j * 2048 + 1024);
#pragma unroll
      for (int e = 0; e < 4; ++e) { y[e] += a[e]; y[4 + e] += d[e]; }
    }
    x1v = resid_epi(y, xres, gav);
    st16_sc1(x1v, rws, xdst3);
    {
      float s = 0.f, q2 = 0.f;
#pragma unroll
      for (int e = 0; e < 4; ++e) { const float a = bflo(x1v[e]), c = bfhi(x1v[e]); s += a + c; q2 = __builtin_fmaf(a, a, __builtin_fmaf(c, c, q2)); }
      swap32f(s, q2);
      sts4f(SC3 + (u32)(mh * 1024 + ksl * 256 + (lane & 31) * 8 + (lane >> 5) * 4), s + q2);
    }
    WAITBAR(0);
    if (w == 2) post(postx, rws, tag);
    if (ksl == 0) {
      const u32 a = SC3 + (u32)(mh * 1024 + (lane & 31) * 8 + (lane >> 5) * 4);
      const float t = (lds4f(a) + lds4f(a + 256)) + (lds4f(a + 512) + lds4f(a + 768));
      st_gran(ws, WS_ST + (u32)(tile3 * 8 + n64) * 512 + (lane & 31) * 16 + (lane >> 5) * 8, t, tag);
      asm volatile("s_waitcnt lgkmcnt(0)" ::: "memory");
    }
  }
  const u32 r5 = (u32)(((n64 * 7) >> 1) + 7 * (bi & 3)) % 28;
  u32 wh = wl + 57344;
  PIN(wh);
  if (w < 4) ffn_up<4>(w, lane, tag, ws, rws, rwd, pv, m128, nt, r5, pollx, wl, wh, xl, stv, adst, b);
  else ffn_up<3>(w, lane, tag, ws, rws, rwd, pv, m128, nt, r5, pollx, wl, wh, xl, stv, adst, b);
  {
    u32 t0 = tb[0] + ksl * 40960, t1 = tb[1] + ksl * 40960;
    PIN(t0); PIN(t1);
    WAITBAR(0);
    if (w == 0) post(posta, rws, tag);
    bf16x8 wp[8][2];
#pragma unroll
    for (int s = 0; s < 8; ++s) { wp[s][0] = ldtr(t0 + s * 2048); wp[s][1] = ldtr(t1 + s * 2048); }
    LBAR();
#define DN_RF(i) dma((u32)((w >> 1) * 40 + (w & 1) * 8 + (i)) * 1024, pv, rwd, (u32)((w >> 1) * 448 + ((i) & 1) * 8 + rot28(20 + (w & 1) * 4 + ((i) >> 1), r5) * 16) * 1024)
    DN_RF(0); DN_RF(1); DN_RF(2); DN_RF(3);
    DN_RF(4); DN_RF(5); DN_RF(6); DN_RF(7);
    poll(polla, rws, tag);
    u32x4 af[28];
    const u32 asrc = WS_ACT + (u32)(tile3 * 112 + ksl * 28) * 1024;
#pragma unroll
    for (int ks = 0; ks < DEP; ++ks) ld16_sc1(af[ks], lv, rws, asrc + rot28(ks, r5) * 1024);
    f32x16 acc[2] = {};
    bf16x8 wa0, wa1, wb0, wb1;
#define DN_PG(s) ((s) < 20 ? (s) * 2048 : ((s) - 20) * 2048)
#define DN_RD(A, B, s) do { A = ldtr(t0 + DN_PG(s)); B = ldtr(t1 + DN_PG(s)); } while (0)
#define DN_MM(A, B, ks) do { if ((ks) + DEP < 28) ld16_sc1(af[(ks) + DEP < 28 ? (ks) + DEP : 0], lv, rws, asrc + rot28((ks) + DEP, r5) * 1024); \
      WAITV1((ks) + DEP < 28 ? DEP : 27 - (ks), af[ks]); \
      acc[0] = MFMA32(A, BF8(af[ks]), acc[0]); PIN(acc[0]); acc[1] = MFMA32(B, BF8(af[ks]), acc[1]); PIN(acc[1]); SBAR(); } while (0)
#pragma unroll
    for (int ks = 0; ks < 7; ++ks) DN_MM(wp[ks][0], wp[ks][1], ks);
    DN_RD(wa0, wa1, 8); DN_MM(wp[7][0], wp[7][1], 7);
#pragma unroll
    for (int ks = 8; ks < 28; ks += 2) {
      DN_RD(wb0, wb1, ks + 1); DN_MM(wa0, wa1, ks);
      if (ks == 18) LBAR();
      if (ks + 2 < 28) DN_RD(wa0, wa1, ks + 2);
      DN_MM(wb0, wb1, ks + 1);
    }
    const u32 sb = (u32)ksl * 40960 + 16384 + mh * 6144 + lv;
#pragma unroll
    for (int j = 1; j < 4; ++j) {
      const int o = (j & 1) * 8;
      sts16f(sb + (j - 1) * 2048, (f32x4){acc[j >> 1][o], acc[j >> 1][o + 1], acc[j >> 1][o + 2], acc[j >> 1][o + 3]});
      sts16f(sb + (j - 1) * 2048 + 1024, (f32x4){acc[j >> 1][o + 4], acc[j >> 1][o + 5], acc[j >> 1][o + 6], acc[j >> 1][o + 7]});
    }
    LBAR();
    float y[8];
#pragma unroll
    for (int e = 0; e < 8; ++e) y[e] = acc[0][e];
#pragma unroll
    for (int j = 1; j < 4; ++j) {
      const u32 a = (u32)((ksl - j) & 3) * 40960 + 16384 + mh * 6144 + (j - 1) * 2048 + lv;
      const f32x4 c = lds16f(a), d = lds16f(a + 1024);
#pragma unroll
      for (int e = 0; e < 4; ++e) { y[e] += c[e]; y[4 + e] += d[e]; }
    }
    const u32x4 ov = resid_epi(y, x1v, gbv);
    __builtin_amdgcn_raw_buffer_store_b128(ov, rout, xro, 0, 0);
  }
}

extern "C" void kernel_launch(void* const* d_in, const int* in_sizes, int n_in, void* d_out, int out_size, void* d_ws, size_t ws_size,
                              hipStream_t stream) {
  fused_block<<<256, 512, 0, stream>>>(d_in[0], d_in[1], d_in[2], d_in[4], d_in[5], d_in[6], d_in[7], d_in[8], d_out, (char*)d_ws);
}
```
